```python
import torch

def _softmax(x, axis=-1):
    return torch.softmax(x, dim=axis)
def _gelu(x):
    return torch.nn.functional.gelu(x, approximate="tanh")
def _silu(x):
    return torch.nn.functional.silu(x)
def _sigmoid(x):
    return torch.sigmoid(x)
def _layernorm(x, axis=-1, eps=1e-5):
    m = x.mean(dim=axis, keepdim=True)
    v = x.var(dim=axis, keepdim=True, unbiased=False)
    return (x - m) / torch.sqrt(v + eps)
def _scatter_add(tgt, idx, src, axis=0):
    return tgt.clone().index_add_(axis, idx, src)
def _segment_sum(src, seg, n):
    out = torch.zeros((n,) + src.shape[1:], dtype=src.dtype)
    return out.index_add_(0, seg, src)
def _pad(x, pw):
    flat = tuple(v for pair in reversed(pw) for v in pair)
    return torch.nn.functional.pad(x, flat)

kernel_name = 'gated_mix__mha_block__geglu_ffn__prompt'

def setup_inputs():

    _w = (lambda a: (lambda t: t.to(torch.bfloat16) if t.is_floating_point() else t)(torch.as_tensor(a)))
    def _A_setup():
        B, S, D, H = 1, 2048, 640, 32
        return {
            "x": _w((lambda *s: torch.randn(*s, dtype=torch.bfloat16) * 0.05)(B, S, D)),
            "wqkv": _w((lambda *s: torch.randn(*s, dtype=torch.bfloat16) * 0.8)(D, 3 * D)),
            "wo": _w((lambda *s: torch.randn(*s, dtype=torch.bfloat16) * 0.05)(D, D)),
        }
    _A_in = _A_setup()

    def _B_setup():
        B, S, D, F = 1, 2048, 640, 1536
        return {
            "x": _w((lambda *s: torch.randn(*s, dtype=torch.bfloat16) * 0.05)(B, S, D)),
            "w_gate": _w((lambda *s: torch.randn(*s, dtype=torch.bfloat16) * 0.1)(D, F)),
            "w_up": _w((lambda *s: torch.randn(*s, dtype=torch.bfloat16) * 0.1)(D, F)),
            "w_down": _w((lambda *s: torch.randn(*s, dtype=torch.bfloat16) * 0.1)(F, D)),
        }
    _B_in = _B_setup()

    return {
        "A_x": _A_in["x"],
        "A_wqkv": _A_in["wqkv"],
        "A_wo": _A_in["wo"],
        "B_x": _B_in["x"],
        "B_w_gate": _B_in["w_gate"],
        "B_w_up": _B_in["w_up"],
        "B_w_down": _B_in["w_down"],
        "gate_w": _w((lambda *s: torch.randn(*s, dtype=torch.bfloat16) * 0.05)(640) * 16.0),
    }

def reference(A_x, A_wqkv, A_wo, B_x, B_w_gate, B_w_up, B_w_down, gate_w):
    def _A_ref(x, wqkv, wo):
        B, S, D = x.shape
        H = 32
        Dh = D // H
        qkv = (lambda x, *d: x.permute(*d))((x @ wqkv).reshape(B, S, 3, H, Dh), 2, 0, 3, 1, 4)
        q, k, v = qkv[0], qkv[1], qkv[2]
        scores = torch.einsum("bhqd,bhkd->bhqk", q, k) * (Dh ** -0.5)
        p = _softmax(scores, axis=-1)
        o = torch.einsum("bhqk,bhkd->bhqd", p, v)
        return (lambda x, *d: x.permute(*d))(o, 0, 2, 1, 3).reshape(B, S, D) @ wo

    def _B_ref(x, w_gate, w_up, w_down):
        return (_gelu(x @ w_gate) * (x @ w_up)) @ w_down

    x = A_x
    g = _sigmoid((x * gate_w).sum(-1, keepdims=True))
    a = _A_ref(x, A_wqkv, A_wo)
    b = _B_ref(x, B_w_gate, B_w_up, B_w_down)
    return g * a + (1 - g) * b
```

```cpp
#include <hip/hip_runtime.h>
#include <hip/hip_bf16.h>
#include <stdint.h>

typedef __attribute__((ext_vector_type(8))) __bf16 bf16x8;
typedef __attribute__((ext_vector_type(8))) short s16x8;
typedef __attribute__((ext_vector_type(4))) short s16x4;
typedef __attribute__((ext_vector_type(2))) __bf16 bf16x2;
typedef __attribute__((ext_vector_type(2))) float f32x2;
typedef __attribute__((ext_vector_type(16))) float f32x16;
typedef __attribute__((ext_vector_type(4))) unsigned u32x4;
typedef __attribute__((ext_vector_type(2))) unsigned u32x2;
typedef __attribute__((ext_vector_type(4))) int i32x4;
typedef __attribute__((address_space(3))) const char* lds_cptr;
typedef __attribute__((address_space(3))) char* lds_ptr;

#define DEV __device__ __forceinline__

constexpr int T_ = 2048, D_ = 640, NH = 32, DH = 20, F_ = 1536;
constexpr float QSCALE = 0.22360679774997896f * 1.4426950408889634f;

constexpr size_t WS_QF = 0;
constexpr size_t WS_KV = WS_QF + (size_t)NH * 64 * 1280;
constexpr size_t WS_HF = WS_KV + (size_t)NH * 64 * 3072;
constexpr size_t WS_OF = WS_HF + (size_t)64 * 96 * 1024;
constexpr size_t WS_G  = WS_OF + (size_t)64 * 40 * 1024;
constexpr size_t WS_END = WS_G + 2048 * 4;

DEV i32x4 make_srd(const void* p) {
    i32x4 s;
    s[0] = __builtin_amdgcn_readfirstlane((int)(uint32_t)(uintptr_t)p);
    s[1] = __builtin_amdgcn_readfirstlane((int)(uint32_t)((uintptr_t)p >> 32)) & 0xffff;
    s[2] = 0x7fffffff;
    s[3] = 0x00020000;
    return s;
}
DEV void dma16(const i32x4& srd, unsigned voff, unsigned soff, unsigned ldsaddr) {
    asm volatile("s_mov_b32 m0, %3\n\ts_nop 0\n\tbuffer_load_dwordx4 %0, %1, %2 offen lds"
                 :: "v"(voff), "s"(srd), "s"(soff), "s"(ldsaddr) : "memory");
}
#define WAIT_VM(n) asm volatile("s_waitcnt vmcnt(" #n ")" ::: "memory")
#define WAIT_VM_BAR(n) asm volatile("s_waitcnt vmcnt(" #n ")\n\ts_barrier" ::: "memory")
#define SBAR() __builtin_amdgcn_sched_barrier(0)

DEV f32x16 mfma16(bf16x8 a, bf16x8 b, f32x16 c) { return __builtin_amdgcn_mfma_f32_32x32x16_bf16(a, b, c, 0, 0, 0); }
DEV f32x16 mfma8(s16x4 a, s16x4 b, f32x16 c) { return __builtin_amdgcn_mfma_f32_32x32x8bf16_1k(a, b, c, 0, 0, 0); }
DEV unsigned pk2(float lo, float hi) {
    f32x2 v = {lo, hi};
    bf16x2 r = __builtin_convertvector(v, bf16x2);
    return __builtin_bit_cast(unsigned, r);
}
DEV bf16x8 lds_ld128(lds_cptr p) { return *(const __attribute__((address_space(3))) bf16x8*)p; }
DEV s16x4 lds_ld64(lds_cptr p) { return *(const __attribute__((address_space(3))) s16x4*)p; }
DEV s16x4 lds_tr(lds_cptr p) { return __builtin_amdgcn_ds_read_tr16_b64_v4i16((__attribute__((address_space(3))) s16x4*)p); }
DEV bf16x8 cat8(s16x4 lo, s16x4 hi) {
    s16x8 r = __builtin_shufflevector(lo, hi, 0, 1, 2, 3, 4, 5, 6, 7);
    return __builtin_bit_cast(bf16x8, r);
}
DEV bf16x8 pack8(const f32x16& v, int base) {
    u32x4 r = {pk2(v[base], v[base + 1]), pk2(v[base + 2], v[base + 3]), pk2(v[base + 4], v[base + 5]), pk2(v[base + 6], v[base + 7])};
    return __builtin_bit_cast(bf16x8, r);
}

constexpr int K1_STAGE = 32768, K1_LDS = 4 * K1_STAGE;

template <bool VMODE>
DEV void k1_gemm(const char* x, const char* wqkv, char* ws, int mt, int nt, lds_ptr lds) {
    const int tid = threadIdx.x, lane = tid & 63, w = __builtin_amdgcn_readfirstlane(tid >> 6);
    const int wt = w >> 1, ww = w & 1, r = lane & 31, h = lane >> 5;
    const int m0 = mt * 128, n0 = nt * 128;
    const i32x4 sx = make_srd(x), sw = make_srd(wqkv);
    const unsigned l0 = (unsigned)(uintptr_t)lds;
    const unsigned xq = lane >> 4;
    const unsigned vx0 = (unsigned)(lane >> 3) * 1280u + (unsigned)(((lane & 7) ^ xq) * 16);
    const unsigned vx1 = (unsigned)(lane >> 3) * 1280u + (unsigned)(((lane & 7) ^ xq ^ 4) * 16);
    const unsigned vw = (unsigned)(lane >> 4) * 3840u + (unsigned)(((lane & 15) ^ ((lane >> 4) << 2)) * 16);
    const unsigned sxb = (unsigned)(m0 + 32 * w) * 1280u, swb = (unsigned)(16 * w) * 3840u + (unsigned)n0 * 2u;
    auto issue = [&](int t) {
        const unsigned lb = l0 + (unsigned)(t & 3) * K1_STAGE;
#pragma unroll
        for (int j = 0; j < 4; ++j) dma16(sx, (j & 1) ? vx1 : vx0, sxb + (unsigned)j * 10240u + (unsigned)t * 128u, lb + (unsigned)(4 * w + j) * 1024u);
#pragma unroll
        for (int j = 0; j < 4; ++j) dma16(sw, vw, swb + (unsigned)(t * 64 + 4 * j) * 3840u, lb + 16384u + (unsigned)(4 * w + j) * 1024u);
    };
    issue(0); issue(1); issue(2);
    lds_cptr xa[4], wa[2];
#pragma unroll
    for (int s = 0; s < 4; ++s) xa[s] = (lds_cptr)lds + (wt * 64 + r) * 128 + (((2 * s + h) ^ ((r >> 1) & 7)) * 16);
    {
        const int g = lane >> 4, nh = g & 1, q = (lane & 15) >> 2, p = lane & 3;
#pragma unroll
        for (int ct = 0; ct < 2; ++ct) {
            const int slot = ww * 8 + ct * 4 + 2 * nh + (p >> 1);
            wa[ct] = (lds_cptr)lds + 16384 + (8 * h + q) * 256 + ((slot ^ (q << 2)) * 16) + 8 * (p & 1);
        }
    }
    f32x16 acc[2][2];
#pragma unroll
    for (int i = 0; i < 2; ++i)
#pragma unroll
        for (int j = 0; j < 2; ++j) acc[i][j] = f32x16{};
    bf16x8 xf[2][2], wf[2][2];
    auto ldfrag = [&](int buf, int t, int s) {
        const int so = (t & 3) * K1_STAGE;
#pragma unroll
        for (int tt = 0; tt < 2; ++tt) xf[buf][tt] = lds_ld128(xa[s] + so + tt * 4096);
#pragma unroll
        for (int ct = 0; ct < 2; ++ct) wf[buf][ct] = cat8(lds_tr(wa[ct] + so + s * 4096), lds_tr(wa[ct] + so + s * 4096 + 1024));
    };
    WAIT_VM_BAR(16);
    ldfrag(0, 0, 0);
#pragma unroll
    for (int t = 0; t < 10; ++t) {
        if (t < 8) WAIT_VM_BAR(8); else if (t == 8) WAIT_VM_BAR(0);
        if (t + 3 < 10) issue(t + 3);
#pragma unroll
        for (int s = 0; s < 4; ++s) {
            const int cur = s & 1;
            if (s < 3) ldfrag(cur ^ 1, t, s + 1); else if (t < 9) ldfrag(cur ^ 1, t + 1, 0);
#pragma unroll
            for (int i = 0; i < 2; ++i)
#pragma unroll
                for (int j = 0; j < 2; ++j)
                    acc[i][j] = VMODE ? mfma16(xf[cur][i], wf[cur][j], acc[i][j]) : mfma16(wf[cur][i], xf[cur][j], acc[i][j]);
        }
    }
    if constexpr (!VMODE) {
        const bool isq = nt < 5;
        const int csec = (isq ? n0 : n0 - 640) + ww * 64;
        char* base = ws + (isq ? WS_QF : WS_KV);
        const int tstride = isq ? 1280 : 3072;
        const float sc = isq ? QSCALE : 1.0f;
#pragma unroll
        for (int ct = 0; ct < 2; ++ct)
#pragma unroll
            for (int g = 0; g < 4; ++g) {
                const int qd = (csec + ct * 32 + 8 * g + 4 * h) >> 2, head = qd / 5, dq = qd - head * 5;
                const int inoff = dq < 4 ? (dq >> 1) * 512 + r * 16 + (dq & 1) * 8 : 1024 + r * 8;
#pragma unroll
                for (int tt = 0; tt < 2; ++tt) {
                    const int tile = mt * 4 + wt * 2 + tt;
                    char* p = base + (size_t)(head * 64 + tile) * tstride + inoff;
                    const f32x16& a = acc[ct][tt];
                    u32x2 v = {pk2(a[4 * g] * sc, a[4 * g + 1] * sc), pk2(a[4 * g + 2] * sc, a[4 * g + 3] * sc)};
                    *(u32x2*)p = v;
                    if (!isq && dq == 4) *(u32x2*)(p + 256) = (u32x2){0x00003f80u, 0u};
                }
            }
    } else {
#pragma unroll
        for (int ct = 0; ct < 2; ++ct) {
            const int col = n0 - 1280 + ww * 64 + ct * 32 + r, head = col / 20, d = col - head * 20;
#pragma unroll
            for (int tt = 0; tt < 2; ++tt) {
                const int tile = mt * 4 + wt * 2 + tt;
                char* p = ws + WS_KV + (size_t)(head * 64 + tile) * 3072 + 1536 + h * 384 + d * 16;
#pragma unroll
                for (int s = 0; s < 2; ++s) {
                    *(bf16x8*)(p + s * 768) = pack8(acc[tt][ct], 8 * s);
                    if (d == 0) *(u32x4*)(p + s * 768 + 320) = (u32x4){0x3f803f80u, 0x3f803f80u, 0x3f803f80u, 0x3f803f80u};
                }
            }
        }
    }
}

DEV void k1_gate(const char* x, const char* gw, char* ws, int mt) {
    const int tid = threadIdx.x, lane = tid & 63, w = tid >> 6, r = lane & 31, h = lane >> 5;
    const char* xr = x + (size_t)(mt * 128 + w * 32 + r) * 1280 + h * 16;
    f32x16 acc = f32x16{};
#pragma unroll 10
    for (int s = 0; s < 40; ++s) {
        const bf16x8 a = *(const bf16x8*)(xr + s * 32);
        const bf16x8 b = *(const bf16x8*)(gw + s * 32 + h * 16);
        acc = mfma16(a, b, acc);
    }
    if (r == 0) {
        float* g = (float*)(ws + WS_G) + mt * 128 + w * 32;
#pragma unroll
        for (int i = 0; i < 16; ++i) g[(i & 3) + 8 * (i >> 2) + 4 * h] = 1.0f / (1.0f + __expf(-acc[i]));
    }
}

__global__ __launch_bounds__(256, 1) void k1_kernel(const char* __restrict__ x, const char* __restrict__ wqkv, const char* __restrict__ gw, char* __restrict__ ws) {
    extern __shared__ __attribute__((aligned(16))) char lds_raw[];
    lds_ptr lds = (lds_ptr)lds_raw;
    const int b = blockIdx.x, xcd = b & 7, j = b >> 3, rg = xcd & 3, cg = xcd >> 2;
    if (cg == 1 && j >= 28) { k1_gate(x, gw, ws, rg * 4 + (j - 28)); return; }
    const int mt = rg * 4 + (j & 3), nt = cg * 8 + (j >> 2);
    if (nt < 10) k1_gemm<false>(x, wqkv, ws, mt, nt, lds); else k1_gemm<true>(x, wqkv, ws, mt, nt, lds);
}

constexpr int AT_CHUNK = 24576;

DEV void attn_slow(const char* kvh, bf16x8 q1, s16x4 q2, int lane, f32x16& o) {
    const int r = lane & 31, h = lane >> 5;
    const int vrow = r < 21 ? r : (r < 28 ? 20 : 16);
    float m = -INFINITY;
    o = f32x16{};
    for (int t = 0; t < 64; ++t) {
        const char* p = kvh + t * 3072;
        const bf16x8 k1 = *(const bf16x8*)(p + lane * 16);
        const s16x4 k2 = *(const s16x4*)(p + 1024 + lane * 8);
        const bf16x8 v0 = *(const bf16x8*)(p + 1536 + h * 384 + vrow * 16);
        const bf16x8 v1 = *(const bf16x8*)(p + 2304 + h * 384 + vrow * 16);
        f32x16 s = mfma16(k1, q1, f32x16{});
        s = mfma8(k2, q2, s);
        float tm = s[0];
#pragma unroll
        for (int i = 1; i < 16; ++i) tm = fmaxf(tm, s[i]);
        tm = fmaxf(tm, __shfl_xor(tm, 32));
        const float mn = fmaxf(m, tm);
        const float alpha = __builtin_amdgcn_exp2f(m - mn);
        m = mn;
#pragma unroll
        for (int i = 0; i < 16; ++i) { s[i] = __builtin_amdgcn_exp2f(s[i] - mn); o[i] *= alpha; }
        o = mfma16(v0, pack8(s, 0), o);
        o = mfma16(v1, pack8(s, 8), o);
    }
}

DEV void attn_role(char* ws, int item, lds_ptr lds) {
    const int tid = threadIdx.x, lane = tid & 63, w = __builtin_amdgcn_readfirstlane(tid >> 6);
    const int r = lane & 31, h = lane >> 5;
    const int head = (item & 7) * 4 + (item >> 6), qb = (item >> 3) & 7;
    const char* kvh = ws + WS_KV + (size_t)head * 64 * 3072;
    const i32x4 skv = make_srd(kvh);
    const unsigned l0 = (unsigned)(uintptr_t)lds;
    const unsigned vo = lane * 16;
    auto issue = [&](int c, int slot) {
#pragma unroll
        for (int j = 0; j < 6; ++j) dma16(skv, vo, (unsigned)(c * AT_CHUNK + (6 * w + j) * 1024), l0 + (unsigned)(slot * AT_CHUNK + (6 * w + j) * 1024));
    };
    issue(0, 0); issue(1, 1);
    bf16x8 q1[2]; s16x4 q2[2];
#pragma unroll
    for (int a = 0; a < 2; ++a) {
        const char* qp = ws + WS_QF + (size_t)(head * 64 + qb * 8 + 2 * w + a) * 1280;
        q1[a] = *(const bf16x8*)(qp + lane * 16);
        s16x4 t = *(const s16x4*)(qp + 1024 + r * 8);
        q2[a] = h ? (s16x4){0, 0, 0, 0} : t;
    }
    const int vrow = r < 21 ? r : (r < 28 ? 20 : 16);
    lds_cptr ka = (lds_cptr)lds + lane * 16, kb = (lds_cptr)lds + 1024 + lane * 8, va = (lds_cptr)lds + 1536 + h * 384 + vrow * 16;
    WAIT_VM_BAR(6);
    {
        const bf16x8 k1 = lds_ld128(ka); const s16x4 k2 = lds_ld64(kb);
#pragma unroll
        for (int a = 0; a < 2; ++a) {
            f32x16 s = mfma16(k1, q1[a], f32x16{});
            s = mfma8(k2, q2[a], s);
            float tm = s[0];
#pragma unroll
            for (int i = 1; i < 16; ++i) tm = fmaxf(tm, s[i]);
            tm = fmaxf(tm, __shfl_xor(tm, 32));
            const unsigned nb = pk2(-tm, 0.f) & 0xffffu;
            if (h) q2[a][0] = (short)nb;
        }
    }
    f32x16 o[2] = {f32x16{}, f32x16{}};
    f32x16 sA[2], sB[2];
    bf16x8 k1A, k1B, v0A, v1A, v0B, v1B; s16x4 k2A, k2B;
    {
        const bf16x8 k1 = lds_ld128(ka); const s16x4 k2 = lds_ld64(kb);
#pragma unroll
        for (int a = 0; a < 2; ++a) { sA[a] = mfma16(k1, q1[a], f32x16{}); sA[a] = mfma8(k2, q2[a], sA[a]); }
        k1A = lds_ld128(ka + 3072); k2A = lds_ld64(kb + 3072);
        v0A = lds_ld128(va); v1A = lds_ld128(va + 768);
    }
#define ATT_STEP(sc, sn, k1c, k2c, v0c, v1c, k1n, k2n, v0n, v1n, KOFF, VOFF, KP, KP2, VP)                     \
    do {                                                                                                   \
        k1n = lds_ld128(KP + (KOFF)); k2n = lds_ld64(KP2 + (KOFF));                                          \
        v0n = lds_ld128(VP + (VOFF)); v1n = lds_ld128(VP + (VOFF) + 768);                                    \
        sn[0] = mfma16(k1c, q1[0], f32x16{});                                                              \
        _Pragma("unroll") for (int i = 0; i < 8; ++i) sc[0][i] = __builtin_amdgcn_exp2f(sc[0][i]);          \
        sn[0] = mfma8(k2c, q2[0], sn[0]);                                                                  \
        const bf16x8 pa0 = pack8(sc[0], 0);                                                                \
        _Pragma("unroll") for (int i = 8; i < 16; ++i) sc[0][i] = __builtin_amdgcn_exp2f(sc[0][i]);         \
        sn[1] = mfma16(k1c, q1[1], f32x16{});                                                              \
        const bf16x8 pa1 = pack8(sc[0], 8);                                                                \
        o[0] = mfma16(v0c, pa0, o[0]);                                                                     \
        _Pragma("unroll") for (int i = 0; i < 8; ++i) sc[1][i] = __builtin_amdgcn_exp2f(sc[1][i]);          \
        sn[1] = mfma8(k2c, q2[1], sn[1]);                                                                  \
        const bf16x8 pb0 = pack8(sc[1], 0);                                                                \
        o[0] = mfma16(v1c, pa1, o[0]);                                                                     \
        _Pragma("unroll") for (int i = 8; i < 16; ++i) sc[1][i] = __builtin_amdgcn_exp2f(sc[1][i]);         \
        o[1] = mfma16(v0c, pb0, o[1]);                                                                     \
        const bf16x8 pb1 = pack8(sc[1], 8);                                                                \
        o[1] = mfma16(v1c, pb1, o[1]);                                                                     \
    } while (0)

    int slot = 0;
    for (int c = 0; c < 8; ++c) {
        WAIT_VM_BAR(0);
        const int nslot = slot == 2 ? 0 : slot + 1, n2slot = nslot == 2 ? 0 : nslot + 1;
        if (c + 2 < 8) issue(c + 2, n2slot);
        lds_cptr kc = ka + slot * AT_CHUNK, kc2 = kb + slot * AT_CHUNK, vc = va + slot * AT_CHUNK;
        const int ns = (c + 1 < 8) ? nslot : slot;
        lds_cptr kn = ka + ns * AT_CHUNK, kn2 = kb + ns * AT_CHUNK, vn = va + ns * AT_CHUNK;
        ATT_STEP(sA, sB, k1A, k2A, v0A, v1A, k1B, k2B, v0B, v1B, 2 * 3072, 1 * 3072, kc, kc2, vc);
        ATT_STEP(sB, sA, k1B, k2B, v0B, v1B, k1A, k2A, v0A, v1A, 3 * 3072, 2 * 3072, kc, kc2, vc);
        ATT_STEP(sA, sB, k1A, k2A, v0A, v1A, k1B, k2B, v0B, v1B, 4 * 3072, 3 * 3072, kc, kc2, vc);
        ATT_STEP(sB, sA, k1B, k2B, v0B, v1B, k1A, k2A, v0A, v1A, 5 * 3072, 4 * 3072, kc, kc2, vc);
        ATT_STEP(sA, sB, k1A, k2A, v0A, v1A, k1B, k2B, v0B, v1B, 6 * 3072, 5 * 3072, kc, kc2, vc);
        ATT_STEP(sB, sA, k1B, k2B, v0B, v1B, k1A, k2A, v0A, v1A, 7 * 3072, 6 * 3072, kc, kc2, vc);
        ATT_STEP(sA, sB, k1A, k2A, v0A, v1A, k1B, k2B, v0B, v1B, 0, 7 * 3072, kn, kn2, vc);
        ATT_STEP(sB, sA, k1B, k2B, v0B, v1B, k1A, k2A, v0A, v1A, 3072, 0, kn, kn2, vn);
        slot = nslot;
    }
#undef ATT_STEP
#pragma unroll
    for (int a = 0; a < 2; ++a) {
        float l = __shfl(o[a][8], r + 32);
        bool ok = (l > 9.094947e-13f) && (l < 1.8446744e19f);
#pragma unroll
        for (int i = 0; i < 12; ++i) ok = ok && (fabsf(o[a][i]) < INFINITY || (h && i >= 8));
        if (!__all(ok)) {
            s16x4 q2z = q2[a];
            if (h) q2z[0] = 0;
            attn_slow(kvh, q1[a], q2z, lane, o[a]);
            l = __shfl(o[a][8], r + 32);
        }
        const int tile = qb * 8 + 2 * w + a;
        const float g = ((const float*)(ws + WS_G))[tile * 32 + r];
        const float sc = g / l;
        char* op = ws + WS_OF + (size_t)tile * 40 * 1024;
        u32x4 m = {pk2(o[a][0] * sc, o[a][1] * sc), pk2(o[a][2] * sc, o[a][3] * sc), pk2(o[a][4] * sc, o[a][5] * sc), pk2(o[a][6] * sc, o[a][7] * sc)};
        *(u32x4*)(op + head * 1024 + lane * 16) = m;
        if (h == 0) {
            u32x2 e = {pk2(o[a][8] * sc, o[a][9] * sc), pk2(o[a][10] * sc, o[a][11] * sc)};
            *(u32x2*)(op + (32 + (head >> 2)) * 1024 + (((head >> 1) & 1) * 32 + r) * 16 + (head & 1) * 8) = e;
        }
    }
}

constexpr int F1_STAGE = 20480;

DEV float gelu_tanh(float x) {
    const float c0 = -2.0f * 0.7978845608028654f * 1.4426950408889634f, c1 = c0 * 0.044715f;
    const float e = __builtin_amdgcn_exp2f(x * __builtin_fmaf(x * x, c1, c0));
    return x * __builtin_amdgcn_rcpf(1.0f + e);
}

DEV void ffn1_role(const char* x, const char* wg, const char* wu, char* ws, int item, lds_ptr lds) {
    const int tid = threadIdx.x, lane = tid & 63, w = __builtin_amdgcn_readfirstlane(tid >> 6);
    const int th = w >> 1, fh = w & 1, r = lane & 31, h = lane >> 5;
    const int xcd = item & 7, j = item >> 3;
    const int mt = (xcd & 1) * 8 + (j & 7), ft = (xcd >> 1) * 4 + (j >> 3);
    const int m0 = mt * 128, f0 = ft * 96;
    const i32x4 sx = make_srd(x), sw = make_srd(w < 2 ? wg : wu);
    const unsigned l0 = (unsigned)(uintptr_t)lds;
    const unsigned vx = (unsigned)(lane >> 2) * 1280u + (unsigned)(((lane & 3) ^ ((lane >> 4) & 3)) * 16);
    unsigned vw[3];
#pragma unroll
    for (int i = 0; i < 3; ++i) {
        const int L = ((w & 1) * 3 + i) * 64 + lane, row = L / 12, c16 = L - row * 12;
        vw[i] = (unsigned)row * 3072u + (unsigned)((w < 2 ? c16 : (c16 ^ 2)) * 16);
    }
    const unsigned sxb = (unsigned)(m0 + 32 * w) * 1280u, swb = (unsigned)f0 * 2u;
    auto issue = [&](int t) {
        const unsigned lb = l0 + (unsigned)(t & 3) * F1_STAGE;
#pragma unroll
        for (int i = 0; i < 2; ++i) dma16(sx, vx, sxb + (unsigned)i * 20480u + (unsigned)t * 64u, lb + (unsigned)(2 * w + i) * 1024u);
#pragma unroll
        for (int i = 0; i < 3; ++i) dma16(sw, vw[i], swb + (unsigned)t * (32u * 3072u), lb + 8192u + (unsigned)(3 * w + i) * 1024u);
    };
    issue(0); issue(1); issue(2);
    lds_cptr xa[2], wa[3];
#pragma unroll
    for (int s = 0; s < 2; ++s) xa[s] = (lds_cptr)lds + (th * 64 + r) * 64 + (((2 * s + h) ^ ((r >> 2) & 3)) * 16);
    {
        const int g = lane >> 4, sel = g & 1, q = (lane & 15) >> 2, p = lane & 3;
#pragma unroll
        for (int a = 0; a < 3; ++a) {
            const int aa = fh * 3 + a, c16 = 2 * (sel ? (aa ^ 1) : aa) + (p >> 1);
            wa[a] = (lds_cptr)lds + 8192 + sel * 6144 + (8 * h + q) * 192 + c16 * 16 + 8 * (p & 1);
        }
    }
    f32x16 acc[3][2];
#pragma unroll
    for (int a = 0; a < 3; ++a) { acc[a][0] = f32x16{}; acc[a][1] = f32x16{}; }
    bf16x8 xf[2][2], wf[2][3];
    auto ldfrag = [&](int buf, int so, int s) {
#pragma unroll
        for (int tt = 0; tt < 2; ++tt) xf[buf][tt] = lds_ld128(xa[s] + so + tt * 2048);
#pragma unroll
        for (int a = 0; a < 3; ++a) wf[buf][a] = cat8(lds_tr(wa[a] + so + s * 3072), lds_tr(wa[a] + so + s * 3072 + 768));
    };
    WAIT_VM_BAR(10);
    ldfrag(0, 0, 0);
#pragma unroll
    for (int t = 0; t < 20; ++t) {
        if (t < 18) WAIT_VM_BAR(5); else if (t == 18) WAIT_VM_BAR(0);
        if (t + 3 < 20) issue(t + 3);
#pragma unroll
        for (int s = 0; s < 2; ++s) {
            if (s == 0) ldfrag(1, (t & 3) * F1_STAGE, 1); else if (t < 19) ldfrag(0, ((t + 1) & 3) * F1_STAGE, 0);
#pragma unroll
            for (int a = 0; a < 3; ++a)
#pragma unroll
                for (int tt = 0; tt < 2; ++tt) acc[a][tt] = mfma16(wf[s][a], xf[s][tt], acc[a][tt]);
        }
    }
#pragma unroll
    for (int a = 0; a < 3; ++a)
#pragma unroll
        for (int tt = 0; tt < 2; ++tt) {
            f32x16 hv;
#pragma unroll
            for (int i = 0; i < 8; ++i) hv[i] = gelu_tanh(acc[a][tt][i]) * acc[a][tt][8 + i];
            const int tile = mt * 4 + th * 2 + tt, fs = ft * 6 + fh * 3 + a;
            *(bf16x8*)(ws + WS_HF + (size_t)(tile * 96 + fs) * 1024 + lane * 16) = pack8(hv, 0);
        }
}

__global__ __launch_bounds__(256, 2) void k2_kernel(const char* __restrict__ xb, const char* __restrict__ wg, const char* __restrict__ wu, char* __restrict__ ws) {
    extern __shared__ __attribute__((aligned(16))) char lds_raw[];
    lds_ptr lds = (lds_ptr)lds_raw;
    if (blockIdx.x < 256) attn_role(ws, blockIdx.x, lds);
    else ffn1_role(xb, wg, wu, ws, blockIdx.x - 256, lds);
}

constexpr int O_WAVE_LDS = 4 * 3072;

DEV void bufld16(bf16x8& dst, const i32x4& srd, unsigned voff, unsigned soff) {
    asm volatile("buffer_load_dwordx4 %0, %1, %2, %3 offen" : "=v"(dst) : "v"(voff), "s"(srd), "s"(soff) : "memory");
}
#define WAIT_VM_2(n, a, b) asm volatile("s_waitcnt vmcnt(" #n ")" : "+v"(a), "+v"(b) :: "memory")

DEV void out_role(const char* wd, const char* wo, char* ws, char* out, int item, lds_ptr lds) {
    const int tid = threadIdx.x, lane = tid & 63, w = __builtin_amdgcn_readfirstlane(tid >> 6);
    const int r = lane & 31, h = lane >> 5;
    const int xcd = item & 7, j = item >> 3;
    const int reg = j >> 2, sub = j & 3;
    const int rgp = (xcd & 3) * 4 + (reg & 3), cgp = (xcd >> 2) * 2 + (reg >> 2);
    const int rp = sub >> 1, cp = sub & 1;
    const int t0 = rgp * 4 + 2 * rp + cp, t1 = rgp * 4 + 2 * rp + (cp ^ 1);
    const int c0 = cgp * 160 + cp * 64, c4 = cgp * 160 + 128;
    const unsigned l0 = (unsigned)(uintptr_t)lds + (unsigned)w * O_WAVE_LDS;
    const i32x4 swd = make_srd(wd), swo = make_srd(wo), sh = make_srd(ws + WS_HF), so_ = make_srd(ws + WS_OF);
    const unsigned vw = (unsigned)(lane >> 2) * 1280u + (unsigned)(lane & 3) * 16u;
    const int rho = lane >> 2;
    const unsigned vwr = (unsigned)(20 * (2 * ((rho >> 2) & 1) + (rho >> 3)) + 16 + (rho & 3)) * 1280u + (unsigned)(lane & 3) * 16u;
    const unsigned va = lane * 16;
    bf16x8 a0[4], a1[4];
    auto issue = [&](int s) {
        const unsigned lb = l0 + (unsigned)(s & 3) * 3072u;
        if (s < 24) {
            const int ks = w * 24 + s;
            bufld16(a0[s & 3], sh, va, (unsigned)(t0 * 96 + ks) * 1024u); bufld16(a1[s & 3], sh, va, (unsigned)(t1 * 96 + ks) * 1024u);
            const unsigned so = (unsigned)(ks * 16) * 1280u;
            dma16(swd, vw, so + (unsigned)c0 * 2u, lb); dma16(swd, vw, so + (unsigned)c0 * 2u + 64u, lb + 1024u); dma16(swd, vw, so + (unsigned)c4 * 2u, lb + 2048u);
        } else {
            const int ks = w * 10 + (s - 24);
            bufld16(a0[s & 3], so_, va, (unsigned)(t0 * 40 + ks) * 1024u); bufld16(a1[s & 3], so_, va, (unsigned)(t1 * 40 + ks) * 1024u);
            const unsigned so = ks < 32 ? (unsigned)(ks * 20) * 1280u : (unsigned)((ks - 32) * 80) * 1280u;
            const unsigned vv = ks < 32 ? vw : vwr;
            dma16(swo, vv, so + (unsigned)c0 * 2u, lb); dma16(swo, vv, so + (unsigned)c0 * 2u + 64u, lb + 1024u); dma16(swo, vv, so + (unsigned)c4 * 2u, lb + 2048u);
        }
    };
    lds_cptr wa;
    {
        const int g = lane >> 4, nh = g & 1, q = (lane & 15) >> 2, p = lane & 3;
        wa = (lds_cptr)lds + w * O_WAVE_LDS + (4 * h + q) * 64 + nh * 32 + p * 8;
    }
    f32x16 acc[5];
#pragma unroll
    for (int i = 0; i < 5; ++i) acc[i] = f32x16{};
    const float g0 = ((const float*)(ws + WS_G))[t0 * 32 + r], g1 = ((const float*)(ws + WS_G))[t1 * 32 + r];
    asm volatile("" :: "v"(g0), "v"(g1));
    WAIT_VM(0);
    issue(0); issue(1); issue(2);
#pragma unroll
    for (int s = 0; s < 34; ++s) {
        if (s + 3 < 34) { issue(s + 3); WAIT_VM_2(15, a0[s & 3], a1[s & 3]); }
        else if (s == 31) WAIT_VM_2(10, a0[s & 3], a1[s & 3]);
        else if (s == 32) WAIT_VM_2(5, a0[s & 3], a1[s & 3]);
        else WAIT_VM_2(0, a0[s & 3], a1[s & 3]);
        lds_cptr p = wa + (s & 3) * 3072;
        const bf16x8 w0 = cat8(lds_tr(p), lds_tr(p + 512));
        const bf16x8 w1 = cat8(lds_tr(p + 1024), lds_tr(p + 1536));
        const bf16x8 w4 = cat8(lds_tr(p + 2048), lds_tr(p + 2560));
        acc[0] = mfma16(w0, a0[s & 3], acc[0]); acc[1] = mfma16(w1, a0[s & 3], acc[1]);
        acc[2] = mfma16(w0, a1[s & 3], acc[2]); acc[3] = mfma16(w1, a1[s & 3], acc[3]);
        acc[4] = mfma16(w4, a0[s & 3], acc[4]);
        if (s == 23) {
#pragma unroll
            for (int i = 0; i < 16; ++i) { acc[0][i] *= 1.f - g0; acc[1][i] *= 1.f - g0; acc[4][i] *= 1.f - g0; acc[2][i] *= 1.f - g1; acc[3][i] *= 1.f - g1; }
        }
    }
    __syncthreads();
    typedef __attribute__((ext_vector_type(4))) float f32x4;
    typedef __attribute__((address_space(3))) f32x4* lds_f4;
#pragma unroll
    for (int i = 0; i < 5; ++i)
#pragma unroll
        for (int g = 0; g < 4; ++g) {
            f32x4 v = {acc[i][4 * g], acc[i][4 * g + 1], acc[i][4 * g + 2], acc[i][4 * g + 3]};
            *(lds_f4)(lds + w * 20480 + i * 4096 + r * 128 + (((2 * g + h) ^ (r & 7)) * 16)) = v;
        }
    __syncthreads();
#pragma unroll
    for (int it = 0; it < 3; ++it) {
        const int u = tid + 256 * it;
        if (u < 640) {
            const int tl = u >> 7, tok = (u & 127) >> 2, ch = u & 3;
            f32x4 lo = {0, 0, 0, 0}, hi = {0, 0, 0, 0};
#pragma unroll
            for (int ww = 0; ww < 4; ++ww) {
                lds_ptr p = lds + ww * 20480 + tl * 4096 + tok * 128;
                lo += *(lds_f4)(p + (((2 * ch) ^ (tok & 7)) * 16));
                hi += *(lds_f4)(p + (((2 * ch + 1) ^ (tok & 7)) * 16));
            }
            const int tt = (tl == 2 || tl == 3) ? t1 : t0, cc = tl == 4 ? c4 : c0 + (tl & 1) * 32;
            u32x4 o = {pk2(lo[0], lo[1]), pk2(lo[2], lo[3]), pk2(hi[0], hi[1]), pk2(hi[2], hi[3])};
            *(u32x4*)(out + (size_t)(tt * 32 + tok) * 1280 + (cc + ch * 8) * 2) = o;
        }
    }
}

__global__ __launch_bounds__(256, 2) void k3_kernel(const char* __restrict__ wd, const char* __restrict__ wo, char* __restrict__ ws, char* __restrict__ out) {
    extern __shared__ __attribute__((aligned(16))) char lds_raw[];
    out_role(wd, wo, ws, out, blockIdx.x, (lds_ptr)lds_raw);
}

extern "C" void kernel_launch(void* const* d_in, const int* in_sizes, int n_in, void* d_out, int out_size, void* d_ws, size_t ws_size, hipStream_t stream) {
    const char* ax = (const char*)d_in[0];
    const char* wqkv = (const char*)d_in[1];
    const char* wo = (const char*)d_in[2];
    const char* bx = (const char*)d_in[3];
    const char* wg = (const char*)d_in[4];
    const char* wu = (const char*)d_in[5];
    const char* wd = (const char*)d_in[6];
    const char* gw = (const char*)d_in[7];
    char* ws = (char*)d_ws;
    (void)hipFuncSetAttribute((const void*)k1_kernel, hipFuncAttributeMaxDynamicSharedMemorySize, K1_LDS);
    (void)hipFuncSetAttribute((const void*)k2_kernel, hipFuncAttributeMaxDynamicSharedMemorySize, 81920);
    (void)hipFuncSetAttribute((const void*)k3_kernel, hipFuncAttributeMaxDynamicSharedMemorySize, 81920);
    k1_kernel<<<256, 256, K1_LDS, stream>>>(ax, wqkv, gw, ws);
    k2_kernel<<<512, 256, 81920, stream>>>(ax, wg, wu, ws);
    k3_kernel<<<256, 256, 81920, stream>>>(wd, wo, ws, (char*)d_out);
}
```

```cpp
#define A_PRIO 0
#define G_PRIO 2
#include <hip/hip_runtime.h>
#include <hip/hip_bf16.h>
#include <stdint.h>
#include <type_traits>

typedef __attribute__((ext_vector_type(8))) __bf16 bf16x8;
typedef __attribute__((ext_vector_type(8))) short s16x8;
typedef __attribute__((ext_vector_type(4))) short s16x4;
typedef __attribute__((ext_vector_type(2))) __bf16 bf16x2;
typedef __attribute__((ext_vector_type(2))) float f32x2;
typedef __attribute__((ext_vector_type(16))) float f32x16;
typedef __attribute__((ext_vector_type(4))) unsigned u32x4;
typedef __attribute__((ext_vector_type(2))) unsigned u32x2;
typedef __attribute__((ext_vector_type(4))) int i32x4;
typedef __attribute__((address_space(3))) const char* lds_cptr;
typedef __attribute__((address_space(3))) char* lds_ptr;

#define DEV __device__ __forceinline__

constexpr int T_ = 2048, D_ = 640, NH = 32, DH = 20, F_ = 1536;
constexpr float QSCALE = 0.22360679774997896f * 1.4426950408889634f;

constexpr size_t WS_QF = 0;
constexpr size_t WS_KV = WS_QF + (size_t)NH * 64 * 1280;
constexpr size_t WS_HF = WS_KV + (size_t)NH * 64 * 3072;
constexpr size_t WS_OF = WS_HF + (size_t)64 * 96 * 1024;
constexpr size_t WS_XF = WS_OF + (size_t)64 * 40 * 1024;
constexpr size_t WS_G  = WS_XF + (size_t)64 * 40 * 1024;
constexpr size_t WS_FLAG = WS_G + 2048 * 4;
constexpr int FLAG_LINES = 32;
constexpr size_t WS_END = WS_FLAG + FLAG_LINES * 256;

DEV i32x4 make_srd(const void* p) {
    i32x4 s;
    s[0] = __builtin_amdgcn_readfirstlane((int)(uint32_t)(uintptr_t)p);
    s[1] = __builtin_amdgcn_readfirstlane((int)(uint32_t)((uintptr_t)p >> 32)) & 0xffff;
    s[2] = 0x7fffffff;
    s[3] = 0x00020000;
    return s;
}
#define RFL(x) (x)
#define PADS
DEV void dma16(const i32x4& srd, unsigned voff, unsigned soff, unsigned ldsaddr) {
    asm volatile(PADS "s_mov_b32 m0, %3\n\ts_nop 0\n\tbuffer_load_dwordx4 %0, %1, %2 offen lds"
                 :: "v"(voff), "s"(srd), "s"(RFL(soff)), "s"(RFL(ldsaddr)) : "memory");
}
DEV void warm(unsigned& sink, const char* base, unsigned voff) {
    asm volatile("global_load_dword %0, %1, %2" : "=v"(sink) : "v"(voff), "s"(base) : "memory");
}
#define WAIT_VM(n) asm volatile("s_waitcnt vmcnt(" #n ")" ::: "memory")
#define WAIT_VM_BAR(n) asm volatile("s_waitcnt vmcnt(" #n ")\n\ts_barrier" ::: "memory")
#define SBAR() __builtin_amdgcn_sched_barrier(0)

DEV f32x16 mfma16(bf16x8 a, bf16x8 b, f32x16 c) { return __builtin_amdgcn_mfma_f32_32x32x16_bf16(a, b, c, 0, 0, 0); }
DEV f32x16 mfma8(s16x4 a, s16x4 b, f32x16 c) { return __builtin_amdgcn_mfma_f32_32x32x8bf16_1k(a, b, c, 0, 0, 0); }
DEV unsigned pk2(float lo, float hi) {
    f32x2 v = {lo, hi};
    bf16x2 r = __builtin_convertvector(v, bf16x2);
    return __builtin_bit_cast(unsigned, r);
}
DEV bf16x8 lds_ld128(lds_cptr p) { return *(const __attribute__((address_space(3))) bf16x8*)p; }
DEV s16x4 lds_ld64(lds_cptr p) { return *(const __attribute__((address_space(3))) s16x4*)p; }
DEV s16x4 lds_tr(lds_cptr p) { return __builtin_amdgcn_ds_read_tr16_b64_v4i16((__attribute__((address_space(3))) s16x4*)p); }
DEV bf16x8 cat8(s16x4 lo, s16x4 hi) {
    s16x8 r = __builtin_shufflevector(lo, hi, 0, 1, 2, 3, 4, 5, 6, 7);
    return __builtin_bit_cast(bf16x8, r);
}
DEV bf16x8 pack8(const f32x16& v, int base) {
    u32x4 r = {pk2(v[base], v[base + 1]), pk2(v[base + 2], v[base + 3]), pk2(v[base + 4], v[base + 5]), pk2(v[base + 6], v[base + 7])};
    return __builtin_bit_cast(bf16x8, r);
}

typedef __attribute__((address_space(1))) unsigned gu32;
#define RLX_AGENT __ATOMIC_RELAXED, __HIP_MEMORY_SCOPE_AGENT
DEV __amdgpu_buffer_rsrc_t make_rsrc(void* p) { return __builtin_amdgcn_make_buffer_rsrc(p, 0, 0x7fffffff, 0x00020000); }
DEV void st16_sc1(__amdgpu_buffer_rsrc_t rs, unsigned voff, u32x4 v) { __builtin_amdgcn_raw_buffer_store_b128(v, rs, voff, 0, 16); }
DEV void st8_sc1(__amdgpu_buffer_rsrc_t rs, unsigned voff, u32x2 v) { __builtin_amdgcn_raw_buffer_store_b64(v, rs, voff, 0, 16); }
DEV void publish(char* ws, int line) {
    asm volatile("s_waitcnt vmcnt(0)\n\ts_barrier" ::: "memory");
    if (threadIdx.x == 0) __hip_atomic_fetch_add((gu32*)(ws + WS_FLAG + line * 256), 1u, RLX_AGENT);
}
DEV void consume(char* ws, int line, unsigned target) {
    if (threadIdx.x < 64) {
        gu32* f = (gu32*)(ws + WS_FLAG + line * 256);
        for (unsigned spins = 0; spins < (1u << 20); ++spins) {
            if ((unsigned)__builtin_amdgcn_readfirstlane(__hip_atomic_load(f, RLX_AGENT)) >= target) break;
            __builtin_amdgcn_s_sleep(2);
        }
        __builtin_amdgcn_fence(__ATOMIC_ACQUIRE, "agent");
        asm volatile("s_waitcnt vmcnt(0)" ::: "memory");
    }
    asm volatile("s_barrier" ::: "memory");
}

#define STAMP(i) do {} while (0)
constexpr int K1_STAGE = 32768, K1_LDS = 4 * K1_STAGE;

template <bool VMODE>
DEV void k1_gemm(const char* x, const char* wqkv, const char* wg, const char* wu, char* ws, int mt, int nt, lds_ptr lds) {
    constexpr int SK = 0;
    const int tid = threadIdx.x, lane = tid & 63, w8 = __builtin_amdgcn_readfirstlane(tid >> 6), w = w8 & 3;
    const int wt = w >> 1, ww = w & 1, r = lane & 31, h = lane >> 5;
    const int m0 = mt * 128, n0 = nt * 128;
    STAMP(0);
    if (w8 >= 4) {
        const i32x4 sx = make_srd(x), sw = make_srd(wqkv);
        const unsigned l0 = (unsigned)(uintptr_t)lds;
        const unsigned xq = lane >> 4;
        const unsigned vx0 = (unsigned)(lane >> 3) * 1280u + (unsigned)(((lane & 7) ^ xq) * 16);
        const unsigned vx1 = (unsigned)(lane >> 3) * 1280u + (unsigned)(((lane & 7) ^ xq ^ 4) * 16);
        const unsigned vw = (unsigned)(lane >> 4) * 3840u + (unsigned)(((lane & 15) ^ ((lane >> 4) << 2)) * 16);
        const unsigned sxb = (unsigned)(m0 + 32 * w) * 1280u, swb = (unsigned)(16 * w) * 3840u + (unsigned)n0 * 2u;
        auto issue = [&](int t) {
            const unsigned lb = l0 + (unsigned)(t & 3) * K1_STAGE;
#pragma unroll
            for (int j = 0; j < 4; ++j) {
                dma16(sx, (j & 1) ? vx1 : vx0, sxb + (unsigned)j * 10240u + (unsigned)t * 128u, lb + (unsigned)(4 * w + j) * 1024u);
                dma16(sw, vw, swb + (unsigned)(t * 64 + 4 * j) * 3840u, lb + 16384u + (unsigned)(4 * w + j) * 1024u);
            }
        };
        issue(0); issue(1); issue(2);
        WAIT_VM_BAR(16);
#pragma unroll
        for (int t = 0; t < 9; ++t) {
            if (t < 8) WAIT_VM_BAR(8); else WAIT_VM_BAR(0);
            if (t + 3 < 10) issue(t + 3);
        }
        return;
    }
    lds_cptr xa[4], wa[2];
#pragma unroll
    for (int s = 0; s < 4; ++s) xa[s] = (lds_cptr)lds + (wt * 64 + r) * 128 + (((2 * s + h) ^ ((r >> 1) & 7)) * 16);
    {
        const int g = lane >> 4, nh = g & 1, q = (lane & 15) >> 2, p = lane & 3;
#pragma unroll
        for (int ct = 0; ct < 2; ++ct) {
            const int slot = ww * 8 + ct * 4 + 2 * nh + (p >> 1);
            wa[ct] = (lds_cptr)lds + 16384 + (8 * h + q) * 256 + ((slot ^ (q << 2)) * 16) + 8 * (p & 1);
        }
    }
    f32x16 acc[2][2];
#pragma unroll
    for (int i = 0; i < 2; ++i)
#pragma unroll
        for (int j = 0; j < 2; ++j) acc[i][j] = f32x16{};
    bf16x8 xf[2][2], wf[2][2];
    auto ldfrag = [&](int buf, int t, int s) {
        const int so = (t & 3) * K1_STAGE;
#pragma unroll
        for (int i = 0; i < 2; ++i) {
            xf[buf][i] = lds_ld128(xa[s] + so + i * 4096);
            wf[buf][i] = cat8(lds_tr(wa[i] + so + s * 4096), lds_tr(wa[i] + so + s * 4096 + 1024));
        }
    };
    asm volatile("s_barrier" ::: "memory");
    STAMP(1);
    ldfrag(0, 0, 0);
#pragma unroll
    for (int t = 0; t < 10; ++t) {
        if (t < 9) asm volatile("s_barrier" ::: "memory");
#pragma unroll
        for (int s = 0; s < 4; ++s) {
            const int cur = s & 1;
            if (s < 3) ldfrag(cur ^ 1, t, s + 1); else if (t < 9) ldfrag(cur ^ 1, t + 1, 0);
            SBAR();
#pragma unroll
            for (int i = 0; i < 2; ++i)
#pragma unroll
                for (int j = 0; j < 2; ++j)
                    acc[i][j] = VMODE ? mfma16(xf[cur][i], wf[cur][j], acc[i][j]) : mfma16(wf[cur][i], xf[cur][j], acc[i][j]);
            SBAR();
        }
    }
    STAMP(2);
    if constexpr (!VMODE) {
        const bool isq = nt < 5;
        const int csec = (isq ? n0 : n0 - 640) + ww * 64;
        char* base = ws + (isq ? WS_QF : WS_KV);
        const int tstride = isq ? 1280 : 3072;
        const float sc = isq ? QSCALE : 1.0f;
#pragma unroll
        for (int ct = 0; ct < 2; ++ct)
#pragma unroll
            for (int g = 0; g < 4; ++g) {
                const int qd = (csec + ct * 32 + 8 * g + 4 * h) >> 2, head = qd / 5, dq = qd - head * 5;
                const int inoff = dq < 4 ? (dq >> 1) * 512 + r * 16 + (dq & 1) * 8 : 1024 + r * 8;
#pragma unroll
                for (int tt = 0; tt < 2; ++tt) {
                    const int tile = mt * 4 + wt * 2 + tt;
                    char* p = base + (size_t)(head * 64 + tile) * tstride + inoff;
                    const f32x16& a = acc[ct][tt];
                    u32x2 v = {pk2(a[4 * g] * sc, a[4 * g + 1] * sc), pk2(a[4 * g + 2] * sc, a[4 * g + 3] * sc)};
                    *(u32x2*)p = v;
                    if (!isq && dq == 4) *(u32x2*)(p + 256) = (u32x2){0x00003f80u, 0u};
                }
            }
    } else {
#pragma unroll
        for (int ct = 0; ct < 2; ++ct) {
            const int col = n0 - 1280 + ww * 64 + ct * 32 + r, head = col / 20, d = col - head * 20;
#pragma unroll
            for (int tt = 0; tt < 2; ++tt) {
                const int tile = mt * 4 + wt * 2 + tt;
                char* p = ws + WS_KV + (size_t)(head * 64 + tile) * 3072 + 1536 + h * 384 + d * 16;
#pragma unroll
                for (int s = 0; s < 2; ++s) {
                    *(bf16x8*)(p + s * 768) = pack8(acc[tt][ct], 8 * s);
                    if (d == 0) *(u32x4*)(p + s * 768 + 320) = (u32x4){0x3f803f80u, 0x3f803f80u, 0x3f803f80u, 0x3f803f80u};
                    if (d == 1) *(u32x4*)(p + s * 768 + 320) = (u32x4){0u, 0u, 0u, 0u};
                }
            }
        }
    }
    STAMP(3);
}

DEV void k1_gate(const char* x, const char* gw, char* ws, int mt) {
    const int tid = threadIdx.x, lane = tid & 63, w = tid >> 6, r = lane & 31, h = lane >> 5;
    const char* xr = x + (size_t)(mt * 128 + w * 32 + r) * 1280 + h * 16;
    f32x16 acc = f32x16{};
#pragma unroll 10
    for (int s = 0; s < 40; ++s) {
        const bf16x8 a = *(const bf16x8*)(xr + s * 32);
        const bf16x8 b = *(const bf16x8*)(gw + s * 32 + h * 16);
        *(bf16x8*)(ws + WS_XF + (size_t)((mt * 4 + w) * 40 + s) * 1024 + lane * 16) = a;
        acc = mfma16(a, b, acc);
    }
    if (r == 0) {
        float* g = (float*)(ws + WS_G) + mt * 128 + w * 32;
#pragma unroll
        for (int i = 0; i < 16; ++i) g[(i & 3) + 8 * (i >> 2) + 4 * h] = 1.0f / (1.0f + __expf(-acc[i]));
    }
}

__global__ __launch_bounds__(512, 2) void k1_kernel(const char* __restrict__ x, const char* __restrict__ wqkv, const char* __restrict__ gw, const char* __restrict__ wg, const char* __restrict__ wu, char* __restrict__ ws) {
    extern __shared__ __attribute__((aligned(16))) char lds_raw[];
    lds_ptr lds = (lds_ptr)lds_raw;
    const int b = blockIdx.x, xcd = b & 7, j = b >> 3, rg = xcd & 3, cg = xcd >> 2;
    if (cg == 1 && j >= 28) {
        if (b == 255 && threadIdx.x < FLAG_LINES) *(unsigned*)(ws + WS_FLAG + threadIdx.x * 256) = 0u;
        if (threadIdx.x < 256) k1_gate(x, gw, ws, rg * 4 + (j - 28));
        return;
    }
    const int mt = rg * 4 + (j & 3), nt = cg * 8 + (j >> 2);
    if (nt < 10) k1_gemm<false>(x, wqkv, wg, wu, ws, mt, nt, lds); else k1_gemm<true>(x, wqkv, wg, wu, ws, mt, nt, lds);
}

constexpr int AT_CHUNK = 24576;

DEV void attn_slow(const char* kvh, bf16x8 q1, s16x4 q2, int lane, f32x16& o) {
    const int r = lane & 31, h = lane >> 5;
    const int vrow = r < 21 ? r : 21;
    float m = -INFINITY;
    o = f32x16{};
    for (int t = 0; t < 64; ++t) {
        const char* p = kvh + t * 3072;
        const bf16x8 k1 = *(const bf16x8*)(p + lane * 16);
        const s16x4 k2 = *(const s16x4*)(p + 1024 + lane * 8);
        const bf16x8 v0 = *(const bf16x8*)(p + 1536 + h * 384 + vrow * 16);
        const bf16x8 v1 = *(const bf16x8*)(p + 2304 + h * 384 + vrow * 16);
        f32x16 s = mfma16(k1, q1, f32x16{});
        s = mfma8(k2, q2, s);
        float tm = s[0];
#pragma unroll
        for (int i = 1; i < 16; ++i) tm = fmaxf(tm, s[i]);
        tm = fmaxf(tm, __shfl_xor(tm, 32));
        const float mn = fmaxf(m, tm);
        const float alpha = __builtin_amdgcn_exp2f(m - mn);
        m = mn;
#pragma unroll
        for (int i = 0; i < 16; ++i) { s[i] = __builtin_amdgcn_exp2f(s[i] - mn); o[i] *= alpha; }
        o = mfma16(v0, pack8(s, 0), o);
        o = mfma16(v1, pack8(s, 8), o);
    }
}

#define ATT_LOOP_ASM \
    "ds_read_b128 v[72:75], %1\n\t" \
    "ds_read_b64 v[80:81], %2\n\t" \
    "ds_read_b128 v[76:79], %1 offset:3072\n\t" \
    "ds_read_b64 v[82:83], %2 offset:3072\n\t" \
    "ds_read_b128 v[84:87], %3\n\t" \
    "ds_read_b128 v[88:91], %3 offset:768\n\t" \
    "ds_read_b128 v[92:95], %3 offset:3072\n\t" \
    "ds_read_b128 v[96:99], %3 offset:3840\n\t" \
    "s_waitcnt lgkmcnt(6)\n\t" \
    "v_mfma_f32_32x32x16_bf16 v[32:47], v[72:75], %8, 0\n\t" \
    "v_mfma_f32_32x32x8_bf16 v[32:47], v[80:81], %9, v[32:47]\n\t" \
    "s_nop 15\n\t" \
    "1:\n\t" \
    "s_waitcnt vmcnt(3) lgkmcnt(0)\n\t" \
    "s_barrier\n\t" \
    "v_add_u32 %1, %6, %12\n\t" \
    "v_add_u32 %2, %6, %13\n\t" \
    "v_add_u32 %3, %6, %14\n\t" \
    "s_sub_u32 %5, %6, 0x6000\n\t" \
    "s_cmp_eq_u32 %6, 0\n\t" \
    "s_cselect_b32 %5, 0xc000, %5\n\t" \
    "s_add_u32 %5, %5, %15\n\t" \
    "v_mfma_f32_32x32x16_bf16 v[48:63], v[76:79], %8, 0\n\t" \
    "v_exp_f32 v32, v32\n\t" \
    "ds_read_b128 v[72:75], %1 offset:0\n\t" \
    "v_exp_f32 v33, v33\n\t" \
    "ds_read_b64 v[80:81], %2 offset:0\n\t" \
    "v_exp_f32 v34, v34\n\t" \
    "v_exp_f32 v35, v35\n\t" \
    "v_exp_f32 v36, v36\n\t" \
    "s_mov_b32 m0, %5\n\t" \
    "v_exp_f32 v37, v37\n\t" \
    "buffer_load_dwordx4 %10, %11, %4 offen offset:0 lds\n\t" \
    "v_exp_f32 v38, v38\n\t" \
    "v_exp_f32 v39, v39\n\t" \
    "v_mfma_f32_32x32x8_bf16 v[48:63], v[82:83], %9, v[48:63]\n\t" \
    "v_cvt_pk_bf16_f32 v64, v32, v33\n\t" \
    "v_cvt_pk_bf16_f32 v65, v34, v35\n\t" \
    "v_cvt_pk_bf16_f32 v66, v36, v37\n\t" \
    "v_cvt_pk_bf16_f32 v67, v38, v39\n\t" \
    "v_exp_f32 v40, v40\n\t" \
    "v_exp_f32 v41, v41\n\t" \
    "v_exp_f32 v42, v42\n\t" \
    "v_exp_f32 v43, v43\n\t" \
    "v_mfma_f32_32x32x16_bf16 %0, v[84:87], v[64:67], %0\n\t" \
    "ds_read_b128 v[84:87], %3 offset:0\n\t" \
    "v_exp_f32 v44, v44\n\t" \
    "v_exp_f32 v45, v45\n\t" \
    "v_exp_f32 v46, v46\n\t" \
    "v_exp_f32 v47, v47\n\t" \
    "v_cvt_pk_bf16_f32 v68, v40, v41\n\t" \
    "v_cvt_pk_bf16_f32 v69, v42, v43\n\t" \
    "v_cvt_pk_bf16_f32 v70, v44, v45\n\t" \
    "v_cvt_pk_bf16_f32 v71, v46, v47\n\t" \
    "s_waitcnt lgkmcnt(1)\n\t" \
    "v_mfma_f32_32x32x16_bf16 v[32:47], v[72:75], %8, 0\n\t" \
    "v_exp_f32 v48, v48\n\t" \
    "ds_read_b128 v[76:79], %1 offset:3072\n\t" \
    "v_exp_f32 v49, v49\n\t" \
    "ds_read_b64 v[82:83], %2 offset:3072\n\t" \
    "v_exp_f32 v50, v50\n\t" \
    "v_exp_f32 v51, v51\n\t" \
    "v_mfma_f32_32x32x16_bf16 %0, v[88:91], v[68:71], %0\n\t" \
    "ds_read_b128 v[88:91], %3 offset:768\n\t" \
    "v_exp_f32 v52, v52\n\t" \
    "v_exp_f32 v53, v53\n\t" \
    "buffer_load_dwordx4 %10, %11, %4 offen offset:1024 lds\n\t" \
    "v_exp_f32 v54, v54\n\t" \
    "v_exp_f32 v55, v55\n\t" \
    "v_mfma_f32_32x32x8_bf16 v[32:47], v[80:81], %9, v[32:47]\n\t" \
    "v_cvt_pk_bf16_f32 v64, v48, v49\n\t" \
    "v_cvt_pk_bf16_f32 v65, v50, v51\n\t" \
    "v_cvt_pk_bf16_f32 v66, v52, v53\n\t" \
    "v_cvt_pk_bf16_f32 v67, v54, v55\n\t" \
    "v_exp_f32 v56, v56\n\t" \
    "v_exp_f32 v57, v57\n\t" \
    "v_exp_f32 v58, v58\n\t" \
    "v_exp_f32 v59, v59\n\t" \
    "v_mfma_f32_32x32x16_bf16 %0, v[92:95], v[64:67], %0\n\t" \
    "ds_read_b128 v[92:95], %3 offset:3072\n\t" \
    "v_exp_f32 v60, v60\n\t" \
    "v_exp_f32 v61, v61\n\t" \
    "v_exp_f32 v62, v62\n\t" \
    "v_exp_f32 v63, v63\n\t" \
    "v_cvt_pk_bf16_f32 v68, v56, v57\n\t" \
    "v_cvt_pk_bf16_f32 v69, v58, v59\n\t" \
    "v_cvt_pk_bf16_f32 v70, v60, v61\n\t" \
    "v_cvt_pk_bf16_f32 v71, v62, v63\n\t" \
    "s_waitcnt lgkmcnt(1)\n\t" \
    "v_mfma_f32_32x32x16_bf16 v[48:63], v[76:79], %8, 0\n\t" \
    "v_exp_f32 v32, v32\n\t" \
    "ds_read_b128 v[72:75], %1 offset:6144\n\t" \
    "v_exp_f32 v33, v33\n\t" \
    "ds_read_b64 v[80:81], %2 offset:6144\n\t" \
    "v_exp_f32 v34, v34\n\t" \
    "v_exp_f32 v35, v35\n\t" \
    "v_mfma_f32_32x32x16_bf16 %0, v[96:99], v[68:71], %0\n\t" \
    "ds_read_b128 v[96:99], %3 offset:3840\n\t" \
    "v_exp_f32 v36, v36\n\t" \
    "v_exp_f32 v37, v37\n\t" \
    "buffer_load_dwordx4 %10, %11, %4 offen offset:2048 lds\n\t" \
    "v_exp_f32 v38, v38\n\t" \
    "v_exp_f32 v39, v39\n\t" \
    "v_mfma_f32_32x32x8_bf16 v[48:63], v[82:83], %9, v[48:63]\n\t" \
    "v_cvt_pk_bf16_f32 v64, v32, v33\n\t" \
    "v_cvt_pk_bf16_f32 v65, v34, v35\n\t" \
    "v_cvt_pk_bf16_f32 v66, v36, v37\n\t" \
    "v_cvt_pk_bf16_f32 v67, v38, v39\n\t" \
    "v_exp_f32 v40, v40\n\t" \
    "v_exp_f32 v41, v41\n\t" \
    "v_exp_f32 v42, v42\n\t" \
    "v_exp_f32 v43, v43\n\t" \
    "v_mfma_f32_32x32x16_bf16 %0, v[84:87], v[64:67], %0\n\t" \
    "ds_read_b128 v[84:87], %3 offset:6144\n\t" \
    "v_exp_f32 v44, v44\n\t" \
    "v_exp_f32 v45, v45\n\t" \
    "v_exp_f32 v46, v46\n\t" \
    "v_exp_f32 v47, v47\n\t" \
    "v_cvt_pk_bf16_f32 v68, v40, v41\n\t" \
    "v_cvt_pk_bf16_f32 v69, v42, v43\n\t" \
    "v_cvt_pk_bf16_f32 v70, v44, v45\n\t" \
    "v_cvt_pk_bf16_f32 v71, v46, v47\n\t" \
    "s_waitcnt lgkmcnt(1)\n\t" \
    "v_mfma_f32_32x32x16_bf16 v[32:47], v[72:75], %8, 0\n\t" \
    "v_exp_f32 v48, v48\n\t" \
    "ds_read_b128 v[76:79], %1 offset:9216\n\t" \
    "v_exp_f32 v49, v49\n\t" \
    "ds_read_b64 v[82:83], %2 offset:9216\n\t" \
    "v_exp_f32 v50, v50\n\t" \
    "v_exp_f32 v51, v51\n\t" \
    "v_mfma_f32_32x32x16_bf16 %0, v[88:91], v[68:71], %0\n\t" \
    "ds_read_b128 v[88:91], %3 offset:6912\n\t" \
    "v_exp_f32 v52, v52\n\t" \
    "v_exp_f32 v53, v53\n\t" \
    "v_exp_f32 v54, v54\n\t" \
    "v_exp_f32 v55, v55\n\t" \
    "v_mfma_f32_32x32x8_bf16 v[32:47], v[80:81], %9, v[32:47]\n\t" \
    "v_cvt_pk_bf16_f32 v64, v48, v49\n\t" \
    "v_cvt_pk_bf16_f32 v65, v50, v51\n\t" \
    "v_cvt_pk_bf16_f32 v66, v52, v53\n\t" \
    "v_cvt_pk_bf16_f32 v67, v54, v55\n\t" \
    "v_exp_f32 v56, v56\n\t" \
    "v_exp_f32 v57, v57\n\t" \
    "v_exp_f32 v58, v58\n\t" \
    "v_exp_f32 v59, v59\n\t" \
    "v_mfma_f32_32x32x16_bf16 %0, v[92:95], v[64:67], %0\n\t" \
    "ds_read_b128 v[92:95], %3 offset:9216\n\t" \
    "v_exp_f32 v60, v60\n\t" \
    "v_exp_f32 v61, v61\n\t" \
    "v_exp_f32 v62, v62\n\t" \
    "v_exp_f32 v63, v63\n\t" \
    "v_cvt_pk_bf16_f32 v68, v56, v57\n\t" \
    "v_cvt_pk_bf16_f32 v69, v58, v59\n\t" \
    "v_cvt_pk_bf16_f32 v70, v60, v61\n\t" \
    "v_cvt_pk_bf16_f32 v71, v62, v63\n\t" \
    "s_waitcnt lgkmcnt(1)\n\t" \
    "v_mfma_f32_32x32x16_bf16 v[48:63], v[76:79], %8, 0\n\t" \
    "v_exp_f32 v32, v32\n\t" \
    "ds_read_b128 v[72:75], %1 offset:12288\n\t" \
    "v_exp_f32 v33, v33\n\t" \
    "ds_read_b64 v[80:81], %2 offset:12288\n\t" \
    "v_exp_f32 v34, v34\n\t" \
    "v_exp_f32 v35, v35\n\t" \
    "v_mfma_f32_32x32x16_bf16 %0, v[96:99], v[68:71], %0\n\t" \
    "ds_read_b128 v[96:99], %3 offset:9984\n\t" \
    "v_exp_f32 v36, v36\n\t" \
    "v_exp_f32 v37, v37\n\t" \
    "v_exp_f32 v38, v38\n\t" \
    "v_exp_f32 v39, v39\n\t" \
    "v_mfma_f32_32x32x8_bf16 v[48:63], v[82:83], %9, v[48:63]\n\t" \
    "v_cvt_pk_bf16_f32 v64, v32, v33\n\t" \
    "v_cvt_pk_bf16_f32 v65, v34, v35\n\t" \
    "v_cvt_pk_bf16_f32 v66, v36, v37\n\t" \
    "v_cvt_pk_bf16_f32 v67, v38, v39\n\t" \
    "v_exp_f32 v40, v40\n\t" \
    "v_exp_f32 v41, v41\n\t" \
    "v_exp_f32 v42, v42\n\t" \
    "v_exp_f32 v43, v43\n\t" \
    "v_mfma_f32_32x32x16_bf16 %0, v[84:87], v[64:67], %0\n\t" \
    "ds_read_b128 v[84:87], %3 offset:12288\n\t" \
    "v_exp_f32 v44, v44\n\t" \
    "v_exp_f32 v45, v45\n\t" \
    "v_exp_f32 v46, v46\n\t" \
    "v_exp_f32 v47, v47\n\t" \
    "v_cvt_pk_bf16_f32 v68, v40, v41\n\t" \
    "v_cvt_pk_bf16_f32 v69, v42, v43\n\t" \
    "v_cvt_pk_bf16_f32 v70, v44, v45\n\t" \
    "v_cvt_pk_bf16_f32 v71, v46, v47\n\t" \
    "s_waitcnt lgkmcnt(1)\n\t" \
    "v_mfma_f32_32x32x16_bf16 v[32:47], v[72:75], %8, 0\n\t" \
    "v_exp_f32 v48, v48\n\t" \
    "ds_read_b128 v[76:79], %1 offset:15360\n\t" \
    "v_exp_f32 v49, v49\n\t" \
    "ds_read_b64 v[82:83], %2 offset:15360\n\t" \
    "v_exp_f32 v50, v50\n\t" \
    "v_exp_f32 v51, v51\n\t" \
    "v_mfma_f32_32x32x16_bf16 %0, v[88:91], v[68:71], %0\n\t" \
    "ds_read_b128 v[88:91], %3 offset:13056\n\t" \
    "v_exp_f32 v52, v52\n\t" \
    "v_exp_f32 v53, v53\n\t" \
    "v_exp_f32 v54, v54\n\t" \
    "v_exp_f32 v55, v55\n\t" \
    "v_mfma_f32_32x32x8_bf16 v[32:47], v[80:81], %9, v[32:47]\n\t" \
    "v_cvt_pk_bf16_f32 v64, v48, v49\n\t" \
    "v_cvt_pk_bf16_f32 v65, v50, v51\n\t" \
    "v_cvt_pk_bf16_f32 v66, v52, v53\n\t" \
    "v_cvt_pk_bf16_f32 v67, v54, v55\n\t" \
    "v_exp_f32 v56, v56\n\t" \
    "v_exp_f32 v57, v57\n\t" \
    "v_exp_f32 v58, v58\n\t" \
    "v_exp_f32 v59, v59\n\t" \
    "v_mfma_f32_32x32x16_bf16 %0, v[92:95], v[64:67], %0\n\t" \
    "ds_read_b128 v[92:95], %3 offset:15360\n\t" \
    "v_exp_f32 v60, v60\n\t" \
    "v_exp_f32 v61, v61\n\t" \
    "v_exp_f32 v62, v62\n\t" \
    "v_exp_f32 v63, v63\n\t" \
    "v_cvt_pk_bf16_f32 v68, v56, v57\n\t" \
    "v_cvt_pk_bf16_f32 v69, v58, v59\n\t" \
    "v_cvt_pk_bf16_f32 v70, v60, v61\n\t" \
    "v_cvt_pk_bf16_f32 v71, v62, v63\n\t" \
    "s_waitcnt lgkmcnt(1)\n\t" \
    "v_mfma_f32_32x32x16_bf16 v[48:63], v[76:79], %8, 0\n\t" \
    "v_exp_f32 v32, v32\n\t" \
    "ds_read_b128 v[72:75], %1 offset:18432\n\t" \
    "v_exp_f32 v33, v33\n\t" \
    "ds_read_b64 v[80:81], %2 offset:18432\n\t" \
    "v_exp_f32 v34, v34\n\t" \
    "v_exp_f32 v35, v35\n\t" \
    "v_mfma_f32_32x32x16_bf16 %0, v[96:99], v[68:71], %0\n\t" \
    "ds_read_b128 v[96:99], %3 offset:16128\n\t" \
    "v_exp_f32 v36, v36\n\t" \
    "v_exp_f32 v37, v37\n\t" \
    "v_exp_f32 v38, v38\n\t" \
    "v_exp_f32 v39, v39\n\t" \
    "v_mfma_f32_32x32x8_bf16 v[48:63], v[82:83], %9, v[48:63]\n\t" \
    "v_cvt_pk_bf16_f32 v64, v32, v33\n\t" \
    "v_cvt_pk_bf16_f32 v65, v34, v35\n\t" \
    "v_cvt_pk_bf16_f32 v66, v36, v37\n\t" \
    "v_cvt_pk_bf16_f32 v67, v38, v39\n\t" \
    "v_exp_f32 v40, v40\n\t" \
    "v_exp_f32 v41, v41\n\t" \
    "v_exp_f32 v42, v42\n\t" \
    "v_exp_f32 v43, v43\n\t" \
    "v_mfma_f32_32x32x16_bf16 %0, v[84:87], v[64:67], %0\n\t" \
    "ds_read_b128 v[84:87], %3 offset:18432\n\t" \
    "v_exp_f32 v44, v44\n\t" \
    "v_exp_f32 v45, v45\n\t" \
    "v_exp_f32 v46, v46\n\t" \
    "v_exp_f32 v47, v47\n\t" \
    "v_cvt_pk_bf16_f32 v68, v40, v41\n\t" \
    "v_cvt_pk_bf16_f32 v69, v42, v43\n\t" \
    "v_cvt_pk_bf16_f32 v70, v44, v45\n\t" \
    "v_cvt_pk_bf16_f32 v71, v46, v47\n\t" \
    "s_waitcnt lgkmcnt(1)\n\t" \
    "v_mfma_f32_32x32x16_bf16 v[32:47], v[72:75], %8, 0\n\t" \
    "v_exp_f32 v48, v48\n\t" \
    "ds_read_b128 v[76:79], %1 offset:21504\n\t" \
    "v_exp_f32 v49, v49\n\t" \
    "ds_read_b64 v[82:83], %2 offset:21504\n\t" \
    "v_exp_f32 v50, v50\n\t" \
    "v_exp_f32 v51, v51\n\t" \
    "v_mfma_f32_32x32x16_bf16 %0, v[88:91], v[68:71], %0\n\t" \
    "ds_read_b128 v[88:91], %3 offset:19200\n\t" \
    "v_exp_f32 v52, v52\n\t" \
    "v_exp_f32 v53, v53\n\t" \
    "v_exp_f32 v54, v54\n\t" \
    "v_exp_f32 v55, v55\n\t" \
    "v_mfma_f32_32x32x8_bf16 v[32:47], v[80:81], %9, v[32:47]\n\t" \
    "v_cvt_pk_bf16_f32 v64, v48, v49\n\t" \
    "v_cvt_pk_bf16_f32 v65, v50, v51\n\t" \
    "v_cvt_pk_bf16_f32 v66, v52, v53\n\t" \
    "v_cvt_pk_bf16_f32 v67, v54, v55\n\t" \
    "v_exp_f32 v56, v56\n\t" \
    "v_exp_f32 v57, v57\n\t" \
    "v_exp_f32 v58, v58\n\t" \
    "v_exp_f32 v59, v59\n\t" \
    "v_mfma_f32_32x32x16_bf16 %0, v[92:95], v[64:67], %0\n\t" \
    "ds_read_b128 v[92:95], %3 offset:21504\n\t" \
    "v_exp_f32 v60, v60\n\t" \
    "v_exp_f32 v61, v61\n\t" \
    "v_exp_f32 v62, v62\n\t" \
    "v_exp_f32 v63, v63\n\t" \
    "v_cvt_pk_bf16_f32 v68, v56, v57\n\t" \
    "v_cvt_pk_bf16_f32 v69, v58, v59\n\t" \
    "v_cvt_pk_bf16_f32 v70, v60, v61\n\t" \
    "v_cvt_pk_bf16_f32 v71, v62, v63\n\t" \
    "s_nop 1\n\t" \
    "v_mfma_f32_32x32x16_bf16 %0, v[96:99], v[68:71], %0\n\t" \
    "ds_read_b128 v[96:99], %3 offset:22272\n\t" \
    "s_add_u32 %4, %4, 0x6000\n\t" \
    "s_add_u32 %6, %6, 0x6000\n\t" \
    "s_cmp_eq_u32 %6, 0x12000\n\t" \
    "s_cselect_b32 %6, 0, %6\n\t" \
    "s_sub_u32 %7, %7, 1\n\t" \
    "s_cmp_lg_u32 %7, 0\n\t" \
    "s_cbranch_scc1 1b\n\t" \
    "s_waitcnt vmcnt(0) lgkmcnt(0)\n\t" \
    "s_nop 15"
#define ATT_CLOBBERS "v32", "v33", "v34", "v35", "v36", "v37", "v38", "v39", "v40", "v41", "v42", "v43", "v44", "v45", "v46", "v47", "v48", "v49", "v50", "v51", "v52", "v53", "v54", "v55", "v56", "v57", "v58", "v59", "v60", "v61", "v62", "v63", "v64", "v65", "v66", "v67", "v68", "v69", "v70", "v71", "v72", "v73", "v74", "v75", "v76", "v77", "v78", "v79", "v80", "v81", "v82", "v83", "v84", "v85", "v86", "v87", "v88", "v89", "v90", "v91", "v92", "v93", "v94", "v95", "v96", "v97", "v98", "v99"
DEV void attn_role(char* ws, int item, lds_ptr lds) {
    constexpr int SK = 1;
    const int tid = threadIdx.x, lane = tid & 63, w = __builtin_amdgcn_readfirstlane(tid >> 6);
    const int r = lane & 31, h = lane >> 5;
    const int head = (item & 7) * 4 + (item >> 6), qb = (item >> 3) & 7;
    const char* kvh = ws + WS_KV + (size_t)head * 64 * 3072;
    i32x4 skv = make_srd(kvh);
    skv[2] = 64 * 3072;
    const unsigned l0 = (unsigned)(uintptr_t)lds;
    const unsigned vo = lane * 16;
    STAMP(0);
    __builtin_amdgcn_s_setprio(A_PRIO);
    if (w < 6) dma16(skv, vo, (unsigned)w * 1024u, l0 + 3 * AT_CHUNK + (unsigned)w * 1024u);
#pragma unroll
    for (int c = 0; c < 2; ++c)
#pragma unroll
        for (int j = 0; j < 3; ++j) dma16(skv, vo, (unsigned)(6144 + c * AT_CHUNK + (3 * w + j) * 1024), l0 + (unsigned)(c * AT_CHUNK + (3 * w + j) * 1024));
    const int tile = qb * 8 + w;
    const char* qp = ws + WS_QF + (size_t)(head * 64 + tile) * 1280;
    const bf16x8 q1 = *(const bf16x8*)(qp + lane * 16);
    s16x4 q2 = *(const s16x4*)(qp + 1024 + r * 8);
    if (h) q2 = (s16x4){0, 0, 0, 0};
    const int vrow = r < 21 ? r : 21;
    const unsigned kbase = l0 + lane * 16, k2base = l0 + 1024 + lane * 8, vbase = l0 + 1536 + h * 384 + vrow * 16;
    WAIT_VM_BAR(6);
    STAMP(1);
    {
        const bf16x8 k1 = lds_ld128((lds_cptr)lds + 3 * AT_CHUNK + lane * 16); const s16x4 k2 = lds_ld64((lds_cptr)lds + 3 * AT_CHUNK + 1024 + lane * 8);
        f32x16 s = mfma16(k1, q1, f32x16{});
        s = mfma8(k2, q2, s);
        float tm = s[0];
#pragma unroll
        for (int i = 1; i < 16; ++i) tm = fmaxf(tm, s[i]);
        tm = fmaxf(tm, __shfl_xor(tm, 32));
        const unsigned nb = pk2(-tm, 0.f) & 0xffffu;
        if (h) q2[0] = (short)nb;
    }
    f32x16 o = f32x16{};
    STAMP(2);
    {
        unsigned ka = kbase + 3 * AT_CHUNK, kb = k2base + 3 * AT_CHUNK, va = vbase + 3 * AT_CHUNK;
        unsigned soff = 6144 + 2 * AT_CHUNK + w * 3072, ldst, cur = 0, cnt = 8;
        const unsigned wbase = l0 + w * 3072;
        asm volatile(ATT_LOOP_ASM
                     : "+v"(o), "+v"(ka), "+v"(kb), "+v"(va), "+s"(soff), "=&s"(ldst), "+s"(cur), "+s"(cnt)
                     : "v"(q1), "v"(q2), "v"(vo), "s"(skv), "v"(kbase), "v"(k2base), "v"(vbase), "s"(wbase)
                     : "memory", "scc", ATT_CLOBBERS);
    }
    STAMP(3);
    float l = __shfl(o[8], r + 32);
    bool ok = (l > 9.094947e-13f) && (l < 1.8446744e19f);
#pragma unroll
    for (int i = 0; i < 12; ++i) ok = ok && (fabsf(o[i]) < INFINITY || (h && i >= 8));
    if (!__all(ok)) {
        if (h) q2[0] = 0;
        attn_slow(kvh, q1, q2, lane, o);
        l = __shfl(o[8], r + 32);
    }
    const float g = ((const float*)(ws + WS_G))[tile * 32 + r];
    const float sc = g / l;
    const __amdgpu_buffer_rsrc_t ro = make_rsrc(ws + WS_OF);
    const unsigned ob = (unsigned)tile * 40u * 1024u;
    u32x4 m = {pk2(o[0] * sc, o[1] * sc), pk2(o[2] * sc, o[3] * sc), pk2(o[4] * sc, o[5] * sc), pk2(o[6] * sc, o[7] * sc)};
    st16_sc1(ro, ob + (unsigned)(head * 1024 + lane * 16), m);
    if (h == 0) {
        u32x2 e = {pk2(o[8] * sc, o[9] * sc), pk2(o[10] * sc, o[11] * sc)};
        st8_sc1(ro, ob + (unsigned)((32 + (head >> 2)) * 1024 + (((head >> 1) & 1) * 32 + r) * 16 + (head & 1) * 8), e);
    }
    publish(ws, 16 + qb);
    STAMP(4);
}

constexpr int F1_STAGE = 40960;

DEV float gelu_tanh(float x) {
    const float c0 = -2.0f * 0.7978845608028654f * 1.4426950408889634f, c1 = c0 * 0.044715f;
    const float e = __builtin_amdgcn_exp2f(x * __builtin_fmaf(x * x, c1, c0));
    return x * __builtin_amdgcn_rcpf(1.0f + e);
}

DEV void bufld16(bf16x8& dst, const i32x4& srd, unsigned voff, unsigned soff) {
    asm volatile(PADS "buffer_load_dwordx4 %0, %1, %2, %3 offen" : "=v"(dst) : "v"(voff), "s"(srd), "s"(RFL(soff)) : "memory");
}
#define WAIT_VM_2(n, a, b) asm volatile("s_waitcnt vmcnt(" #n ")" : "+v"(a), "+v"(b) :: "memory")

template <int I, int N, class F> DEV void static_for(F&& f) { if constexpr (I < N) { f(std::integral_constant<int, I>{}); static_for<I + 1, N>(f); } }
template <int N> DEV void wait_vm() { asm volatile("s_waitcnt vmcnt(%0)" :: "n"(N) : "memory"); }
template <int N> DEV void wait_vm_bar() { asm volatile("s_waitcnt vmcnt(%0)\n\ts_barrier" :: "n"(N) : "memory"); }
template <int N> DEV void wait_vm_bar_pin(bf16x8& a) { asm volatile("s_waitcnt vmcnt(%1)\n\ts_barrier" : "+v"(a) : "n"(N) : "memory"); }

constexpr int P2_NF = 12, P2_NS = 17, P2_AD = 6;
constexpr int p2_ops_at(int g) { return (2 * g + 6 < 2 * P2_NS ? 3 : 0) + ((g + 2 < P2_NS && (g + 2 < P2_NF || g >= P2_NF)) ? 2 : 0); }
constexpr int p2_allowed(int g, int hw) {
    if (g == 0) return 2 + hw;
    if (g == 1) return hw + p2_ops_at(0);
    if (g == P2_NF) return 2 + (hw ? 1 : 0);
    if (g == P2_NF + 1) return (hw ? 1 : 0) + p2_ops_at(P2_NF);
    return p2_ops_at(g - 1);
}

constexpr int f1_allowed1(int t, bool lower) {
    int kind[160] = {}, idx[160] = {}, n = 0;
    auto X = [&](int k) { kind[n] = 0; idx[n] = k; ++n; };
    auto W = [&](int k) { const int c = (((k & 1) == 0) == lower) ? 2 : 1; for (int i = 0; i < c; ++i) { kind[n] = 1; idx[n] = k; ++n; } };
    X(0); X(1); W(0); W(1); X(2); X(3); W(2); X(4); X(5); W(3); W(4);
    for (int u = 0; u < t; ++u) { if (u + 5 < 20) W(u + 5); if (2 * u + 7 < 40) { X(2 * u + 6); X(2 * u + 7); } }
    int last = -1;
    for (int i = 0; i < n; ++i) {
        const bool need = t < 0 ? (kind[i] == 1 && idx[i] == 0) : ((kind[i] == 0 && idx[i] <= 2 * t + 1) || (kind[i] == 1 && idx[i] <= t + 1));
        if (need) last = i;
    }
    return n - 1 - last;
}
constexpr int f1_allowed(int t) { return f1_allowed1(t, true) < f1_allowed1(t, false) ? f1_allowed1(t, true) : f1_allowed1(t, false); }

DEV void g_role(const char* x, const char* wg, const char* wu, const char* wd, const char* wo, char* ws, char* out, int item, lds_ptr lds) {
    constexpr int SK = 1;
    const int tid = threadIdx.x, lane = tid & 63, w = __builtin_amdgcn_readfirstlane(tid >> 6);
    const int r = lane & 31, h = lane >> 5;
    const int xcd = item & 7, j = item >> 3;
    const unsigned l0 = (unsigned)(uintptr_t)lds;
    STAMP(0);
    __builtin_amdgcn_s_setprio(G_PRIO);
    {
        const int tq = w >> 1, fh = w & 1;
        const int mt = (xcd & 1) * 8 + (j & 7), ft = (xcd >> 1) * 4 + (j >> 3);
        const int m0 = mt * 128, f0 = ft * 96;
        const i32x4 sxf = make_srd(ws + WS_XF), sg = make_srd(wg), su = make_srd(wu);
        unsigned vw2[2], vw1;
        {
#pragma unroll
            for (int i = 0; i < 2; ++i) { const int p = 2 * (w & 3) + i, L = (p % 6) * 64 + lane, row = L / 12, c16 = L - row * 12; vw2[i] = (unsigned)row * 3072u + (unsigned)((p < 6 ? c16 : (c16 ^ 2)) * 16); }
            const int p = 8 + (w & 3), L = (p % 6) * 64 + lane, row = L / 12, c16 = L - row * 12; vw1 = (unsigned)row * 3072u + (unsigned)((c16 ^ 2) * 16);
        }
        const unsigned swb = (unsigned)f0 * 2u, sxb = (unsigned)((mt * 4 + tq) * 40) * 1024u, va = lane * 16;
        auto issue_w = [&](int t) {
            const unsigned lb = l0 + (unsigned)(t % 6) * 12288u;
            const unsigned so = swb + (unsigned)t * (32u * 3072u);
            if (((t & 1) == 0) == (w < 4)) {
                const int p = 2 * (w & 3);
                if (p < 6) { dma16(sg, vw2[0], so, lb + (unsigned)p * 1024u); dma16(sg, vw2[1], so, lb + (unsigned)(p + 1) * 1024u); }
                else { dma16(su, vw2[0], so, lb + (unsigned)p * 1024u); dma16(su, vw2[1], so, lb + (unsigned)(p + 1) * 1024u); }
            } else {
                dma16(su, vw1, so, lb + (unsigned)(8 + (w & 3)) * 1024u);
            }
        };
        lds_cptr wa[3];
        {
            const int g = lane >> 4, sel = g & 1, q = (lane & 15) >> 2, p = lane & 3;
#pragma unroll
            for (int a = 0; a < 3; ++a) {
                const int aa = fh * 3 + a, c16 = 2 * (sel ? (aa ^ 1) : aa) + (p >> 1);
                wa[a] = (lds_cptr)lds + sel * 6144 + (8 * h + q) * 192 + c16 * 16 + 8 * (p & 1);
            }
        }
        f32x16 acc[3];
#pragma unroll
        for (int a = 0; a < 3; ++a) acc[a] = f32x16{};
        auto run1 = [&](auto upc) {
            constexpr bool UP = decltype(upc)::value;
            bf16x8 xf[8], wf[2][3];
            auto issue_x = [&](int ks) { bufld16(xf[ks & 7], sxf, va, sxb + (unsigned)ks * 1024u); };
            auto ldw = [&](int buf, int t, int s) {
#pragma unroll
                for (int a = 0; a < 3; ++a) wf[buf][a] = cat8(lds_tr(wa[a] + (t % 6) * 12288 + s * 3072), lds_tr(wa[a] + (t % 6) * 12288 + s * 3072 + 768));
            };
            issue_x(0); issue_x(1); issue_w(0); issue_w(1);
            issue_x(2); issue_x(3); issue_w(2);
            issue_x(4); issue_x(5); issue_w(3); issue_w(4);
            wait_vm_bar<f1_allowed(-1)>();
            ldw(0, 0, 0);
            auto stage = [&](auto tc) {
                constexpr int t = decltype(tc)::value;
                asm volatile("s_waitcnt vmcnt(%2) lgkmcnt(0)\n\ts_barrier" : "+v"(xf[(2 * t) & 7]), "+v"(xf[(2 * t + 1) & 7]) : "n"(f1_allowed(t)) : "memory");
                auto feed = [&]() {
                    if constexpr (t + 5 < 20) issue_w(t + 5);
                    if constexpr (2 * t + 7 < 40) { issue_x(2 * t + 6); issue_x(2 * t + 7); }
                };
                if constexpr (UP) feed();
                ldw(1, t, 1);
                SBAR();
#pragma unroll
                for (int a = 0; a < 3; ++a) acc[a] = mfma16(wf[0][a], xf[(2 * t) & 7], acc[a]);
                SBAR();
                if constexpr (t < 19) ldw(0, t + 1, 0);
                SBAR();
#pragma unroll
                for (int a = 0; a < 3; ++a) acc[a] = mfma16(wf[1][a], xf[(2 * t + 1) & 7], acc[a]);
                SBAR();
                if constexpr (!UP) feed();
                SBAR();
            };
            static_for<0, 20>(stage);
        };
        if (w >= 4) run1(std::true_type{}); else run1(std::false_type{});
        STAMP(1);
        const __amdgpu_buffer_rsrc_t rh = make_rsrc(ws + WS_HF);
#pragma unroll
        for (int a = 0; a < 3; ++a) {
            f32x16 hv;
#pragma unroll
            for (int i = 0; i < 8; ++i) hv[i] = gelu_tanh(acc[a][i]) * acc[a][8 + i];
            const int tile = mt * 4 + tq, fs = ft * 6 + fh * 3 + a;
            st16_sc1(rh, (unsigned)((tile * 96 + fs) * 1024 + lane * 16), __builtin_bit_cast(u32x4, pack8(hv, 0)));
        }
        publish(ws, mt);
        STAMP(2);
    }
    const int reg = j >> 2, sub = j & 3, kg = w & 3;
    const bool up = w >= 4;
    const int rgp = (xcd & 3) * 4 + (reg & 3), cgp = (xcd >> 2) * 2 + (reg >> 2);
    const int rp = sub >> 1, cp = sub & 1;
    const int t0 = rgp * 4 + 2 * rp + cp, t1 = rgp * 4 + 2 * rp + (cp ^ 1);
    const int c0 = cgp * 128 + cp * 64, c4 = 512 + cgp * 32;
    const int tme = up ? t1 : t0;
    const unsigned lw = l0 + (unsigned)kg * 18432u;
    const i32x4 swd = make_srd(wd), swo = make_srd(wo), sh = make_srd(ws + WS_HF), so_ = make_srd(ws + WS_OF);
    const unsigned vw = (unsigned)(lane >> 2) * 1280u + (unsigned)(lane & 3) * 16u;
    const int rho = lane >> 2;
    const unsigned vwr = (unsigned)(20 * (2 * ((rho >> 2) & 1) + (rho >> 3)) + 16 + (rho & 3)) * 1280u + (unsigned)(lane & 3) * 16u;
    const unsigned va = lane * 16;
    const int upi = up ? 1 : 0;
    f32x16 acc[3];
#pragma unroll
    for (int i = 0; i < 3; ++i) acc[i] = f32x16{};
    auto issue_w = [&](int s) {
        const unsigned lb = lw + (unsigned)(s % 6) * 3072u;
        if (s < 2 * P2_NF) {
            const unsigned so = (unsigned)((kg * 24 + s) * 16) * 1280u;
            dma16(swd, vw, so + (unsigned)c0 * 2u, lb); dma16(swd, vw, so + (unsigned)c0 * 2u + 64u, lb + 1024u); dma16(swd, vw, so + (unsigned)c4 * 2u, lb + 2048u);
        } else {
            const int ks = kg * 10 + s - 2 * P2_NF;
            const unsigned so = ks < 32 ? (unsigned)(ks * 20) * 1280u : (unsigned)((ks - 32) * 80) * 1280u;
            const unsigned vv = ks < 32 ? vw : vwr;
            dma16(swo, vv, so + (unsigned)c0 * 2u, lb); dma16(swo, vv, so + (unsigned)c0 * 2u + 64u, lb + 1024u); dma16(swo, vv, so + (unsigned)c4 * 2u, lb + 2048u);
        }
    };
    if (up) { issue_w(0); issue_w(2); issue_w(4); } else { issue_w(1); issue_w(3); }
    lds_cptr wa;
    {
        const int g = lane >> 4, nh = g & 1, q = (lane & 15) >> 2, p = lane & 3;
        wa = (lds_cptr)lds + kg * 18432 + (4 * h + q) * 64 + nh * 32 + p * 8;
    }
    const float gm = 1.f - ((const float*)(ws + WS_G))[tme * 32 + r];
    asm volatile("" :: "v"(gm));
    consume(ws, rgp, 16);
    STAMP(3);
    auto run = [&](auto upc) {
        constexpr bool UP = decltype(upc)::value;
        bf16x8 af[P2_AD];
        auto issue_a = [&](int g) {
#pragma unroll
            for (int i = 0; i < 2; ++i) {
                const int s = 2 * g + i;
                if (g < P2_NF) bufld16(af[s % P2_AD], sh, va, (unsigned)(tme * 96 + kg * 24 + s) * 1024u);
                else bufld16(af[s % P2_AD], so_, va, (unsigned)(tme * 40 + kg * 10 + s - 2 * P2_NF) * 1024u);
            }
        };
        bf16x8 wf[2][3];
        auto ldw = [&](int s) {
            lds_cptr p = wa + (s % 6) * 3072;
            wf[s & 1][0] = cat8(lds_tr(p), lds_tr(p + 512));
            wf[s & 1][1] = cat8(lds_tr(p + 1024), lds_tr(p + 1536));
            if constexpr (!UP) wf[s & 1][2] = cat8(lds_tr(p + 2048), lds_tr(p + 2560));
        };
        auto mma = [&](int s) {
            acc[0] = mfma16(wf[s & 1][0], af[s % P2_AD], acc[0]); acc[1] = mfma16(wf[s & 1][1], af[s % P2_AD], acc[1]);
            if constexpr (!UP) acc[2] = mfma16(wf[s & 1][2], af[s % P2_AD], acc[2]);
        };
        issue_a(0); issue_a(1);
        wait_vm_bar<4>();
        ldw(0);
        auto stage = [&](auto gc) {
            constexpr int g = decltype(gc)::value;
            if constexpr (g == P2_NF) {
                STAMP(4);
                consume(ws, 16 + (rgp >> 1), 32);
                STAMP(5);
                issue_a(P2_NF); issue_a(P2_NF + 1);
            }
            asm volatile("s_waitcnt vmcnt(%2) lgkmcnt(0)\n\ts_barrier" : "+v"(af[(2 * g) % P2_AD]), "+v"(af[(2 * g + 1) % P2_AD]) : "n"(p2_allowed(g, 0)) : "memory");
            auto feed = [&]() {
                if constexpr (2 * g + 5 + (UP ? 1 : 0) < 2 * P2_NS) issue_w(2 * g + 5 + (UP ? 1 : 0));
                if constexpr (g + 2 < P2_NS && (g + 2 < P2_NF || g >= P2_NF)) issue_a(g + 2);
            };
            if constexpr (UP) feed();
            ldw(2 * g + 1);
            SBAR();
            mma(2 * g);
            SBAR();
            if constexpr (g + 1 < P2_NS) ldw(2 * g + 2);
            SBAR();
            mma(2 * g + 1);
            SBAR();
            if constexpr (!UP) feed();
            if constexpr (g == P2_NF - 1) {
#pragma unroll
                for (int i = 0; i < 16; ++i) { acc[0][i] *= gm; acc[1][i] *= gm; acc[2][i] *= gm; }
            }
            SBAR();
        };
        static_for<0, P2_NS>(stage);
    };
    if (up) run(std::true_type{}); else run(std::false_type{});
    STAMP(6);
    typedef __attribute__((ext_vector_type(4))) float f32x4;
    typedef __attribute__((address_space(3))) f32x4* lds_f4;
    __syncthreads();
    {
        lds_ptr mine = lds + kg * 20480 + r * 128;
#pragma unroll
        for (int i = 0; i < 3; ++i) {
            if (i == 2 && up) break;
            const int tl = up ? 2 + i : (i == 2 ? 4 : i);
#pragma unroll
            for (int g = 0; g < 4; ++g) {
                f32x4 v = {acc[i][4 * g], acc[i][4 * g + 1], acc[i][4 * g + 2], acc[i][4 * g + 3]};
                *(lds_f4)(mine + tl * 4096 + (((2 * g + h) ^ (r & 7)) * 16)) = v;
            }
        }
    }
    __syncthreads();
#pragma unroll
    for (int it = 0; it < 2; ++it) {
        const int u = tid + 512 * it;
        if (u < 640) {
            const int tl = u >> 7, tok = (u & 127) >> 2, ch = u & 3;
            f32x4 lo = {0, 0, 0, 0}, hi = {0, 0, 0, 0};
#pragma unroll
            for (int ww = 0; ww < 4; ++ww) {
                lds_ptr p = lds + ww * 20480 + tl * 4096 + tok * 128;
                lo += *(lds_f4)(p + (((2 * ch) ^ (tok & 7)) * 16));
                hi += *(lds_f4)(p + (((2 * ch + 1) ^ (tok & 7)) * 16));
            }
            const int tt = (tl == 2 || tl == 3) ? t1 : t0, cc = tl == 4 ? c4 : c0 + (tl & 1) * 32;
            u32x4 o = {pk2(lo[0], lo[1]), pk2(lo[2], lo[3]), pk2(hi[0], hi[1]), pk2(hi[2], hi[3])};
            *(u32x4*)(out + (size_t)(tt * 32 + tok) * 1280 + (cc + ch * 8) * 2) = o;
        }
    }
    STAMP(7);
}

__global__ __launch_bounds__(512, 4) void k2_kernel(const char* __restrict__ x, const char* __restrict__ wg, const char* __restrict__ wu, const char* __restrict__ wd,
                                                   const char* __restrict__ wo, char* __restrict__ ws, char* __restrict__ out) {
    extern __shared__ __attribute__((aligned(16))) char lds_raw[];
    lds_ptr lds = (lds_ptr)lds_raw;
    if (blockIdx.x < 256) attn_role(ws, blockIdx.x, lds);
    else g_role(x, wg, wu, wd, wo, ws, out, blockIdx.x - 256, lds);
}

extern "C" void kernel_launch(void* const* d_in, const int* in_sizes, int n_in, void* d_out, int out_size, void* d_ws, size_t ws_size, hipStream_t stream) {
    const char* ax = (const char*)d_in[0];
    const char* wqkv = (const char*)d_in[1];
    const char* wo = (const char*)d_in[2];
    const char* wg = (const char*)d_in[4];
    const char* wu = (const char*)d_in[5];
    const char* wd = (const char*)d_in[6];
    const char* gw = (const char*)d_in[7];
    char* ws = (char*)d_ws;
    (void)hipFuncSetAttribute((const void*)k1_kernel, hipFuncAttributeMaxDynamicSharedMemorySize, K1_LDS);
    (void)hipFuncSetAttribute((const void*)k2_kernel, hipFuncAttributeMaxDynamicSharedMemorySize, 81920);
    k1_kernel<<<256, 512, K1_LDS, stream>>>(ax, wqkv, gw, wg, wu, ws);
    k2_kernel<<<512, 512, 81920, stream>>>(ax, wg, wu, wd, wo, ws, (char*)d_out);
}
```

```cpp
#define A_PRIO 0
#define G_PRIO 2
#define O_SC1 1
#include <hip/hip_runtime.h>
#include <hip/hip_bf16.h>
#include <stdint.h>
#include <type_traits>

typedef __attribute__((ext_vector_type(8))) __bf16 bf16x8;
typedef __attribute__((ext_vector_type(8))) short s16x8;
typedef __attribute__((ext_vector_type(4))) short s16x4;
typedef __attribute__((ext_vector_type(2))) __bf16 bf16x2;
typedef __attribute__((ext_vector_type(2))) float f32x2;
typedef __attribute__((ext_vector_type(16))) float f32x16;
typedef __attribute__((ext_vector_type(4))) unsigned u32x4;
typedef __attribute__((ext_vector_type(2))) unsigned u32x2;
typedef __attribute__((ext_vector_type(4))) int i32x4;
typedef __attribute__((address_space(3))) const char* lds_cptr;
typedef __attribute__((address_space(3))) char* lds_ptr;

#define DEV __device__ __forceinline__
#define RL_AT 2
#define RL_DO 6
#define WARM_POL ""
#define WARM_AT 1

constexpr int T_ = 2048, D_ = 640, NH = 32, DH = 20, F_ = 1536;
constexpr float QSCALE = 0.22360679774997896f * 1.4426950408889634f;

constexpr size_t WS_QF = 0;
constexpr size_t WS_KV = WS_QF + (size_t)NH * 64 * 1280;
constexpr size_t WS_HF = WS_KV + (size_t)NH * 64 * 3072;
constexpr size_t WS_OF = WS_HF + (size_t)64 * 96 * 1024;
constexpr size_t WS_XF = WS_OF + (size_t)64 * 40 * 1024;
constexpr size_t WS_WF = WS_XF + (size_t)64 * 40 * 1024;
constexpr int WF_WD = 3840, WF_WO = 5760, WF_N = 6560;
constexpr size_t WS_G  = WS_WF + (size_t)WF_N * 1024;
constexpr size_t WS_FLAG = WS_G + 2048 * 4;
constexpr int FLAG_LINES = 32;
constexpr size_t WS_END = WS_FLAG + FLAG_LINES * 256;

DEV i32x4 make_srd(const void* p) {
    i32x4 s;
    s[0] = __builtin_amdgcn_readfirstlane((int)(uint32_t)(uintptr_t)p);
    s[1] = __builtin_amdgcn_readfirstlane((int)(uint32_t)((uintptr_t)p >> 32)) & 0xffff;
    s[2] = 0x7fffffff;
    s[3] = 0x00020000;
    return s;
}
#define RFL(x) (x)
#define PADS
DEV void dma16(const i32x4& srd, unsigned voff, unsigned soff, unsigned ldsaddr) {
    asm volatile(PADS "s_mov_b32 m0, %3\n\ts_nop 0\n\tbuffer_load_dwordx4 %0, %1, %2 offen lds"
                 :: "v"(voff), "s"(srd), "s"(RFL(soff)), "s"(RFL(ldsaddr)) : "memory");
}
DEV void warm(unsigned& sink, const char* base, unsigned voff) {
    asm volatile("global_load_dword %0, %1, %2" : "=v"(sink) : "v"(voff), "s"(base) : "memory");
}
#define WAIT_VM(n) asm volatile("s_waitcnt vmcnt(" #n ")" ::: "memory")
#define WAIT_VM_BAR(n) asm volatile("s_waitcnt vmcnt(" #n ")\n\ts_barrier" ::: "memory")
#define SBAR() __builtin_amdgcn_sched_barrier(0)

DEV f32x16 mfma16(bf16x8 a, bf16x8 b, f32x16 c) { return __builtin_amdgcn_mfma_f32_32x32x16_bf16(a, b, c, 0, 0, 0); }
DEV f32x16 mfma8(s16x4 a, s16x4 b, f32x16 c) { return __builtin_amdgcn_mfma_f32_32x32x8bf16_1k(a, b, c, 0, 0, 0); }
DEV unsigned pk2(float lo, float hi) {
    f32x2 v = {lo, hi};
    bf16x2 r = __builtin_convertvector(v, bf16x2);
    return __builtin_bit_cast(unsigned, r);
}
DEV bf16x8 lds_ld128(lds_cptr p) { return *(const __attribute__((address_space(3))) bf16x8*)p; }
DEV s16x4 lds_ld64(lds_cptr p) { return *(const __attribute__((address_space(3))) s16x4*)p; }
DEV s16x4 lds_tr(lds_cptr p) { return __builtin_amdgcn_ds_read_tr16_b64_v4i16((__attribute__((address_space(3))) s16x4*)p); }
DEV bf16x8 cat8(s16x4 lo, s16x4 hi) {
    s16x8 r = __builtin_shufflevector(lo, hi, 0, 1, 2, 3, 4, 5, 6, 7);
    return __builtin_bit_cast(bf16x8, r);
}
DEV bf16x8 pack8(const f32x16& v, int base) {
    u32x4 r = {pk2(v[base], v[base + 1]), pk2(v[base + 2], v[base + 3]), pk2(v[base + 4], v[base + 5]), pk2(v[base + 6], v[base + 7])};
    return __builtin_bit_cast(bf16x8, r);
}

typedef __attribute__((address_space(1))) unsigned gu32;
#define RLX_AGENT __ATOMIC_RELAXED, __HIP_MEMORY_SCOPE_AGENT
DEV __amdgpu_buffer_rsrc_t make_rsrc(void* p) { return __builtin_amdgcn_make_buffer_rsrc(p, 0, 0x7fffffff, 0x00020000); }
DEV void st16_sc1(__amdgpu_buffer_rsrc_t rs, unsigned voff, u32x4 v) { __builtin_amdgcn_raw_buffer_store_b128(v, rs, voff, 0, 16); }
DEV void st8_sc1(__amdgpu_buffer_rsrc_t rs, unsigned voff, u32x2 v) { __builtin_amdgcn_raw_buffer_store_b64(v, rs, voff, 0, 16); }
#define K1_WT_AUX 16
DEV void st16_pol(__amdgpu_buffer_rsrc_t rs, unsigned voff, u32x4 v) { __builtin_amdgcn_raw_buffer_store_b128(v, rs, voff, 0, K1_WT_AUX); }
DEV void st8_pol(__amdgpu_buffer_rsrc_t rs, unsigned voff, u32x2 v) { __builtin_amdgcn_raw_buffer_store_b64(v, rs, voff, 0, K1_WT_AUX); }
DEV void publish(char* ws, int line) {
    asm volatile("s_waitcnt vmcnt(0)\n\ts_barrier" ::: "memory");
    if (threadIdx.x == 0) __hip_atomic_fetch_add((gu32*)(ws + WS_FLAG + line * 256), 1u, RLX_AGENT);
}
template <bool ACQ = true>
DEV void consume(char* ws, int line, unsigned target) {
    if (threadIdx.x < 64) {
        gu32* f = (gu32*)(ws + WS_FLAG + line * 256);
        for (unsigned spins = 0; spins < (1u << 20); ++spins) {
            if ((unsigned)__builtin_amdgcn_readfirstlane(__hip_atomic_load(f, RLX_AGENT)) >= target) break;
            __builtin_amdgcn_s_sleep(2);
        }
        if constexpr (ACQ) {
            __builtin_amdgcn_fence(__ATOMIC_ACQUIRE, "agent");
            asm volatile("s_waitcnt vmcnt(0)" ::: "memory");
        }
    }
    asm volatile("s_barrier" ::: "memory");
}

#define STAMP(i) do {} while (0)
constexpr int K1_STAGE = 32768, K1_LDS = 4 * K1_STAGE + 4 * 8192;

template <bool VMODE>
DEV void k1_gemm(const char* x, const char* wqkv, const char* wg, const char* wu, const char* wd, const char* wo, char* ws, int mt, int nt, lds_ptr lds) {
    constexpr int SK = 0;
    const int tid = threadIdx.x, lane = tid & 63, w8 = __builtin_amdgcn_readfirstlane(tid >> 6), w = w8 & 3;
    const int wt = w >> 1, ww = w & 1, r = lane & 31, h = lane >> 5;
    const int m0 = mt * 128, n0 = nt * 128;
    STAMP(0);
    const int gw = (nt * 16 + mt) * 4 + w;
    const unsigned wl = (unsigned)(uintptr_t)lds + 4 * K1_STAGE + (unsigned)w * 8192u;
    auto unit_of = [&](int i) -> int {
        return gw < 480 ? 2 * gw + (i >> 2) : min(4 * (gw - 480) + (i >> 1), 1359);
    };
    auto relayout_issue = [&](int i0, int i1) {
        const int row8 = lane >> 3, c16 = lane & 7;
#pragma unroll
        for (int i = i0; i < i1; ++i) {
            const int u = unit_of(i);
            const char* src;
            if (gw < 480) {
                const int ks = u / 24, fq = u - ks * 24;
                src = ((i & 2) ? wu : wg) + (size_t)(16 * ks + 8 * (i & 1) + row8) * 3072 + fq * 128 + c16 * 16;
            } else {
                const int row = 8 * (i & 1) + row8;
                if (u < 960) { const int ks = u / 10, cq = u - ks * 10; src = wd + (size_t)(16 * ks + row) * 1280 + cq * 128 + c16 * 16; }
                else { const int q = u - 960, ks = q / 10, cq = q - ks * 10;
                       const int k = ks < 32 ? 20 * ks + row : 20 * (4 * (ks - 32) + 2 * ((row >> 2) & 1) + (row >> 3)) + 16 + (row & 3);
                       src = wo + (size_t)k * 1280 + cq * 128 + c16 * 16; }
            }
            asm volatile("s_mov_b32 m0, %1\n\ts_nop 0\n\tglobal_load_lds_dwordx4 %0, off" :: "v"(src), "s"(wl + (unsigned)i * 1024u) : "memory");
        }
    };
    if (w8 >= 4) {
        const i32x4 sx = make_srd(x), sw = make_srd(wqkv);
        const unsigned l0 = (unsigned)(uintptr_t)lds;
        const unsigned xq = lane >> 4;
        const unsigned vx0 = (unsigned)(lane >> 3) * 1280u + (unsigned)(((lane & 7) ^ xq) * 16);
        const unsigned vx1 = (unsigned)(lane >> 3) * 1280u + (unsigned)(((lane & 7) ^ xq ^ 4) * 16);
        const unsigned vw = (unsigned)(lane >> 4) * 3840u + (unsigned)(((lane & 15) ^ ((lane >> 4) << 2)) * 16);
        const unsigned sxb = (unsigned)(m0 + 32 * w) * 1280u, swb = (unsigned)(16 * w) * 3840u + (unsigned)n0 * 2u;
        auto issue = [&](int t) {
            const unsigned lb = l0 + (unsigned)(t & 3) * K1_STAGE;
#pragma unroll
            for (int j = 0; j < 4; ++j) {
                dma16(sx, (j & 1) ? vx1 : vx0, sxb + (unsigned)j * 10240u + (unsigned)t * 128u, lb + (unsigned)(4 * w + j) * 1024u);
                dma16(sw, vw, swb + (unsigned)(t * 64 + 4 * j) * 3840u, lb + 16384u + (unsigned)(4 * w + j) * 1024u);
            }
        };
        auto relayout_do = [&]() {
            const int g = lane >> 4, nh = g & 1, q = (lane & 15) >> 2, p = lane & 3;
            lds_cptr base = (lds_cptr)lds + 4 * K1_STAGE + w * 8192;
            const __amdgpu_buffer_rsrc_t rw = make_rsrc(ws + WS_WF);
            bf16x8 v[8];
            if (gw < 480) {
#pragma unroll
                for (int i = 0; i < 8; ++i) { lds_cptr pp = base + (i >> 2) * 4096 + nh * 2048 + (8 * h + q) * 128 + (i & 3) * 32 + p * 8; v[i] = cat8(lds_tr(pp), lds_tr(pp + 512)); }
#pragma unroll
                for (int i = 0; i < 8; ++i) { const int u = unit_of(i), ks = u / 24, fq = u - ks * 24; st16_sc1(rw, (unsigned)(ks * 96 + fq * 4 + (i & 3)) * 1024u + (unsigned)lane * 16u, __builtin_bit_cast(u32x4, v[i])); }
            } else {
#pragma unroll
                for (int i = 0; i < 8; ++i) { lds_cptr pp = base + (i >> 1) * 2048 + (4 * h + q) * 128 + (i & 1) * 64 + nh * 32 + p * 8; v[i] = cat8(lds_tr(pp), lds_tr(pp + 1024)); }
#pragma unroll
                for (int i = 0; i < 8; ++i) { const int u = unit_of(i); st16_sc1(rw, (unsigned)(WF_WD + 2 * u + (i & 1)) * 1024u + (unsigned)lane * 16u, __builtin_bit_cast(u32x4, v[i])); }
            }
        };
        issue(0); issue(1); issue(2);
        WAIT_VM_BAR(16);
        WAIT_VM_BAR(8);  issue(3);
        WAIT_VM_BAR(8);  issue(4);
        WAIT_VM_BAR(8);  issue(5);
        WAIT_VM_BAR(8);  issue(6);
        WAIT_VM_BAR(8);  issue(7);
        WAIT_VM_BAR(8);  issue(8);
        WAIT_VM_BAR(8);  issue(9);
        WAIT_VM_BAR(8);
        WAIT_VM_BAR(0);
        if (gw < 820) relayout_do();
        return;
    }
    lds_cptr xa[4], wa[2];
#pragma unroll
    for (int s = 0; s < 4; ++s) xa[s] = (lds_cptr)lds + (wt * 64 + r) * 128 + (((2 * s + h) ^ ((r >> 1) & 7)) * 16);
    {
        const int g = lane >> 4, nh = g & 1, q = (lane & 15) >> 2, p = lane & 3;
#pragma unroll
        for (int ct = 0; ct < 2; ++ct) {
            const int slot = ww * 8 + ct * 4 + 2 * nh + (p >> 1);
            wa[ct] = (lds_cptr)lds + 16384 + (8 * h + q) * 256 + ((slot ^ (q << 2)) * 16) + 8 * (p & 1);
        }
    }
    f32x16 acc[2][2];
#pragma unroll
    for (int i = 0; i < 2; ++i)
#pragma unroll
        for (int j = 0; j < 2; ++j) acc[i][j] = f32x16{};
    bf16x8 xf[2][2], wf[2][2];
    auto ldfrag = [&](int buf, int t, int s) {
        const int so = (t & 3) * K1_STAGE;
#pragma unroll
        for (int i = 0; i < 2; ++i) {
            xf[buf][i] = lds_ld128(xa[s] + so + i * 4096);
            wf[buf][i] = cat8(lds_tr(wa[i] + so + s * 4096), lds_tr(wa[i] + so + s * 4096 + 1024));
        }
    };
    asm volatile("s_barrier" ::: "memory");
    STAMP(1);
    ldfrag(0, 0, 0);
#pragma unroll
    for (int t = 0; t < 10; ++t) {
        if (t == 8) WAIT_VM(0);
        if (t < 9) asm volatile("s_barrier" ::: "memory");
#pragma unroll
        for (int s = 0; s < 4; ++s) {
            const int cur = s & 1;
            if (s < 3) ldfrag(cur ^ 1, t, s + 1); else if (t < 9) ldfrag(cur ^ 1, t + 1, 0);
            SBAR();
#pragma unroll
            for (int i = 0; i < 2; ++i)
#pragma unroll
                for (int j = 0; j < 2; ++j)
                    acc[i][j] = VMODE ? mfma16(xf[cur][i], wf[cur][j], acc[i][j]) : mfma16(wf[cur][i], xf[cur][j], acc[i][j]);
            if (t == RL_AT && gw < 820) relayout_issue(2 * s, 2 * s + 2);
            if ((4 * t + s) % 15 == nt) {
                bf16x8* xp = (bf16x8*)(ws + WS_XF + (size_t)((mt * 4 + wt * 2 + ww) * 40 + 4 * t + s) * 1024 + lane * 16);
                *xp = lds_ld128(xa[s] + (t & 3) * K1_STAGE + ww * 4096);
            }
            SBAR();
        }
    }
    STAMP(2);
    if constexpr (!VMODE) {
        const bool isq = nt < 5;
        const int csec = (isq ? n0 : n0 - 640) + ww * 64;
        char* base = ws + (isq ? WS_QF : WS_KV);
        const int tstride = isq ? 1280 : 3072;
        const float sc = isq ? QSCALE : 1.0f;
#pragma unroll
        for (int ct = 0; ct < 2; ++ct)
#pragma unroll
            for (int g = 0; g < 4; ++g) {
                const int qd = (csec + ct * 32 + 8 * g + 4 * h) >> 2, head = qd / 5, dq = qd - head * 5;
                const int inoff = dq < 4 ? (dq >> 1) * 512 + r * 16 + (dq & 1) * 8 : 1024 + r * 8;
#pragma unroll
                for (int tt = 0; tt < 2; ++tt) {
                    const int tile = mt * 4 + wt * 2 + tt;
                    char* p = base + (size_t)(head * 64 + tile) * tstride + inoff;
                    const f32x16& a = acc[ct][tt];
                    u32x2 v = {pk2(a[4 * g] * sc, a[4 * g + 1] * sc), pk2(a[4 * g + 2] * sc, a[4 * g + 3] * sc)};
                    *(u32x2*)p = v;
                    if (!isq && dq == 4) *(u32x2*)(p + 256) = (u32x2){0x00003f80u, 0u};
                }
            }
    } else {
#pragma unroll
        for (int ct = 0; ct < 2; ++ct) {
            const int col = n0 - 1280 + ww * 64 + ct * 32 + r, head = col / 20, d = col - head * 20;
#pragma unroll
            for (int tt = 0; tt < 2; ++tt) {
                const int tile = mt * 4 + wt * 2 + tt;
                char* p = ws + WS_KV + (size_t)(head * 64 + tile) * 3072 + 1536 + h * 384 + d * 16;
#pragma unroll
                for (int s = 0; s < 2; ++s) {
                    *(bf16x8*)(p + s * 768) = pack8(acc[tt][ct], 8 * s);
                    if (d == 0) *(u32x4*)(p + s * 768 + 320) = (u32x4){0x3f803f80u, 0x3f803f80u, 0x3f803f80u, 0x3f803f80u};
                    if (d == 1) *(u32x4*)(p + s * 768 + 320) = (u32x4){0u, 0u, 0u, 0u};
                }
            }
        }
    }
    STAMP(3);
}

DEV void k1_gate(const char* x, const char* gw, char* ws, int mt) {
    constexpr int SK = 2;
    STAMP(0);
    const int tid = threadIdx.x, lane = tid & 63, w = tid >> 6, rr = lane >> 3, ch = lane & 7;
    const char* xr = x + (size_t)(mt * 128 + w * 16 + rr) * 1280 + ch * 16;
    u32x4 xv[2][10], gv[10];
#pragma unroll
    for (int i = 0; i < 10; ++i) {
        xv[0][i] = *(const u32x4*)(xr + i * 128);
        xv[1][i] = *(const u32x4*)(xr + 8 * 1280 + i * 128);
        gv[i] = *(const u32x4*)(gw + i * 128 + ch * 16);
    }
    float z[2] = {0.f, 0.f};
#pragma unroll
    for (int i = 0; i < 10; ++i)
#pragma unroll
        for (int e = 0; e < 4; ++e) {
            const float g0 = __uint_as_float(gv[i][e] << 16), g1 = __uint_as_float(gv[i][e] & 0xffff0000u);
#pragma unroll
            for (int a = 0; a < 2; ++a)
                z[a] = __builtin_fmaf(__uint_as_float(xv[a][i][e] & 0xffff0000u), g1, __builtin_fmaf(__uint_as_float(xv[a][i][e] << 16), g0, z[a]));
        }
#pragma unroll
    for (int a = 0; a < 2; ++a) {
        z[a] += __shfl_xor(z[a], 1); z[a] += __shfl_xor(z[a], 2); z[a] += __shfl_xor(z[a], 4);
        if (ch == 0) ((float*)(ws + WS_G))[mt * 128 + w * 16 + a * 8 + rr] = 1.0f / (1.0f + __expf(-z[a]));
    }
    STAMP(1);
}

__global__ __launch_bounds__(512, 2) void k1_kernel(const char* __restrict__ x, const char* __restrict__ wqkv, const char* __restrict__ gw, const char* __restrict__ wg, const char* __restrict__ wu, const char* __restrict__ wd, const char* __restrict__ wo, char* __restrict__ ws) {
    extern __shared__ __attribute__((aligned(16))) char lds_raw[];
    lds_ptr lds = (lds_ptr)lds_raw;
    const int b = blockIdx.x, xcd = b & 7, j = b >> 3, rg = xcd & 3, cg = xcd >> 2;
    if (cg == 1 && j >= 28) {
        if (b == 255 && threadIdx.x < FLAG_LINES) *(unsigned*)(ws + WS_FLAG + threadIdx.x * 256) = 0u;
        k1_gate(x, gw, ws, rg * 4 + (j - 28));
        return;
    }
    const int mt = rg * 4 + (j & 3), nt = cg * 8 + (j >> 2);
    if (nt < 10) k1_gemm<false>(x, wqkv, wg, wu, wd, wo, ws, mt, nt, lds); else k1_gemm<true>(x, wqkv, wg, wu, wd, wo, ws, mt, nt, lds);
}

constexpr int AT_CHUNK = 24576;

DEV void attn_slow(const char* kvh, bf16x8 q1, s16x4 q2, int lane, f32x16& o) {
    const int r = lane & 31, h = lane >> 5;
    const int vrow = r < 21 ? r : 21;
    float m = -INFINITY;
    o = f32x16{};
    for (int t = 0; t < 64; ++t) {
        const char* p = kvh + t * 3072;
        const bf16x8 k1 = *(const bf16x8*)(p + lane * 16);
        const s16x4 k2 = *(const s16x4*)(p + 1024 + lane * 8);
        const bf16x8 v0 = *(const bf16x8*)(p + 1536 + h * 384 + vrow * 16);
        const bf16x8 v1 = *(const bf16x8*)(p + 2304 + h * 384 + vrow * 16);
        f32x16 s = mfma16(k1, q1, f32x16{});
        s = mfma8(k2, q2, s);
        float tm = s[0];
#pragma unroll
        for (int i = 1; i < 16; ++i) tm = fmaxf(tm, s[i]);
        tm = fmaxf(tm, __shfl_xor(tm, 32));
        const float mn = fmaxf(m, tm);
        const float alpha = __builtin_amdgcn_exp2f(m - mn);
        m = mn;
#pragma unroll
        for (int i = 0; i < 16; ++i) { s[i] = __builtin_amdgcn_exp2f(s[i] - mn); o[i] *= alpha; }
        o = mfma16(v0, pack8(s, 0), o);
        o = mfma16(v1, pack8(s, 8), o);
    }
}

#define ATT_LOOP_ASM \
    "ds_read_b128 v[72:75], %1\n\t" \
    "ds_read_b64 v[80:81], %2\n\t" \
    "ds_read_b128 v[76:79], %1 offset:3072\n\t" \
    "ds_read_b64 v[82:83], %2 offset:3072\n\t" \
    "ds_read_b128 v[84:87], %3\n\t" \
    "ds_read_b128 v[88:91], %3 offset:768\n\t" \
    "ds_read_b128 v[92:95], %3 offset:3072\n\t" \
    "ds_read_b128 v[96:99], %3 offset:3840\n\t" \
    "s_waitcnt lgkmcnt(6)\n\t" \
    "v_mfma_f32_32x32x16_bf16 v[32:47], v[72:75], %8, 0\n\t" \
    "v_mfma_f32_32x32x8_bf16 v[32:47], v[80:81], %9, v[32:47]\n\t" \
    "s_nop 15\n\t" \
    "1:\n\t" \
    "s_waitcnt vmcnt(3) lgkmcnt(0)\n\t" \
    "s_barrier\n\t" \
    "v_add_u32 %1, %6, %12\n\t" \
    "v_add_u32 %2, %6, %13\n\t" \
    "v_add_u32 %3, %6, %14\n\t" \
    "s_sub_u32 %5, %6, 0x6000\n\t" \
    "s_cmp_eq_u32 %6, 0\n\t" \
    "s_cselect_b32 %5, 0xc000, %5\n\t" \
    "s_add_u32 %5, %5, %15\n\t" \
    "s_setprio 3\n\t" \
    "v_mfma_f32_32x32x16_bf16 v[48:63], v[76:79], %8, 0\n\t" \
    "s_setprio 0\n\t" \
    "v_exp_f32 v32, v32\n\t" \
    "ds_read_b128 v[72:75], %1 offset:0\n\t" \
    "v_exp_f32 v33, v33\n\t" \
    "ds_read_b64 v[80:81], %2 offset:0\n\t" \
    "v_exp_f32 v34, v34\n\t" \
    "v_exp_f32 v35, v35\n\t" \
    "v_exp_f32 v36, v36\n\t" \
    "s_mov_b32 m0, %5\n\t" \
    "v_exp_f32 v37, v37\n\t" \
    "s_setprio 3\n\t" \
    "buffer_load_dwordx4 %10, %11, %4 offen offset:0 lds\n\t" \
    "s_setprio 0\n\t" \
    "v_exp_f32 v38, v38\n\t" \
    "v_exp_f32 v39, v39\n\t" \
    "s_setprio 3\n\t" \
    "v_mfma_f32_32x32x8_bf16 v[48:63], v[82:83], %9, v[48:63]\n\t" \
    "s_setprio 0\n\t" \
    "v_cvt_pk_bf16_f32 v64, v32, v33\n\t" \
    "v_cvt_pk_bf16_f32 v65, v34, v35\n\t" \
    "v_cvt_pk_bf16_f32 v66, v36, v37\n\t" \
    "v_cvt_pk_bf16_f32 v67, v38, v39\n\t" \
    "v_exp_f32 v40, v40\n\t" \
    "v_exp_f32 v41, v41\n\t" \
    "v_exp_f32 v42, v42\n\t" \
    "v_exp_f32 v43, v43\n\t" \
    "s_setprio 3\n\t" \
    "v_mfma_f32_32x32x16_bf16 %0, v[84:87], v[64:67], %0\n\t" \
    "s_setprio 0\n\t" \
    "ds_read_b128 v[84:87], %3 offset:0\n\t" \
    "v_exp_f32 v44, v44\n\t" \
    "v_exp_f32 v45, v45\n\t" \
    "v_exp_f32 v46, v46\n\t" \
    "v_exp_f32 v47, v47\n\t" \
    "v_cvt_pk_bf16_f32 v68, v40, v41\n\t" \
    "v_cvt_pk_bf16_f32 v69, v42, v43\n\t" \
    "v_cvt_pk_bf16_f32 v70, v44, v45\n\t" \
    "v_cvt_pk_bf16_f32 v71, v46, v47\n\t" \
    "s_waitcnt lgkmcnt(1)\n\t" \
    "s_setprio 3\n\t" \
    "v_mfma_f32_32x32x16_bf16 v[32:47], v[72:75], %8, 0\n\t" \
    "s_setprio 0\n\t" \
    "v_exp_f32 v48, v48\n\t" \
    "ds_read_b128 v[76:79], %1 offset:3072\n\t" \
    "v_exp_f32 v49, v49\n\t" \
    "ds_read_b64 v[82:83], %2 offset:3072\n\t" \
    "v_exp_f32 v50, v50\n\t" \
    "v_exp_f32 v51, v51\n\t" \
    "s_setprio 3\n\t" \
    "v_mfma_f32_32x32x16_bf16 %0, v[88:91], v[68:71], %0\n\t" \
    "s_setprio 0\n\t" \
    "ds_read_b128 v[88:91], %3 offset:768\n\t" \
    "v_exp_f32 v52, v52\n\t" \
    "v_exp_f32 v53, v53\n\t" \
    "s_setprio 3\n\t" \
    "buffer_load_dwordx4 %10, %11, %4 offen offset:1024 lds\n\t" \
    "s_setprio 0\n\t" \
    "v_exp_f32 v54, v54\n\t" \
    "v_exp_f32 v55, v55\n\t" \
    "s_setprio 3\n\t" \
    "v_mfma_f32_32x32x8_bf16 v[32:47], v[80:81], %9, v[32:47]\n\t" \
    "s_setprio 0\n\t" \
    "v_cvt_pk_bf16_f32 v64, v48, v49\n\t" \
    "v_cvt_pk_bf16_f32 v65, v50, v51\n\t" \
    "v_cvt_pk_bf16_f32 v66, v52, v53\n\t" \
    "v_cvt_pk_bf16_f32 v67, v54, v55\n\t" \
    "v_exp_f32 v56, v56\n\t" \
    "v_exp_f32 v57, v57\n\t" \
    "v_exp_f32 v58, v58\n\t" \
    "v_exp_f32 v59, v59\n\t" \
    "s_setprio 3\n\t" \
    "v_mfma_f32_32x32x16_bf16 %0, v[92:95], v[64:67], %0\n\t" \
    "s_setprio 0\n\t" \
    "ds_read_b128 v[92:95], %3 offset:3072\n\t" \
    "v_exp_f32 v60, v60\n\t" \
    "v_exp_f32 v61, v61\n\t" \
    "v_exp_f32 v62, v62\n\t" \
    "v_exp_f32 v63, v63\n\t" \
    "v_cvt_pk_bf16_f32 v68, v56, v57\n\t" \
    "v_cvt_pk_bf16_f32 v69, v58, v59\n\t" \
    "v_cvt_pk_bf16_f32 v70, v60, v61\n\t" \
    "v_cvt_pk_bf16_f32 v71, v62, v63\n\t" \
    "s_waitcnt lgkmcnt(1)\n\t" \
    "s_setprio 3\n\t" \
    "v_mfma_f32_32x32x16_bf16 v[48:63], v[76:79], %8, 0\n\t" \
    "s_setprio 0\n\t" \
    "v_exp_f32 v32, v32\n\t" \
    "ds_read_b128 v[72:75], %1 offset:6144\n\t" \
    "v_exp_f32 v33, v33\n\t" \
    "ds_read_b64 v[80:81], %2 offset:6144\n\t" \
    "v_exp_f32 v34, v34\n\t" \
    "v_exp_f32 v35, v35\n\t" \
    "s_setprio 3\n\t" \
    "v_mfma_f32_32x32x16_bf16 %0, v[96:99], v[68:71], %0\n\t" \
    "s_setprio 0\n\t" \
    "ds_read_b128 v[96:99], %3 offset:3840\n\t" \
    "v_exp_f32 v36, v36\n\t" \
    "v_exp_f32 v37, v37\n\t" \
    "s_setprio 3\n\t" \
    "buffer_load_dwordx4 %10, %11, %4 offen offset:2048 lds\n\t" \
    "s_setprio 0\n\t" \
    "v_exp_f32 v38, v38\n\t" \
    "v_exp_f32 v39, v39\n\t" \
    "s_setprio 3\n\t" \
    "v_mfma_f32_32x32x8_bf16 v[48:63], v[82:83], %9, v[48:63]\n\t" \
    "s_setprio 0\n\t" \
    "v_cvt_pk_bf16_f32 v64, v32, v33\n\t" \
    "v_cvt_pk_bf16_f32 v65, v34, v35\n\t" \
    "v_cvt_pk_bf16_f32 v66, v36, v37\n\t" \
    "v_cvt_pk_bf16_f32 v67, v38, v39\n\t" \
    "v_exp_f32 v40, v40\n\t" \
    "v_exp_f32 v41, v41\n\t" \
    "v_exp_f32 v42, v42\n\t" \
    "v_exp_f32 v43, v43\n\t" \
    "s_setprio 3\n\t" \
    "v_mfma_f32_32x32x16_bf16 %0, v[84:87], v[64:67], %0\n\t" \
    "s_setprio 0\n\t" \
    "ds_read_b128 v[84:87], %3 offset:6144\n\t" \
    "v_exp_f32 v44, v44\n\t" \
    "v_exp_f32 v45, v45\n\t" \
    "v_exp_f32 v46, v46\n\t" \
    "v_exp_f32 v47, v47\n\t" \
    "v_cvt_pk_bf16_f32 v68, v40, v41\n\t" \
    "v_cvt_pk_bf16_f32 v69, v42, v43\n\t" \
    "v_cvt_pk_bf16_f32 v70, v44, v45\n\t" \
    "v_cvt_pk_bf16_f32 v71, v46, v47\n\t" \
    "s_waitcnt lgkmcnt(1)\n\t" \
    "s_setprio 3\n\t" \
    "v_mfma_f32_32x32x16_bf16 v[32:47], v[72:75], %8, 0\n\t" \
    "s_setprio 0\n\t" \
    "v_exp_f32 v48, v48\n\t" \
    "ds_read_b128 v[76:79], %1 offset:9216\n\t" \
    "v_exp_f32 v49, v49\n\t" \
    "ds_read_b64 v[82:83], %2 offset:9216\n\t" \
    "v_exp_f32 v50, v50\n\t" \
    "v_exp_f32 v51, v51\n\t" \
    "s_setprio 3\n\t" \
    "v_mfma_f32_32x32x16_bf16 %0, v[88:91], v[68:71], %0\n\t" \
    "s_setprio 0\n\t" \
    "ds_read_b128 v[88:91], %3 offset:6912\n\t" \
    "v_exp_f32 v52, v52\n\t" \
    "v_exp_f32 v53, v53\n\t" \
    "v_exp_f32 v54, v54\n\t" \
    "v_exp_f32 v55, v55\n\t" \
    "s_setprio 3\n\t" \
    "v_mfma_f32_32x32x8_bf16 v[32:47], v[80:81], %9, v[32:47]\n\t" \
    "s_setprio 0\n\t" \
    "v_cvt_pk_bf16_f32 v64, v48, v49\n\t" \
    "v_cvt_pk_bf16_f32 v65, v50, v51\n\t" \
    "v_cvt_pk_bf16_f32 v66, v52, v53\n\t" \
    "v_cvt_pk_bf16_f32 v67, v54, v55\n\t" \
    "v_exp_f32 v56, v56\n\t" \
    "v_exp_f32 v57, v57\n\t" \
    "v_exp_f32 v58, v58\n\t" \
    "v_exp_f32 v59, v59\n\t" \
    "s_setprio 3\n\t" \
    "v_mfma_f32_32x32x16_bf16 %0, v[92:95], v[64:67], %0\n\t" \
    "s_setprio 0\n\t" \
    "ds_read_b128 v[92:95], %3 offset:9216\n\t" \
    "v_exp_f32 v60, v60\n\t" \
    "v_exp_f32 v61, v61\n\t" \
    "v_exp_f32 v62, v62\n\t" \
    "v_exp_f32 v63, v63\n\t" \
    "v_cvt_pk_bf16_f32 v68, v56, v57\n\t" \
    "v_cvt_pk_bf16_f32 v69, v58, v59\n\t" \
    "v_cvt_pk_bf16_f32 v70, v60, v61\n\t" \
    "v_cvt_pk_bf16_f32 v71, v62, v63\n\t" \
    "s_waitcnt lgkmcnt(1)\n\t" \
    "s_setprio 3\n\t" \
    "v_mfma_f32_32x32x16_bf16 v[48:63], v[76:79], %8, 0\n\t" \
    "s_setprio 0\n\t" \
    "v_exp_f32 v32, v32\n\t" \
    "ds_read_b128 v[72:75], %1 offset:12288\n\t" \
    "v_exp_f32 v33, v33\n\t" \
    "ds_read_b64 v[80:81], %2 offset:12288\n\t" \
    "v_exp_f32 v34, v34\n\t" \
    "v_exp_f32 v35, v35\n\t" \
    "s_setprio 3\n\t" \
    "v_mfma_f32_32x32x16_bf16 %0, v[96:99], v[68:71], %0\n\t" \
    "s_setprio 0\n\t" \
    "ds_read_b128 v[96:99], %3 offset:9984\n\t" \
    "v_exp_f32 v36, v36\n\t" \
    "v_exp_f32 v37, v37\n\t" \
    "v_exp_f32 v38, v38\n\t" \
    "v_exp_f32 v39, v39\n\t" \
    "s_setprio 3\n\t" \
    "v_mfma_f32_32x32x8_bf16 v[48:63], v[82:83], %9, v[48:63]\n\t" \
    "s_setprio 0\n\t" \
    "v_cvt_pk_bf16_f32 v64, v32, v33\n\t" \
    "v_cvt_pk_bf16_f32 v65, v34, v35\n\t" \
    "v_cvt_pk_bf16_f32 v66, v36, v37\n\t" \
    "v_cvt_pk_bf16_f32 v67, v38, v39\n\t" \
    "v_exp_f32 v40, v40\n\t" \
    "v_exp_f32 v41, v41\n\t" \
    "v_exp_f32 v42, v42\n\t" \
    "v_exp_f32 v43, v43\n\t" \
    "s_setprio 3\n\t" \
    "v_mfma_f32_32x32x16_bf16 %0, v[84:87], v[64:67], %0\n\t" \
    "s_setprio 0\n\t" \
    "ds_read_b128 v[84:87], %3 offset:12288\n\t" \
    "v_exp_f32 v44, v44\n\t" \
    "v_exp_f32 v45, v45\n\t" \
    "v_exp_f32 v46, v46\n\t" \
    "v_exp_f32 v47, v47\n\t" \
    "v_cvt_pk_bf16_f32 v68, v40, v41\n\t" \
    "v_cvt_pk_bf16_f32 v69, v42, v43\n\t" \
    "v_cvt_pk_bf16_f32 v70, v44, v45\n\t" \
    "v_cvt_pk_bf16_f32 v71, v46, v47\n\t" \
    "s_waitcnt lgkmcnt(1)\n\t" \
    "s_setprio 3\n\t" \
    "v_mfma_f32_32x32x16_bf16 v[32:47], v[72:75], %8, 0\n\t" \
    "s_setprio 0\n\t" \
    "v_exp_f32 v48, v48\n\t" \
    "ds_read_b128 v[76:79], %1 offset:15360\n\t" \
    "v_exp_f32 v49, v49\n\t" \
    "ds_read_b64 v[82:83], %2 offset:15360\n\t" \
    "v_exp_f32 v50, v50\n\t" \
    "v_exp_f32 v51, v51\n\t" \
    "s_setprio 3\n\t" \
    "v_mfma_f32_32x32x16_bf16 %0, v[88:91], v[68:71], %0\n\t" \
    "s_setprio 0\n\t" \
    "ds_read_b128 v[88:91], %3 offset:13056\n\t" \
    "v_exp_f32 v52, v52\n\t" \
    "v_exp_f32 v53, v53\n\t" \
    "v_exp_f32 v54, v54\n\t" \
    "v_exp_f32 v55, v55\n\t" \
    "s_setprio 3\n\t" \
    "v_mfma_f32_32x32x8_bf16 v[32:47], v[80:81], %9, v[32:47]\n\t" \
    "s_setprio 0\n\t" \
    "v_cvt_pk_bf16_f32 v64, v48, v49\n\t" \
    "v_cvt_pk_bf16_f32 v65, v50, v51\n\t" \
    "v_cvt_pk_bf16_f32 v66, v52, v53\n\t" \
    "v_cvt_pk_bf16_f32 v67, v54, v55\n\t" \
    "v_exp_f32 v56, v56\n\t" \
    "v_exp_f32 v57, v57\n\t" \
    "v_exp_f32 v58, v58\n\t" \
    "v_exp_f32 v59, v59\n\t" \
    "s_setprio 3\n\t" \
    "v_mfma_f32_32x32x16_bf16 %0, v[92:95], v[64:67], %0\n\t" \
    "s_setprio 0\n\t" \
    "ds_read_b128 v[92:95], %3 offset:15360\n\t" \
    "v_exp_f32 v60, v60\n\t" \
    "v_exp_f32 v61, v61\n\t" \
    "v_exp_f32 v62, v62\n\t" \
    "v_exp_f32 v63, v63\n\t" \
    "v_cvt_pk_bf16_f32 v68, v56, v57\n\t" \
    "v_cvt_pk_bf16_f32 v69, v58, v59\n\t" \
    "v_cvt_pk_bf16_f32 v70, v60, v61\n\t" \
    "v_cvt_pk_bf16_f32 v71, v62, v63\n\t" \
    "s_waitcnt lgkmcnt(1)\n\t" \
    "s_setprio 3\n\t" \
    "v_mfma_f32_32x32x16_bf16 v[48:63], v[76:79], %8, 0\n\t" \
    "s_setprio 0\n\t" \
    "v_exp_f32 v32, v32\n\t" \
    "ds_read_b128 v[72:75], %1 offset:18432\n\t" \
    "v_exp_f32 v33, v33\n\t" \
    "ds_read_b64 v[80:81], %2 offset:18432\n\t" \
    "v_exp_f32 v34, v34\n\t" \
    "v_exp_f32 v35, v35\n\t" \
    "s_setprio 3\n\t" \
    "v_mfma_f32_32x32x16_bf16 %0, v[96:99], v[68:71], %0\n\t" \
    "s_setprio 0\n\t" \
    "ds_read_b128 v[96:99], %3 offset:16128\n\t" \
    "v_exp_f32 v36, v36\n\t" \
    "v_exp_f32 v37, v37\n\t" \
    "v_exp_f32 v38, v38\n\t" \
    "v_exp_f32 v39, v39\n\t" \
    "s_setprio 3\n\t" \
    "v_mfma_f32_32x32x8_bf16 v[48:63], v[82:83], %9, v[48:63]\n\t" \
    "s_setprio 0\n\t" \
    "v_cvt_pk_bf16_f32 v64, v32, v33\n\t" \
    "v_cvt_pk_bf16_f32 v65, v34, v35\n\t" \
    "v_cvt_pk_bf16_f32 v66, v36, v37\n\t" \
    "v_cvt_pk_bf16_f32 v67, v38, v39\n\t" \
    "v_exp_f32 v40, v40\n\t" \
    "v_exp_f32 v41, v41\n\t" \
    "v_exp_f32 v42, v42\n\t" \
    "v_exp_f32 v43, v43\n\t" \
    "s_setprio 3\n\t" \
    "v_mfma_f32_32x32x16_bf16 %0, v[84:87], v[64:67], %0\n\t" \
    "s_setprio 0\n\t" \
    "ds_read_b128 v[84:87], %3 offset:18432\n\t" \
    "v_exp_f32 v44, v44\n\t" \
    "v_exp_f32 v45, v45\n\t" \
    "v_exp_f32 v46, v46\n\t" \
    "v_exp_f32 v47, v47\n\t" \
    "v_cvt_pk_bf16_f32 v68, v40, v41\n\t" \
    "v_cvt_pk_bf16_f32 v69, v42, v43\n\t" \
    "v_cvt_pk_bf16_f32 v70, v44, v45\n\t" \
    "v_cvt_pk_bf16_f32 v71, v46, v47\n\t" \
    "s_waitcnt lgkmcnt(1)\n\t" \
    "s_setprio 3\n\t" \
    "v_mfma_f32_32x32x16_bf16 v[32:47], v[72:75], %8, 0\n\t" \
    "s_setprio 0\n\t" \
    "v_exp_f32 v48, v48\n\t" \
    "ds_read_b128 v[76:79], %1 offset:21504\n\t" \
    "v_exp_f32 v49, v49\n\t" \
    "ds_read_b64 v[82:83], %2 offset:21504\n\t" \
    "v_exp_f32 v50, v50\n\t" \
    "v_exp_f32 v51, v51\n\t" \
    "s_setprio 3\n\t" \
    "v_mfma_f32_32x32x16_bf16 %0, v[88:91], v[68:71], %0\n\t" \
    "s_setprio 0\n\t" \
    "ds_read_b128 v[88:91], %3 offset:19200\n\t" \
    "v_exp_f32 v52, v52\n\t" \
    "v_exp_f32 v53, v53\n\t" \
    "v_exp_f32 v54, v54\n\t" \
    "v_exp_f32 v55, v55\n\t" \
    "s_setprio 3\n\t" \
    "v_mfma_f32_32x32x8_bf16 v[32:47], v[80:81], %9, v[32:47]\n\t" \
    "s_setprio 0\n\t" \
    "v_cvt_pk_bf16_f32 v64, v48, v49\n\t" \
    "v_cvt_pk_bf16_f32 v65, v50, v51\n\t" \
    "v_cvt_pk_bf16_f32 v66, v52, v53\n\t" \
    "v_cvt_pk_bf16_f32 v67, v54, v55\n\t" \
    "v_exp_f32 v56, v56\n\t" \
    "v_exp_f32 v57, v57\n\t" \
    "v_exp_f32 v58, v58\n\t" \
    "v_exp_f32 v59, v59\n\t" \
    "s_setprio 3\n\t" \
    "v_mfma_f32_32x32x16_bf16 %0, v[92:95], v[64:67], %0\n\t" \
    "s_setprio 0\n\t" \
    "ds_read_b128 v[92:95], %3 offset:21504\n\t" \
    "v_exp_f32 v60, v60\n\t" \
    "v_exp_f32 v61, v61\n\t" \
    "v_exp_f32 v62, v62\n\t" \
    "v_exp_f32 v63, v63\n\t" \
    "v_cvt_pk_bf16_f32 v68, v56, v57\n\t" \
    "v_cvt_pk_bf16_f32 v69, v58, v59\n\t" \
    "v_cvt_pk_bf16_f32 v70, v60, v61\n\t" \
    "v_cvt_pk_bf16_f32 v71, v62, v63\n\t" \
    "s_nop 1\n\t" \
    "s_setprio 3\n\t" \
    "v_mfma_f32_32x32x16_bf16 %0, v[96:99], v[68:71], %0\n\t" \
    "s_setprio 0\n\t" \
    "ds_read_b128 v[96:99], %3 offset:22272\n\t" \
    "s_add_u32 %4, %4, 0x6000\n\t" \
    "s_add_u32 %6, %6, 0x6000\n\t" \
    "s_cmp_eq_u32 %6, 0x12000\n\t" \
    "s_cselect_b32 %6, 0, %6\n\t" \
    "s_sub_u32 %7, %7, 1\n\t" \
    "s_cmp_lg_u32 %7, 0\n\t" \
    "s_cbranch_scc1 1b\n\t" \
    "s_waitcnt vmcnt(0) lgkmcnt(0)\n\t" \
    "s_nop 15"
#define ATT_CLOBBERS "v32", "v33", "v34", "v35", "v36", "v37", "v38", "v39", "v40", "v41", "v42", "v43", "v44", "v45", "v46", "v47", "v48", "v49", "v50", "v51", "v52", "v53", "v54", "v55", "v56", "v57", "v58", "v59", "v60", "v61", "v62", "v63", "v64", "v65", "v66", "v67", "v68", "v69", "v70", "v71", "v72", "v73", "v74", "v75", "v76", "v77", "v78", "v79", "v80", "v81", "v82", "v83", "v84", "v85", "v86", "v87", "v88", "v89", "v90", "v91", "v92", "v93", "v94", "v95", "v96", "v97", "v98", "v99"
DEV void attn_role(char* ws, int item, lds_ptr lds) {
    constexpr int SK = 1;
    const int tid = threadIdx.x, lane = tid & 63, w = __builtin_amdgcn_readfirstlane(tid >> 6);
    const int r = lane & 31, h = lane >> 5;
    const int head = (item & 7) * 4 + (item >> 6), qb = (item >> 3) & 7;
    const char* kvh = ws + WS_KV + (size_t)head * 64 * 3072;
    i32x4 skv = make_srd(kvh);
    skv[2] = 64 * 3072;
    const unsigned l0 = (unsigned)(uintptr_t)lds;
    const unsigned vo = lane * 16;
    STAMP(0);
    __builtin_amdgcn_s_setprio(3);
    if (w < 6) dma16(skv, vo, (unsigned)w * 1024u, l0 + 3 * AT_CHUNK + (unsigned)w * 1024u);
#pragma unroll
    for (int c = 0; c < 2; ++c)
#pragma unroll
        for (int j = 0; j < 3; ++j) dma16(skv, vo, (unsigned)(6144 + c * AT_CHUNK + (3 * w + j) * 1024), l0 + (unsigned)(c * AT_CHUNK + (3 * w + j) * 1024));
    const int tile = qb * 8 + w;
    const char* qp = ws + WS_QF + (size_t)(head * 64 + tile) * 1280;
    const bf16x8 q1 = *(const bf16x8*)(qp + lane * 16);
    s16x4 q2 = *(const s16x4*)(qp + 1024 + r * 8);
    if (h) q2 = (s16x4){0, 0, 0, 0};
    const int vrow = r < 21 ? r : 21;
    const unsigned kbase = l0 + lane * 16, k2base = l0 + 1024 + lane * 8, vbase = l0 + 1536 + h * 384 + vrow * 16;
    WAIT_VM_BAR(6);
    STAMP(1);
    {
        const bf16x8 k1 = lds_ld128((lds_cptr)lds + 3 * AT_CHUNK + lane * 16); const s16x4 k2 = lds_ld64((lds_cptr)lds + 3 * AT_CHUNK + 1024 + lane * 8);
        f32x16 s = mfma16(k1, q1, f32x16{});
        s = mfma8(k2, q2, s);
        float tm = s[0];
#pragma unroll
        for (int i = 1; i < 16; ++i) tm = fmaxf(tm, s[i]);
        tm = fmaxf(tm, __shfl_xor(tm, 32));
        const unsigned nb = pk2(-tm, 0.f) & 0xffffu;
        if (h) q2[0] = (short)nb;
    }
    const float gq = ((const float*)(ws + WS_G))[tile * 32 + r];
    asm volatile("" :: "v"(gq));
    f32x16 o = f32x16{};
    __builtin_amdgcn_s_setprio(A_PRIO);
    STAMP(2);
    {
        unsigned ka = kbase + 3 * AT_CHUNK, kb = k2base + 3 * AT_CHUNK, va = vbase + 3 * AT_CHUNK;
        unsigned soff = 6144 + 2 * AT_CHUNK + w * 3072, ldst, cur = 0, cnt = 8;
        const unsigned wbase = l0 + w * 3072;
        asm volatile(ATT_LOOP_ASM
                     : "+v"(o), "+v"(ka), "+v"(kb), "+v"(va), "+s"(soff), "=&s"(ldst), "+s"(cur), "+s"(cnt)
                     : "v"(q1), "v"(q2), "v"(vo), "s"(skv), "v"(kbase), "v"(k2base), "v"(vbase), "s"(wbase)
                     : "memory", "scc", ATT_CLOBBERS);
    }
    STAMP(3);
    float l = __shfl(o[8], r + 32);
    bool ok = (l > 9.094947e-13f) && (l < 1.8446744e19f);
#pragma unroll
    for (int i = 0; i < 12; ++i) ok = ok && (fabsf(o[i]) < INFINITY || (h && i >= 8));
    if (!__all(ok)) {
        if (h) q2[0] = 0;
        attn_slow(kvh, q1, q2, lane, o);
        l = __shfl(o[8], r + 32);
    }
    const float sc = gq * __builtin_amdgcn_rcpf(l);
    const __amdgpu_buffer_rsrc_t ro = make_rsrc(ws + WS_OF);
    const unsigned ob = (unsigned)tile * 40u * 1024u;
    u32x4 m = {pk2(o[0] * sc, o[1] * sc), pk2(o[2] * sc, o[3] * sc), pk2(o[4] * sc, o[5] * sc), pk2(o[6] * sc, o[7] * sc)};
    st16_sc1(ro, ob + (unsigned)(head * 1024 + lane * 16), m);
    if (h == 0) {
        u32x2 e = {pk2(o[8] * sc, o[9] * sc), pk2(o[10] * sc, o[11] * sc)};
        st8_sc1(ro, ob + (unsigned)((32 + (head >> 2)) * 1024 + (((head >> 1) & 1) * 32 + r) * 16 + (head & 1) * 8), e);
    }
    publish(ws, 16 + qb);
    STAMP(4);
}

constexpr int F1_STAGE = 40960;

DEV float gelu_tanh(float x) {
    const float c0 = -2.0f * 0.7978845608028654f * 1.4426950408889634f, c1 = c0 * 0.044715f;
    const float e = __builtin_amdgcn_exp2f(x * __builtin_fmaf(x * x, c1, c0));
    return x * __builtin_amdgcn_rcpf(1.0f + e);
}

DEV void bufld16(bf16x8& dst, const i32x4& srd, unsigned voff, unsigned soff) {
    asm volatile(PADS "buffer_load_dwordx4 %0, %1, %2, %3 offen" : "=v"(dst) : "v"(voff), "s"(srd), "s"(RFL(soff)) : "memory");
}
DEV void bufld16_o(bf16x8& dst, const i32x4& srd, unsigned voff, unsigned soff) {
    asm volatile(PADS "buffer_load_dwordx4 %0, %1, %2, %3 offen sc1" : "=v"(dst) : "v"(voff), "s"(srd), "s"(RFL(soff)) : "memory");
}
#define WAIT_VM_2(n, a, b) asm volatile("s_waitcnt vmcnt(" #n ")" : "+v"(a), "+v"(b) :: "memory")

template <int I, int N, class F> DEV void static_for(F&& f) { if constexpr (I < N) { f(std::integral_constant<int, I>{}); static_for<I + 1, N>(f); } }
template <int N> DEV void wait_vm() { asm volatile("s_waitcnt vmcnt(%0)" :: "n"(N) : "memory"); }
template <int N> DEV void wait_vm_bar() { asm volatile("s_waitcnt vmcnt(%0)\n\ts_barrier" :: "n"(N) : "memory"); }
template <int N> DEV void wait_vm_bar_pin(bf16x8& a) { asm volatile("s_waitcnt vmcnt(%1)\n\ts_barrier" : "+v"(a) : "n"(N) : "memory"); }

constexpr int P2_NF = 24, P2_NS = 34, P2_PD = 3, P2_AD = 6;
constexpr int p2_allowed(int s, bool up) {
    int kind[256] = {}, idx[256] = {}, n = 0;
    const int nw = up ? 2 : 3;
    auto A = [&](int k) { kind[n] = 0; idx[n] = k; ++n; };
    auto W = [&](int k) { for (int i = 0; i < nw; ++i) { kind[n] = 1; idx[n] = k; ++n; } };
    for (int k = 0; k < P2_PD; ++k) W(k);
    for (int k = 0; k < P2_AD; ++k) A(k);
    for (int u = 0; u <= s; ++u) {
        if (u == P2_NF) for (int k = P2_NF; k < P2_NF + P2_AD; ++k) A(k);
        if (u == s) break;
        if (u + P2_PD < P2_NS) W(u + P2_PD);
        if (u + P2_AD < P2_NS && (u + P2_AD < P2_NF || u >= P2_NF)) A(u + P2_AD);
    }
    int last = -1;
    for (int i = 0; i < n; ++i) if (idx[i] <= s) last = i;
    return n - 1 - last;
}

constexpr int f1_allowed1(int t, bool lower) {
    int kind[160] = {}, idx[160] = {}, n = 0;
    auto X = [&](int k) { kind[n] = 0; idx[n] = k; ++n; };
    auto W = [&](int k) { const int c = (((k & 1) == 0) == lower) ? 2 : 1; for (int i = 0; i < c; ++i) { kind[n] = 1; idx[n] = k; ++n; } };
    X(0); X(1); W(0); W(1); X(2); X(3); W(2); X(4); X(5); W(3); W(4);
    for (int u = 0; u < t; ++u) { if (u + 5 < 20) W(u + 5); if (2 * u + 7 < 40) { X(2 * u + 6); X(2 * u + 7); } }
    int last = -1;
    for (int i = 0; i < n; ++i) {
        const bool need = t < 0 ? (kind[i] == 1 && idx[i] == 0) : ((kind[i] == 0 && idx[i] <= 2 * t + 1) || (kind[i] == 1 && idx[i] <= t + 1));
        if (need) last = i;
    }
    return n - 1 - last;
}
constexpr int f1_allowed(int t) { return f1_allowed1(t, true) < f1_allowed1(t, false) ? f1_allowed1(t, true) : f1_allowed1(t, false); }

DEV void g_role(char* ws, char* out, int item, lds_ptr lds) {
    constexpr int SK = 1;
    const int tid = threadIdx.x, lane = tid & 63, w = __builtin_amdgcn_readfirstlane(tid >> 6);
    const int r = lane & 31, h = lane >> 5;
    const int xcd = item & 7, j = item >> 3;
    const unsigned l0 = (unsigned)(uintptr_t)lds;
    STAMP(0);
    __builtin_amdgcn_s_setprio(G_PRIO);
    {
        const int tq = w >> 1, fh = w & 1;
        const int mt = (xcd & 1) * 8 + (j & 7), ft = (xcd >> 1) * 4 + (j >> 3);
        const i32x4 sxf = make_srd(ws + WS_XF), swf = make_srd(ws + WS_WF);
        const unsigned sxb = (unsigned)((mt * 4 + tq) * 40) * 1024u, va = lane * 16;
        auto issue_w = [&](int t) {
            const unsigned lb = l0 + (unsigned)(t % 6) * 12288u;
            auto piece = [&](int p) { const int ks = 2 * t + (p >= 6), fg = ft * 6 + (p >= 6 ? p - 6 : p); dma16(swf, va, (unsigned)(ks * 96 + fg) * 1024u, lb + (unsigned)p * 1024u); };
            if (((t & 1) == 0) == (w < 4)) { piece(2 * (w & 3)); piece(2 * (w & 3) + 1); } else piece(8 + (w & 3));
        };
        const lds_cptr wa = (lds_cptr)lds + fh * 3072 + lane * 16;
        f32x16 acc[3];
#pragma unroll
        for (int a = 0; a < 3; ++a) acc[a] = f32x16{};
        auto run1 = [&](auto upc) {
            constexpr bool UP = decltype(upc)::value;
            bf16x8 xf[8], wf[2][3];
            auto issue_x = [&](int ks) { bufld16(xf[ks & 7], sxf, va, sxb + (unsigned)ks * 1024u); };
            auto ldw = [&](int buf, int t, int s) {
#pragma unroll
                for (int a = 0; a < 3; ++a) wf[buf][a] = lds_ld128(wa + (t % 6) * 12288 + s * 6144 + a * 1024);
            };
            issue_x(0); issue_x(1); issue_w(0); issue_w(1);
            issue_x(2); issue_x(3); issue_w(2);
            issue_x(4); issue_x(5); issue_w(3); issue_w(4);
            wait_vm_bar<f1_allowed(-1)>();
            ldw(0, 0, 0);
            auto stage = [&](auto tc) {
                constexpr int t = decltype(tc)::value;
                asm volatile("s_waitcnt vmcnt(%2) lgkmcnt(0)\n\ts_barrier" : "+v"(xf[(2 * t) & 7]), "+v"(xf[(2 * t + 1) & 7]) : "n"(f1_allowed(t)) : "memory");
                auto feed = [&]() {
                    if constexpr (t + 5 < 20) issue_w(t + 5);
                    if constexpr (2 * t + 7 < 40) { issue_x(2 * t + 6); issue_x(2 * t + 7); }
                };
                if constexpr (UP) feed();
                ldw(1, t, 1);
                SBAR();
#pragma unroll
                for (int a = 0; a < 3; ++a) acc[a] = mfma16(wf[0][a], xf[(2 * t) & 7], acc[a]);
                SBAR();
                if constexpr (t < 19) ldw(0, t + 1, 0);
                SBAR();
#pragma unroll
                for (int a = 0; a < 3; ++a) acc[a] = mfma16(wf[1][a], xf[(2 * t + 1) & 7], acc[a]);
                SBAR();
                if constexpr (!UP) feed();
                SBAR();
            };
            static_for<0, 20>(stage);
        };
        if (w >= 4) run1(std::true_type{}); else run1(std::false_type{});
        STAMP(1);
        const __amdgpu_buffer_rsrc_t rh = make_rsrc(ws + WS_HF);
#pragma unroll
        for (int a = 0; a < 3; ++a) {
            f32x16 hv;
#pragma unroll
            for (int i = 0; i < 8; ++i) hv[i] = gelu_tanh(acc[a][i]) * acc[a][8 + i];
            const int tile = mt * 4 + tq, fs = ft * 6 + fh * 3 + a;
            st16_sc1(rh, (unsigned)((tile * 96 + fs) * 1024 + lane * 16), __builtin_bit_cast(u32x4, pack8(hv, 0)));
        }
        publish(ws, mt);
        STAMP(2);
    }
    const int reg = j >> 2, sub = j & 3, kg = w & 3;
    const bool up = w >= 4;
    const int rgp = (xcd & 3) * 4 + (reg & 3), cgp = (xcd >> 2) * 2 + (reg >> 2);
    const int rp = sub >> 1, cp = sub & 1;
    const int t0 = rgp * 4 + 2 * rp + cp, t1 = rgp * 4 + 2 * rp + (cp ^ 1);
    const int c0 = cgp * 128 + cp * 64, c4 = 512 + cgp * 32;
    const int ct0 = cgp * 4 + cp * 2, ct4 = 16 + cgp;
    const int tme = up ? t1 : t0;
    const i32x4 swf = make_srd(ws + WS_WF), sh = make_srd(ws + WS_HF), so_ = make_srd(ws + WS_OF);
    const unsigned va = lane * 16;
    f32x16 acc[3];
#pragma unroll
    for (int i = 0; i < 3; ++i) acc[i] = f32x16{};
    const float gm = 1.f - ((const float*)(ws + WS_G))[tme * 32 + r];
    asm volatile("" :: "v"(gm));
    auto run = [&](auto upc) {
        constexpr bool UP = decltype(upc)::value;
        bf16x8 af[P2_AD], wf[P2_PD][3];
        auto issue_w = [&](int s) {
            const unsigned fb = s < P2_NF ? (unsigned)(WF_WD + (kg * 24 + s) * 20) * 1024u : (unsigned)(WF_WO + (kg * 10 + s - P2_NF) * 20) * 1024u;
            bufld16(wf[s % P2_PD][0], swf, va, fb + (unsigned)ct0 * 1024u); bufld16(wf[s % P2_PD][1], swf, va, fb + (unsigned)(ct0 + 1) * 1024u);
            if constexpr (!UP) bufld16(wf[s % P2_PD][2], swf, va, fb + (unsigned)ct4 * 1024u);
        };
        auto issue_a = [&](int s) {
            if (s < P2_NF) bufld16(af[s % P2_AD], sh, va, (unsigned)(tme * 96 + kg * 24 + s) * 1024u);
            else bufld16_o(af[s % P2_AD], so_, va, (unsigned)(tme * 40 + kg * 10 + s - P2_NF) * 1024u);
        };
#pragma unroll
        for (int s = 0; s < P2_PD; ++s) issue_w(s);
        consume(ws, rgp, 16);
        STAMP(3);
#pragma unroll
        for (int s = 0; s < P2_AD; ++s) issue_a(s);
        auto step = [&](auto sc) {
            constexpr int s = decltype(sc)::value;
            if constexpr (s == P2_NF) {
                STAMP(4);
                consume<false>(ws, 16 + (rgp >> 1), 32);
                STAMP(5);
#pragma unroll
                for (int t = P2_NF; t < P2_NF + P2_AD; ++t) issue_a(t);
            }
            if constexpr (UP) asm volatile("s_waitcnt vmcnt(%3)" : "+v"(af[s % P2_AD]), "+v"(wf[s % P2_PD][0]), "+v"(wf[s % P2_PD][1]) : "n"(p2_allowed(s, UP)) : "memory");
            else asm volatile("s_waitcnt vmcnt(%4)" : "+v"(af[s % P2_AD]), "+v"(wf[s % P2_PD][0]), "+v"(wf[s % P2_PD][1]), "+v"(wf[s % P2_PD][2]) : "n"(p2_allowed(s, UP)) : "memory");
            acc[0] = mfma16(wf[s % P2_PD][0], af[s % P2_AD], acc[0]); acc[1] = mfma16(wf[s % P2_PD][1], af[s % P2_AD], acc[1]);
            if constexpr (!UP) acc[2] = mfma16(wf[s % P2_PD][2], af[s % P2_AD], acc[2]);
            SBAR();
            if constexpr (s + P2_PD < P2_NS) issue_w(s + P2_PD);
            if constexpr (s + P2_AD < P2_NS && (s + P2_AD < P2_NF || s >= P2_NF)) issue_a(s + P2_AD);
            if constexpr (s == P2_NF - 1) {
#pragma unroll
                for (int i = 0; i < 16; ++i) { acc[0][i] *= gm; acc[1][i] *= gm; acc[2][i] *= gm; }
            }
            SBAR();
        };
        static_for<0, P2_NS>(step);
    };
    if (up) run(std::true_type{}); else run(std::false_type{});
    STAMP(6);
    typedef __attribute__((ext_vector_type(4))) float f32x4;
    typedef __attribute__((address_space(3))) f32x4* lds_f4;
    __syncthreads();
    {
        lds_ptr mine = lds + kg * 20480 + r * 128;
#pragma unroll
        for (int i = 0; i < 3; ++i) {
            if (i == 2 && up) break;
            const int tl = up ? 2 + i : (i == 2 ? 4 : i);
#pragma unroll
            for (int g = 0; g < 4; ++g) {
                f32x4 v = {acc[i][4 * g], acc[i][4 * g + 1], acc[i][4 * g + 2], acc[i][4 * g + 3]};
                *(lds_f4)(mine + tl * 4096 + (((2 * g + h) ^ (r & 7)) * 16)) = v;
            }
        }
    }
    __syncthreads();
#pragma unroll
    for (int it = 0; it < 2; ++it) {
        const int u = tid + 512 * it;
        if (u < 640) {
            const int tl = u >> 7, tok = (u & 127) >> 2, ch = u & 3;
            f32x4 lo = {0, 0, 0, 0}, hi = {0, 0, 0, 0};
#pragma unroll
            for (int ww = 0; ww < 4; ++ww) {
                lds_ptr p = lds + ww * 20480 + tl * 4096 + tok * 128;
                lo += *(lds_f4)(p + (((2 * ch) ^ (tok & 7)) * 16));
                hi += *(lds_f4)(p + (((2 * ch + 1) ^ (tok & 7)) * 16));
            }
            const int tt = (tl == 2 || tl == 3) ? t1 : t0, cc = tl == 4 ? c4 : c0 + (tl & 1) * 32;
            u32x4 o = {pk2(lo[0], lo[1]), pk2(lo[2], lo[3]), pk2(hi[0], hi[1]), pk2(hi[2], hi[3])};
            *(u32x4*)(out + (size_t)(tt * 32 + tok) * 1280 + (cc + ch * 8) * 2) = o;
        }
    }
    STAMP(7);
}

__global__ __launch_bounds__(512, 4) void k2_kernel(char* __restrict__ ws, char* __restrict__ out) {
    extern __shared__ __attribute__((aligned(16))) char lds_raw[];
    lds_ptr lds = (lds_ptr)lds_raw;
    if (blockIdx.x < 256) attn_role(ws, blockIdx.x, lds);
    else g_role(ws, out, blockIdx.x - 256, lds);
}

extern "C" void kernel_launch(void* const* d_in, const int* in_sizes, int n_in, void* d_out, int out_size, void* d_ws, size_t ws_size, hipStream_t stream) {
    const char* ax = (const char*)d_in[0];
    const char* wqkv = (const char*)d_in[1];
    const char* wo = (const char*)d_in[2];
    const char* wg = (const char*)d_in[4];
    const char* wu = (const char*)d_in[5];
    const char* wd = (const char*)d_in[6];
    const char* gw = (const char*)d_in[7];
    char* ws = (char*)d_ws;
    (void)hipFuncSetAttribute((const void*)k1_kernel, hipFuncAttributeMaxDynamicSharedMemorySize, K1_LDS);
    (void)hipFuncSetAttribute((const void*)k2_kernel, hipFuncAttributeMaxDynamicSharedMemorySize, 81920);
    k1_kernel<<<256, 512, K1_LDS, stream>>>(ax, wqkv, gw, wg, wu, wd, wo, ws);
    k2_kernel<<<512, 512, 81920, stream>>>(ws, (char*)d_out);
}
```

```cpp
#define A_PRIO 0
#define G_PRIO 2
#define O_SC1 1
#define K1_WT 16
#include <hip/hip_runtime.h>
#include <hip/hip_bf16.h>
#include <stdint.h>
#include <type_traits>

typedef __attribute__((ext_vector_type(8))) __bf16 bf16x8;
typedef __attribute__((ext_vector_type(8))) short s16x8;
typedef __attribute__((ext_vector_type(4))) short s16x4;
typedef __attribute__((ext_vector_type(2))) __bf16 bf16x2;
typedef __attribute__((ext_vector_type(2))) float f32x2;
typedef __attribute__((ext_vector_type(16))) float f32x16;
typedef __attribute__((ext_vector_type(4))) unsigned u32x4;
typedef __attribute__((ext_vector_type(2))) unsigned u32x2;
typedef __attribute__((ext_vector_type(4))) int i32x4;
typedef __attribute__((address_space(3))) const char* lds_cptr;
typedef __attribute__((address_space(3))) char* lds_ptr;

#define DEV __device__ __forceinline__
#define RL_AT 2
#define RL_DO 6
#define WARM_POL ""
#define WARM_AT 1

constexpr int T_ = 2048, D_ = 640, NH = 32, DH = 20, F_ = 1536;
constexpr float QSCALE = 0.22360679774997896f * 1.4426950408889634f;

constexpr size_t WS_QF = 0;
constexpr size_t WS_KV = WS_QF + (size_t)NH * 64 * 1280;
constexpr size_t WS_HF = WS_KV + (size_t)NH * 64 * 3072;
constexpr size_t WS_OF = WS_HF + (size_t)64 * 96 * 1024;
constexpr size_t WS_XF = WS_OF + (size_t)64 * 40 * 1024;
constexpr size_t WS_WF = WS_XF + (size_t)64 * 40 * 1024;
constexpr int WF_WD = 3840, WF_WO = 5760, WF_N = 6560;
constexpr size_t WS_G  = WS_WF + (size_t)WF_N * 1024;
constexpr size_t WS_FLAG = WS_G + 2048 * 4;
constexpr int FLAG_LINES = 32;
constexpr size_t WS_END = WS_FLAG + FLAG_LINES * 256;

DEV i32x4 make_srd(const void* p) {
    i32x4 s;
    s[0] = __builtin_amdgcn_readfirstlane((int)(uint32_t)(uintptr_t)p);
    s[1] = __builtin_amdgcn_readfirstlane((int)(uint32_t)((uintptr_t)p >> 32)) & 0xffff;
    s[2] = 0x7fffffff;
    s[3] = 0x00020000;
    return s;
}
#define RFL(x) (x)
#define PADS
DEV void dma16(const i32x4& srd, unsigned voff, unsigned soff, unsigned ldsaddr) {
    asm volatile(PADS "s_mov_b32 m0, %3\n\ts_nop 0\n\tbuffer_load_dwordx4 %0, %1, %2 offen lds"
                 :: "v"(voff), "s"(srd), "s"(RFL(soff)), "s"(RFL(ldsaddr)) : "memory");
}
DEV void warm(unsigned& sink, const char* base, unsigned voff) {
    asm volatile("global_load_dword %0, %1, %2" : "=v"(sink) : "v"(voff), "s"(base) : "memory");
}
#define WAIT_VM(n) asm volatile("s_waitcnt vmcnt(" #n ")" ::: "memory")
#define WAIT_VM_BAR(n) asm volatile("s_waitcnt vmcnt(" #n ")\n\ts_barrier" ::: "memory")
#define SBAR() __builtin_amdgcn_sched_barrier(0)

DEV f32x16 mfma16(bf16x8 a, bf16x8 b, f32x16 c) { return __builtin_amdgcn_mfma_f32_32x32x16_bf16(a, b, c, 0, 0, 0); }
DEV f32x16 mfma8(s16x4 a, s16x4 b, f32x16 c) { return __builtin_amdgcn_mfma_f32_32x32x8bf16_1k(a, b, c, 0, 0, 0); }
DEV unsigned pk2(float lo, float hi) {
    f32x2 v = {lo, hi};
    bf16x2 r = __builtin_convertvector(v, bf16x2);
    return __builtin_bit_cast(unsigned, r);
}
DEV bf16x8 lds_ld128(lds_cptr p) { return *(const __attribute__((address_space(3))) bf16x8*)p; }
DEV s16x4 lds_ld64(lds_cptr p) { return *(const __attribute__((address_space(3))) s16x4*)p; }
DEV s16x4 lds_tr(lds_cptr p) { return __builtin_amdgcn_ds_read_tr16_b64_v4i16((__attribute__((address_space(3))) s16x4*)p); }
DEV bf16x8 cat8(s16x4 lo, s16x4 hi) {
    s16x8 r = __builtin_shufflevector(lo, hi, 0, 1, 2, 3, 4, 5, 6, 7);
    return __builtin_bit_cast(bf16x8, r);
}
DEV bf16x8 pack8(const f32x16& v, int base) {
    u32x4 r = {pk2(v[base], v[base + 1]), pk2(v[base + 2], v[base + 3]), pk2(v[base + 4], v[base + 5]), pk2(v[base + 6], v[base + 7])};
    return __builtin_bit_cast(bf16x8, r);
}

typedef __attribute__((address_space(1))) unsigned gu32;
#define RLX_AGENT __ATOMIC_RELAXED, __HIP_MEMORY_SCOPE_AGENT
DEV __amdgpu_buffer_rsrc_t make_rsrc(void* p) { return __builtin_amdgcn_make_buffer_rsrc(p, 0, 0x7fffffff, 0x00020000); }
DEV void st16_sc1(__amdgpu_buffer_rsrc_t rs, unsigned voff, u32x4 v) { __builtin_amdgcn_raw_buffer_store_b128(v, rs, voff, 0, 16); }
DEV void st8_sc1(__amdgpu_buffer_rsrc_t rs, unsigned voff, u32x2 v) { __builtin_amdgcn_raw_buffer_store_b64(v, rs, voff, 0, 16); }
#define K1_WT_AUX K1_WT
DEV void st16_pol(__amdgpu_buffer_rsrc_t rs, unsigned voff, u32x4 v) { __builtin_amdgcn_raw_buffer_store_b128(v, rs, voff, 0, K1_WT_AUX); }
DEV void st8_pol(__amdgpu_buffer_rsrc_t rs, unsigned voff, u32x2 v) { __builtin_amdgcn_raw_buffer_store_b64(v, rs, voff, 0, K1_WT_AUX); }
DEV void publish(char* ws, int line) {
    asm volatile("s_waitcnt vmcnt(0)\n\ts_barrier" ::: "memory");
    if (threadIdx.x == 0) __hip_atomic_fetch_add((gu32*)(ws + WS_FLAG + line * 256), 1u, RLX_AGENT);
}
template <bool ACQ = true>
DEV void consume(char* ws, int line, unsigned target) {
    if (threadIdx.x < 64) {
        gu32* f = (gu32*)(ws + WS_FLAG + line * 256);
        for (unsigned spins = 0; spins < (1u << 20); ++spins) {
            if ((unsigned)__builtin_amdgcn_readfirstlane(__hip_atomic_load(f, RLX_AGENT)) >= target) break;
            __builtin_amdgcn_s_sleep(2);
        }
        if constexpr (ACQ) {
            __builtin_amdgcn_fence(__ATOMIC_ACQUIRE, "agent");
            asm volatile("s_waitcnt vmcnt(0)" ::: "memory");
        }
    }
    asm volatile("s_barrier" ::: "memory");
}

#define STAMP(i) do {} while (0)
constexpr int K1_STAGE = 32768, K1_SLOTS = 5, K1_LDS = K1_SLOTS * K1_STAGE;

template <bool VMODE>
DEV void k1_gemm(const char* x, const char* wqkv, const char* wg, const char* wu, const char* wd, const char* wo, char* ws, int mt, int nt, lds_ptr lds) {
    constexpr int SK = 0;
    const int tid = threadIdx.x, lane = tid & 63, w8 = __builtin_amdgcn_readfirstlane(tid >> 6), w = w8 & 3;
    const int wt = w >> 1, ww = w & 1, r = lane & 31, h = lane >> 5;
    const int m0 = mt * 128, n0 = nt * 128;
    STAMP(0);
    const int gw = (nt * 16 + mt) * 4 + w;
    const unsigned wl = (unsigned)(uintptr_t)lds + (unsigned)w * 8192u;
    auto unit_of = [&](int i, int gw_) -> int { return gw_ < 480 ? 2 * gw_ + (i >> 2) : min(4 * (gw_ - 480) + (i >> 1), 1359); };
    auto relayout_src = [&](int i, int gw_) -> const char* {
        const int row8 = lane >> 3, c16 = lane & 7;
        const int u = unit_of(i, gw_);
        if (gw_ < 480) {
            const int ks = u / 24, fq = u - ks * 24;
            return ((i & 2) ? wu : wg) + (size_t)(16 * ks + 8 * (i & 1) + row8) * 3072 + fq * 128 + c16 * 16;
        }
        const int row = 8 * (i & 1) + row8;
        if (u < 960) { const int ks = u / 10, cq = u - ks * 10; return wd + (size_t)(16 * ks + row) * 1280 + cq * 128 + c16 * 16; }
        const int q = u - 960, ks = q / 10, cq = q - ks * 10;
        const int k = ks < 32 ? 20 * ks + row : 20 * (4 * (ks - 32) + 2 * ((row >> 2) & 1) + (row >> 3)) + 16 + (row & 3);
        return wo + (size_t)k * 1280 + cq * 128 + c16 * 16;
    };
    if (w8 >= 4) {
        const i32x4 sx = make_srd(x), sw = make_srd(wqkv);
        const unsigned l0 = (unsigned)(uintptr_t)lds;
        const unsigned xq = lane >> 4;
        const unsigned vx0 = (unsigned)(lane >> 3) * 1280u + (unsigned)(((lane & 7) ^ xq) * 16);
        const unsigned vx1 = (unsigned)(lane >> 3) * 1280u + (unsigned)(((lane & 7) ^ xq ^ 4) * 16);
        const unsigned vw = (unsigned)(lane >> 4) * 3840u + (unsigned)(((lane & 15) ^ ((lane >> 4) << 2)) * 16);
        const unsigned sxb = (unsigned)(m0 + 32 * w) * 1280u, swb = (unsigned)(16 * w) * 3840u + (unsigned)n0 * 2u;
        auto issue = [&](int t) {
            const unsigned lb = l0 + (unsigned)(t % K1_SLOTS) * K1_STAGE;
#pragma unroll
            for (int j = 0; j < 4; ++j) {
                dma16(sx, (j & 1) ? vx1 : vx0, sxb + (unsigned)j * 10240u + (unsigned)t * 128u, lb + (unsigned)(4 * w + j) * 1024u);
                dma16(sw, vw, swb + (unsigned)(t * 64 + 4 * j) * 3840u, lb + 16384u + (unsigned)(4 * w + j) * 1024u);
            }
        };
        auto relayout_do = [&]() {
            const int g = lane >> 4, nh = g & 1, q = (lane & 15) >> 2, p = lane & 3;
            lds_cptr base = (lds_cptr)lds + w * 8192;
            const __amdgpu_buffer_rsrc_t rw = make_rsrc(ws + WS_WF);
            bf16x8 v[8];
            if (gw < 480) {
#pragma unroll
                for (int i = 0; i < 8; ++i) { lds_cptr pp = base + (i >> 2) * 4096 + nh * 2048 + (8 * h + q) * 128 + (i & 3) * 32 + p * 8; v[i] = cat8(lds_tr(pp), lds_tr(pp + 512)); }
#pragma unroll
                for (int i = 0; i < 8; ++i) { const int u = unit_of(i, gw), ks = u / 24, fq = u - ks * 24; st16_sc1(rw, (unsigned)(ks * 96 + fq * 4 + (i & 3)) * 1024u + (unsigned)lane * 16u, __builtin_bit_cast(u32x4, v[i])); }
            } else {
#pragma unroll
                for (int i = 0; i < 8; ++i) { lds_cptr pp = base + (i >> 1) * 2048 + (4 * h + q) * 128 + (i & 1) * 64 + nh * 32 + p * 8; v[i] = cat8(lds_tr(pp), lds_tr(pp + 1024)); }
#pragma unroll
                for (int i = 0; i < 8; ++i) { const int u = unit_of(i, gw); st16_sc1(rw, (unsigned)(WF_WD + 2 * u + (i & 1)) * 1024u + (unsigned)lane * 16u, __builtin_bit_cast(u32x4, v[i])); }
            }
        };
        issue(0); issue(1); issue(2); issue(3);
        const char* rsrc[8];
        auto prep = [&](int i) { int g2 = gw; asm volatile("" : "+s"(g2) :: "memory"); rsrc[i] = relayout_src(i, g2); asm volatile("" : "+v"(rsrc[i]) :: "memory"); };
        auto relayout_issue = [&]() {
#pragma unroll
            for (int i = 0; i < 8; ++i) asm volatile("s_mov_b32 m0, %1\n\ts_nop 0\n\tglobal_load_lds_dwordx4 %0, off" :: "v"(rsrc[i]), "s"(wl + (unsigned)i * 1024u) : "memory");
        };
        WAIT_VM_BAR(24);
        WAIT_VM_BAR(16); issue(4); prep(0); prep(1);
        WAIT_VM_BAR(16); issue(5); prep(2); prep(3);
        WAIT_VM_BAR(16); issue(6); prep(4); prep(5);
        WAIT_VM_BAR(16); issue(7); prep(6); prep(7);
        WAIT_VM_BAR(16); issue(8);
        WAIT_VM_BAR(16); issue(9);
        if (gw < 820) { WAIT_VM_BAR(16); relayout_issue(); WAIT_VM_BAR(16); WAIT_VM_BAR(8); WAIT_VM(0); }
        else { WAIT_VM_BAR(16); WAIT_VM_BAR(8); WAIT_VM_BAR(0); }
        if (gw < 820) relayout_do();
        return;
    }
    lds_cptr xa[4], wa[2];
#pragma unroll
    for (int s = 0; s < 4; ++s) xa[s] = (lds_cptr)lds + (wt * 64 + r) * 128 + (((2 * s + h) ^ ((r >> 1) & 7)) * 16);
    {
        const int g = lane >> 4, nh = g & 1, q = (lane & 15) >> 2, p = lane & 3;
#pragma unroll
        for (int ct = 0; ct < 2; ++ct) {
            const int slot = ww * 8 + ct * 4 + 2 * nh + (p >> 1);
            wa[ct] = (lds_cptr)lds + 16384 + (8 * h + q) * 256 + ((slot ^ (q << 2)) * 16) + 8 * (p & 1);
        }
    }
    f32x16 acc[2][2];
#pragma unroll
    for (int i = 0; i < 2; ++i)
#pragma unroll
        for (int j = 0; j < 2; ++j) acc[i][j] = f32x16{};
    bf16x8 xf[2][2], wf[2][2];
    auto ldfrag = [&](int buf, int t, int s) {
        const int so = (t % K1_SLOTS) * K1_STAGE;
#pragma unroll
        for (int i = 0; i < 2; ++i) {
            xf[buf][i] = lds_ld128(xa[s] + so + i * 4096);
            wf[buf][i] = cat8(lds_tr(wa[i] + so + s * 4096), lds_tr(wa[i] + so + s * 4096 + 1024));
        }
    };
    asm volatile("s_barrier" ::: "memory");
    STAMP(1);
    ldfrag(0, 0, 0);
#pragma unroll
    for (int t = 0; t < 10; ++t) {
        if (t < 9) asm volatile("s_barrier" ::: "memory");
#pragma unroll
        for (int s = 0; s < 4; ++s) {
            const int cur = s & 1;
            const bool more = s < 3 || t < 9;
            const int nt_ = s < 3 ? t : t + 1, ns = s < 3 ? s + 1 : 0, so = (nt_ % K1_SLOTS) * K1_STAGE, nb = cur ^ 1;
            auto mm = [&](int ct, int tt) { if constexpr (VMODE) acc[tt][ct] = mfma16(xf[cur][tt], wf[cur][ct], acc[tt][ct]); else acc[ct][tt] = mfma16(wf[cur][ct], xf[cur][tt], acc[ct][tt]); };
            mm(0, 0); SBAR();
            if (more) { xf[nb][0] = lds_ld128(xa[ns] + so); xf[nb][1] = lds_ld128(xa[ns] + so + 4096); }
            SBAR(); mm(0, 1); SBAR();
            if (more) wf[nb][0] = cat8(lds_tr(wa[0] + so + ns * 4096), lds_tr(wa[0] + so + ns * 4096 + 1024));
            SBAR(); mm(1, 0); SBAR();
            if (more) wf[nb][1] = cat8(lds_tr(wa[1] + so + ns * 4096), lds_tr(wa[1] + so + ns * 4096 + 1024));
            SBAR(); mm(1, 1); SBAR();
            if ((4 * t + s) % 15 == nt) {
                bf16x8* xp = (bf16x8*)(ws + WS_XF + (size_t)((mt * 4 + wt * 2 + ww) * 40 + 4 * t + s) * 1024 + lane * 16);
                *xp = lds_ld128(xa[s] + (t % K1_SLOTS) * K1_STAGE + ww * 4096);
            }
            SBAR();
        }
    }
    STAMP(2);
    const __amdgpu_buffer_rsrc_t rws = make_rsrc(ws);
    if constexpr (!VMODE) {
        const bool isq = nt < 5;
        const int csec = (isq ? n0 : n0 - 640) + ww * 64;
        char* base = ws + (isq ? WS_QF : WS_KV);
        const int tstride = isq ? 1280 : 3072;
        const float sc = isq ? QSCALE : 1.0f;
#pragma unroll
        for (int ct = 0; ct < 2; ++ct)
#pragma unroll
            for (int g = 0; g < 4; ++g) {
                const int qd = (csec + ct * 32 + 8 * g + 4 * h) >> 2, head = qd / 5, dq = qd - head * 5;
                const int inoff = dq < 4 ? (dq >> 1) * 512 + r * 16 + (dq & 1) * 8 : 1024 + r * 8;
#pragma unroll
                for (int tt = 0; tt < 2; ++tt) {
                    const int tile = mt * 4 + wt * 2 + tt;
                    char* p = base + (size_t)(head * 64 + tile) * tstride + inoff;
                    const f32x16& a = acc[ct][tt];
                    u32x2 v = {pk2(a[4 * g] * sc, a[4 * g + 1] * sc), pk2(a[4 * g + 2] * sc, a[4 * g + 3] * sc)};
                    st8_pol(rws, (unsigned)(p - ws), v);
                    if (!isq && dq == 4) st8_pol(rws, (unsigned)(p - ws) + 256u, (u32x2){0x00003f80u, 0u});
                }
            }
    } else {
#pragma unroll
        for (int ct = 0; ct < 2; ++ct) {
            const int col = n0 - 1280 + ww * 64 + ct * 32 + r, head = col / 20, d = col - head * 20;
#pragma unroll
            for (int tt = 0; tt < 2; ++tt) {
                const int tile = mt * 4 + wt * 2 + tt;
                char* p = ws + WS_KV + (size_t)(head * 64 + tile) * 3072 + 1536 + h * 384 + d * 16;
#pragma unroll
                for (int s = 0; s < 2; ++s) {
                    st16_pol(rws, (unsigned)(p - ws) + s * 768, __builtin_bit_cast(u32x4, pack8(acc[tt][ct], 8 * s)));
                    if (d == 0) st16_pol(rws, (unsigned)(p - ws) + s * 768 + 320, (u32x4){0x3f803f80u, 0x3f803f80u, 0x3f803f80u, 0x3f803f80u});
                    if (d == 1) st16_pol(rws, (unsigned)(p - ws) + s * 768 + 320, (u32x4){0u, 0u, 0u, 0u});
                }
            }
        }
    }
    STAMP(3);
}

DEV void k1_gate(const char* x, const char* gw, char* ws, int mt) {
    constexpr int SK = 2;
    STAMP(0);
    const int tid = threadIdx.x, lane = tid & 63, w = tid >> 6, rr = lane >> 3, ch = lane & 7;
    const char* xr = x + (size_t)(mt * 128 + w * 16 + rr) * 1280 + ch * 16;
    u32x4 xv[2][10], gv[10];
#pragma unroll
    for (int i = 0; i < 10; ++i) {
        xv[0][i] = *(const u32x4*)(xr + i * 128);
        xv[1][i] = *(const u32x4*)(xr + 8 * 1280 + i * 128);
        gv[i] = *(const u32x4*)(gw + i * 128 + ch * 16);
    }
    float z[2] = {0.f, 0.f};
#pragma unroll
    for (int i = 0; i < 10; ++i)
#pragma unroll
        for (int e = 0; e < 4; ++e) {
            const float g0 = __uint_as_float(gv[i][e] << 16), g1 = __uint_as_float(gv[i][e] & 0xffff0000u);
#pragma unroll
            for (int a = 0; a < 2; ++a)
                z[a] = __builtin_fmaf(__uint_as_float(xv[a][i][e] & 0xffff0000u), g1, __builtin_fmaf(__uint_as_float(xv[a][i][e] << 16), g0, z[a]));
        }
#pragma unroll
    for (int a = 0; a < 2; ++a) {
        z[a] += __shfl_xor(z[a], 1); z[a] += __shfl_xor(z[a], 2); z[a] += __shfl_xor(z[a], 4);
        if (ch == 0) ((float*)(ws + WS_G))[mt * 128 + w * 16 + a * 8 + rr] = 1.0f / (1.0f + __expf(-z[a]));
    }
    STAMP(1);
}

__global__ __launch_bounds__(512, 2) void k1_kernel(const char* __restrict__ x, const char* __restrict__ wqkv, const char* __restrict__ gw, const char* __restrict__ wg, const char* __restrict__ wu, const char* __restrict__ wd, const char* __restrict__ wo, char* __restrict__ ws) {
    extern __shared__ __attribute__((aligned(16))) char lds_raw[];
    lds_ptr lds = (lds_ptr)lds_raw;
    const int b = blockIdx.x, xcd = b & 7, j = b >> 3, rg = xcd & 3, cg = xcd >> 2;
    if (cg == 1 && j >= 28) {
        if (b == 255 && threadIdx.x < FLAG_LINES) *(unsigned*)(ws + WS_FLAG + threadIdx.x * 256) = 0u;
        k1_gate(x, gw, ws, rg * 4 + (j - 28));
        return;
    }
    const int mt = rg * 4 + (j & 3), nt = cg * 8 + (j >> 2);
    if (nt < 10) k1_gemm<false>(x, wqkv, wg, wu, wd, wo, ws, mt, nt, lds); else k1_gemm<true>(x, wqkv, wg, wu, wd, wo, ws, mt, nt, lds);
}

constexpr int AT_CHUNK = 24576;

DEV void attn_slow(const char* kvh, bf16x8 q1, s16x4 q2, int lane, f32x16& o) {
    const int r = lane & 31, h = lane >> 5;
    const int vrow = r < 21 ? r : 21;
    float m = -INFINITY;
    o = f32x16{};
    for (int t = 0; t < 64; ++t) {
        const char* p = kvh + t * 3072;
        const bf16x8 k1 = *(const bf16x8*)(p + lane * 16);
        const s16x4 k2 = *(const s16x4*)(p + 1024 + lane * 8);
        const bf16x8 v0 = *(const bf16x8*)(p + 1536 + h * 384 + vrow * 16);
        const bf16x8 v1 = *(const bf16x8*)(p + 2304 + h * 384 + vrow * 16);
        f32x16 s = mfma16(k1, q1, f32x16{});
        s = mfma8(k2, q2, s);
        float tm = s[0];
#pragma unroll
        for (int i = 1; i < 16; ++i) tm = fmaxf(tm, s[i]);
        tm = fmaxf(tm, __shfl_xor(tm, 32));
        const float mn = fmaxf(m, tm);
        const float alpha = __builtin_amdgcn_exp2f(m - mn);
        m = mn;
#pragma unroll
        for (int i = 0; i < 16; ++i) { s[i] = __builtin_amdgcn_exp2f(s[i] - mn); o[i] *= alpha; }
        o = mfma16(v0, pack8(s, 0), o);
        o = mfma16(v1, pack8(s, 8), o);
    }
}

#define ATT_LOOP_ASM \
    "ds_read_b128 v[72:75], %1\n\t" \
    "ds_read_b64 v[80:81], %2\n\t" \
    "ds_read_b128 v[76:79], %1 offset:3072\n\t" \
    "ds_read_b64 v[82:83], %2 offset:3072\n\t" \
    "ds_read_b128 v[84:87], %3\n\t" \
    "ds_read_b128 v[88:91], %3 offset:768\n\t" \
    "ds_read_b128 v[92:95], %3 offset:3072\n\t" \
    "ds_read_b128 v[96:99], %3 offset:3840\n\t" \
    "s_waitcnt lgkmcnt(6)\n\t" \
    "v_mfma_f32_32x32x16_bf16 v[32:47], v[72:75], %8, 0\n\t" \
    "v_mfma_f32_32x32x8_bf16 v[32:47], v[80:81], %9, v[32:47]\n\t" \
    "s_nop 15\n\t" \
    "1:\n\t" \
    "s_waitcnt vmcnt(3) lgkmcnt(0)\n\t" \
    "s_barrier\n\t" \
    "v_add_u32 %1, %6, %12\n\t" \
    "v_add_u32 %2, %6, %13\n\t" \
    "v_add_u32 %3, %6, %14\n\t" \
    "s_sub_u32 %5, %6, 0x6000\n\t" \
    "s_cmp_eq_u32 %6, 0\n\t" \
    "s_cselect_b32 %5, 0xc000, %5\n\t" \
    "s_add_u32 %5, %5, %15\n\t" \
    "s_setprio 3\n\t" \
    "v_mfma_f32_32x32x16_bf16 v[48:63], v[76:79], %8, 0\n\t" \
    "s_setprio 0\n\t" \
    "v_exp_f32 v32, v32\n\t" \
    "ds_read_b128 v[72:75], %1 offset:0\n\t" \
    "v_exp_f32 v33, v33\n\t" \
    "ds_read_b64 v[80:81], %2 offset:0\n\t" \
    "v_exp_f32 v34, v34\n\t" \
    "v_exp_f32 v35, v35\n\t" \
    "v_exp_f32 v36, v36\n\t" \
    "s_mov_b32 m0, %5\n\t" \
    "v_exp_f32 v37, v37\n\t" \
    "s_setprio 3\n\t" \
    "buffer_load_dwordx4 %10, %11, %4 offen offset:0 lds\n\t" \
    "s_setprio 0\n\t" \
    "v_exp_f32 v38, v38\n\t" \
    "v_exp_f32 v39, v39\n\t" \
    "s_setprio 3\n\t" \
    "v_mfma_f32_32x32x8_bf16 v[48:63], v[82:83], %9, v[48:63]\n\t" \
    "s_setprio 0\n\t" \
    "v_cvt_pk_bf16_f32 v64, v32, v33\n\t" \
    "v_cvt_pk_bf16_f32 v65, v34, v35\n\t" \
    "v_cvt_pk_bf16_f32 v66, v36, v37\n\t" \
    "v_cvt_pk_bf16_f32 v67, v38, v39\n\t" \
    "v_exp_f32 v40, v40\n\t" \
    "v_exp_f32 v41, v41\n\t" \
    "v_exp_f32 v42, v42\n\t" \
    "v_exp_f32 v43, v43\n\t" \
    "s_setprio 3\n\t" \
    "v_mfma_f32_32x32x16_bf16 %0, v[84:87], v[64:67], %0\n\t" \
    "s_setprio 0\n\t" \
    "ds_read_b128 v[84:87], %3 offset:0\n\t" \
    "v_exp_f32 v44, v44\n\t" \
    "v_exp_f32 v45, v45\n\t" \
    "v_exp_f32 v46, v46\n\t" \
    "v_exp_f32 v47, v47\n\t" \
    "v_cvt_pk_bf16_f32 v68, v40, v41\n\t" \
    "v_cvt_pk_bf16_f32 v69, v42, v43\n\t" \
    "v_cvt_pk_bf16_f32 v70, v44, v45\n\t" \
    "v_cvt_pk_bf16_f32 v71, v46, v47\n\t" \
    "s_waitcnt lgkmcnt(1)\n\t" \
    "s_setprio 3\n\t" \
    "v_mfma_f32_32x32x16_bf16 v[32:47], v[72:75], %8, 0\n\t" \
    "s_setprio 0\n\t" \
    "v_exp_f32 v48, v48\n\t" \
    "ds_read_b128 v[76:79], %1 offset:3072\n\t" \
    "v_exp_f32 v49, v49\n\t" \
    "ds_read_b64 v[82:83], %2 offset:3072\n\t" \
    "v_exp_f32 v50, v50\n\t" \
    "v_exp_f32 v51, v51\n\t" \
    "s_setprio 3\n\t" \
    "v_mfma_f32_32x32x16_bf16 %0, v[88:91], v[68:71], %0\n\t" \
    "s_setprio 0\n\t" \
    "ds_read_b128 v[88:91], %3 offset:768\n\t" \
    "v_exp_f32 v52, v52\n\t" \
    "v_exp_f32 v53, v53\n\t" \
    "s_setprio 3\n\t" \
    "buffer_load_dwordx4 %10, %11, %4 offen offset:1024 lds\n\t" \
    "s_setprio 0\n\t" \
    "v_exp_f32 v54, v54\n\t" \
    "v_exp_f32 v55, v55\n\t" \
    "s_setprio 3\n\t" \
    "v_mfma_f32_32x32x8_bf16 v[32:47], v[80:81], %9, v[32:47]\n\t" \
    "s_setprio 0\n\t" \
    "v_cvt_pk_bf16_f32 v64, v48, v49\n\t" \
    "v_cvt_pk_bf16_f32 v65, v50, v51\n\t" \
    "v_cvt_pk_bf16_f32 v66, v52, v53\n\t" \
    "v_cvt_pk_bf16_f32 v67, v54, v55\n\t" \
    "v_exp_f32 v56, v56\n\t" \
    "v_exp_f32 v57, v57\n\t" \
    "v_exp_f32 v58, v58\n\t" \
    "v_exp_f32 v59, v59\n\t" \
    "s_setprio 3\n\t" \
    "v_mfma_f32_32x32x16_bf16 %0, v[92:95], v[64:67], %0\n\t" \
    "s_setprio 0\n\t" \
    "ds_read_b128 v[92:95], %3 offset:3072\n\t" \
    "v_exp_f32 v60, v60\n\t" \
    "v_exp_f32 v61, v61\n\t" \
    "v_exp_f32 v62, v62\n\t" \
    "v_exp_f32 v63, v63\n\t" \
    "v_cvt_pk_bf16_f32 v68, v56, v57\n\t" \
    "v_cvt_pk_bf16_f32 v69, v58, v59\n\t" \
    "v_cvt_pk_bf16_f32 v70, v60, v61\n\t" \
    "v_cvt_pk_bf16_f32 v71, v62, v63\n\t" \
    "s_waitcnt lgkmcnt(1)\n\t" \
    "s_setprio 3\n\t" \
    "v_mfma_f32_32x32x16_bf16 v[48:63], v[76:79], %8, 0\n\t" \
    "s_setprio 0\n\t" \
    "v_exp_f32 v32, v32\n\t" \
    "ds_read_b128 v[72:75], %1 offset:6144\n\t" \
    "v_exp_f32 v33, v33\n\t" \
    "ds_read_b64 v[80:81], %2 offset:6144\n\t" \
    "v_exp_f32 v34, v34\n\t" \
    "v_exp_f32 v35, v35\n\t" \
    "s_setprio 3\n\t" \
    "v_mfma_f32_32x32x16_bf16 %0, v[96:99], v[68:71], %0\n\t" \
    "s_setprio 0\n\t" \
    "ds_read_b128 v[96:99], %3 offset:3840\n\t" \
    "v_exp_f32 v36, v36\n\t" \
    "v_exp_f32 v37, v37\n\t" \
    "s_setprio 3\n\t" \
    "buffer_load_dwordx4 %10, %11, %4 offen offset:2048 lds\n\t" \
    "s_setprio 0\n\t" \
    "v_exp_f32 v38, v38\n\t" \
    "v_exp_f32 v39, v39\n\t" \
    "s_setprio 3\n\t" \
    "v_mfma_f32_32x32x8_bf16 v[48:63], v[82:83], %9, v[48:63]\n\t" \
    "s_setprio 0\n\t" \
    "v_cvt_pk_bf16_f32 v64, v32, v33\n\t" \
    "v_cvt_pk_bf16_f32 v65, v34, v35\n\t" \
    "v_cvt_pk_bf16_f32 v66, v36, v37\n\t" \
    "v_cvt_pk_bf16_f32 v67, v38, v39\n\t" \
    "v_exp_f32 v40, v40\n\t" \
    "v_exp_f32 v41, v41\n\t" \
    "v_exp_f32 v42, v42\n\t" \
    "v_exp_f32 v43, v43\n\t" \
    "s_setprio 3\n\t" \
    "v_mfma_f32_32x32x16_bf16 %0, v[84:87], v[64:67], %0\n\t" \
    "s_setprio 0\n\t" \
    "ds_read_b128 v[84:87], %3 offset:6144\n\t" \
    "v_exp_f32 v44, v44\n\t" \
    "v_exp_f32 v45, v45\n\t" \
    "v_exp_f32 v46, v46\n\t" \
    "v_exp_f32 v47, v47\n\t" \
    "v_cvt_pk_bf16_f32 v68, v40, v41\n\t" \
    "v_cvt_pk_bf16_f32 v69, v42, v43\n\t" \
    "v_cvt_pk_bf16_f32 v70, v44, v45\n\t" \
    "v_cvt_pk_bf16_f32 v71, v46, v47\n\t" \
    "s_waitcnt lgkmcnt(1)\n\t" \
    "s_setprio 3\n\t" \
    "v_mfma_f32_32x32x16_bf16 v[32:47], v[72:75], %8, 0\n\t" \
    "s_setprio 0\n\t" \
    "v_exp_f32 v48, v48\n\t" \
    "ds_read_b128 v[76:79], %1 offset:9216\n\t" \
    "v_exp_f32 v49, v49\n\t" \
    "ds_read_b64 v[82:83], %2 offset:9216\n\t" \
    "v_exp_f32 v50, v50\n\t" \
    "v_exp_f32 v51, v51\n\t" \
    "s_setprio 3\n\t" \
    "v_mfma_f32_32x32x16_bf16 %0, v[88:91], v[68:71], %0\n\t" \
    "s_setprio 0\n\t" \
    "ds_read_b128 v[88:91], %3 offset:6912\n\t" \
    "v_exp_f32 v52, v52\n\t" \
    "v_exp_f32 v53, v53\n\t" \
    "v_exp_f32 v54, v54\n\t" \
    "v_exp_f32 v55, v55\n\t" \
    "s_setprio 3\n\t" \
    "v_mfma_f32_32x32x8_bf16 v[32:47], v[80:81], %9, v[32:47]\n\t" \
    "s_setprio 0\n\t" \
    "v_cvt_pk_bf16_f32 v64, v48, v49\n\t" \
    "v_cvt_pk_bf16_f32 v65, v50, v51\n\t" \
    "v_cvt_pk_bf16_f32 v66, v52, v53\n\t" \
    "v_cvt_pk_bf16_f32 v67, v54, v55\n\t" \
    "v_exp_f32 v56, v56\n\t" \
    "v_exp_f32 v57, v57\n\t" \
    "v_exp_f32 v58, v58\n\t" \
    "v_exp_f32 v59, v59\n\t" \
    "s_setprio 3\n\t" \
    "v_mfma_f32_32x32x16_bf16 %0, v[92:95], v[64:67], %0\n\t" \
    "s_setprio 0\n\t" \
    "ds_read_b128 v[92:95], %3 offset:9216\n\t" \
    "v_exp_f32 v60, v60\n\t" \
    "v_exp_f32 v61, v61\n\t" \
    "v_exp_f32 v62, v62\n\t" \
    "v_exp_f32 v63, v63\n\t" \
    "v_cvt_pk_bf16_f32 v68, v56, v57\n\t" \
    "v_cvt_pk_bf16_f32 v69, v58, v59\n\t" \
    "v_cvt_pk_bf16_f32 v70, v60, v61\n\t" \
    "v_cvt_pk_bf16_f32 v71, v62, v63\n\t" \
    "s_waitcnt lgkmcnt(1)\n\t" \
    "s_setprio 3\n\t" \
    "v_mfma_f32_32x32x16_bf16 v[48:63], v[76:79], %8, 0\n\t" \
    "s_setprio 0\n\t" \
    "v_exp_f32 v32, v32\n\t" \
    "ds_read_b128 v[72:75], %1 offset:12288\n\t" \
    "v_exp_f32 v33, v33\n\t" \
    "ds_read_b64 v[80:81], %2 offset:12288\n\t" \
    "v_exp_f32 v34, v34\n\t" \
    "v_exp_f32 v35, v35\n\t" \
    "s_setprio 3\n\t" \
    "v_mfma_f32_32x32x16_bf16 %0, v[96:99], v[68:71], %0\n\t" \
    "s_setprio 0\n\t" \
    "ds_read_b128 v[96:99], %3 offset:9984\n\t" \
    "v_exp_f32 v36, v36\n\t" \
    "v_exp_f32 v37, v37\n\t" \
    "v_exp_f32 v38, v38\n\t" \
    "v_exp_f32 v39, v39\n\t" \
    "s_setprio 3\n\t" \
    "v_mfma_f32_32x32x8_bf16 v[48:63], v[82:83], %9, v[48:63]\n\t" \
    "s_setprio 0\n\t" \
    "v_cvt_pk_bf16_f32 v64, v32, v33\n\t" \
    "v_cvt_pk_bf16_f32 v65, v34, v35\n\t" \
    "v_cvt_pk_bf16_f32 v66, v36, v37\n\t" \
    "v_cvt_pk_bf16_f32 v67, v38, v39\n\t" \
    "v_exp_f32 v40, v40\n\t" \
    "v_exp_f32 v41, v41\n\t" \
    "v_exp_f32 v42, v42\n\t" \
    "v_exp_f32 v43, v43\n\t" \
    "s_setprio 3\n\t" \
    "v_mfma_f32_32x32x16_bf16 %0, v[84:87], v[64:67], %0\n\t" \
    "s_setprio 0\n\t" \
    "ds_read_b128 v[84:87], %3 offset:12288\n\t" \
    "v_exp_f32 v44, v44\n\t" \
    "v_exp_f32 v45, v45\n\t" \
    "v_exp_f32 v46, v46\n\t" \
    "v_exp_f32 v47, v47\n\t" \
    "v_cvt_pk_bf16_f32 v68, v40, v41\n\t" \
    "v_cvt_pk_bf16_f32 v69, v42, v43\n\t" \
    "v_cvt_pk_bf16_f32 v70, v44, v45\n\t" \
    "v_cvt_pk_bf16_f32 v71, v46, v47\n\t" \
    "s_waitcnt lgkmcnt(1)\n\t" \
    "s_setprio 3\n\t" \
    "v_mfma_f32_32x32x16_bf16 v[32:47], v[72:75], %8, 0\n\t" \
    "s_setprio 0\n\t" \
    "v_exp_f32 v48, v48\n\t" \
    "ds_read_b128 v[76:79], %1 offset:15360\n\t" \
    "v_exp_f32 v49, v49\n\t" \
    "ds_read_b64 v[82:83], %2 offset:15360\n\t" \
    "v_exp_f32 v50, v50\n\t" \
    "v_exp_f32 v51, v51\n\t" \
    "s_setprio 3\n\t" \
    "v_mfma_f32_32x32x16_bf16 %0, v[88:91], v[68:71], %0\n\t" \
    "s_setprio 0\n\t" \
    "ds_read_b128 v[88:91], %3 offset:13056\n\t" \
    "v_exp_f32 v52, v52\n\t" \
    "v_exp_f32 v53, v53\n\t" \
    "v_exp_f32 v54, v54\n\t" \
    "v_exp_f32 v55, v55\n\t" \
    "s_setprio 3\n\t" \
    "v_mfma_f32_32x32x8_bf16 v[32:47], v[80:81], %9, v[32:47]\n\t" \
    "s_setprio 0\n\t" \
    "v_cvt_pk_bf16_f32 v64, v48, v49\n\t" \
    "v_cvt_pk_bf16_f32 v65, v50, v51\n\t" \
    "v_cvt_pk_bf16_f32 v66, v52, v53\n\t" \
    "v_cvt_pk_bf16_f32 v67, v54, v55\n\t" \
    "v_exp_f32 v56, v56\n\t" \
    "v_exp_f32 v57, v57\n\t" \
    "v_exp_f32 v58, v58\n\t" \
    "v_exp_f32 v59, v59\n\t" \
    "s_setprio 3\n\t" \
    "v_mfma_f32_32x32x16_bf16 %0, v[92:95], v[64:67], %0\n\t" \
    "s_setprio 0\n\t" \
    "ds_read_b128 v[92:95], %3 offset:15360\n\t" \
    "v_exp_f32 v60, v60\n\t" \
    "v_exp_f32 v61, v61\n\t" \
    "v_exp_f32 v62, v62\n\t" \
    "v_exp_f32 v63, v63\n\t" \
    "v_cvt_pk_bf16_f32 v68, v56, v57\n\t" \
    "v_cvt_pk_bf16_f32 v69, v58, v59\n\t" \
    "v_cvt_pk_bf16_f32 v70, v60, v61\n\t" \
    "v_cvt_pk_bf16_f32 v71, v62, v63\n\t" \
    "s_waitcnt lgkmcnt(1)\n\t" \
    "s_setprio 3\n\t" \
    "v_mfma_f32_32x32x16_bf16 v[48:63], v[76:79], %8, 0\n\t" \
    "s_setprio 0\n\t" \
    "v_exp_f32 v32, v32\n\t" \
    "ds_read_b128 v[72:75], %1 offset:18432\n\t" \
    "v_exp_f32 v33, v33\n\t" \
    "ds_read_b64 v[80:81], %2 offset:18432\n\t" \
    "v_exp_f32 v34, v34\n\t" \
    "v_exp_f32 v35, v35\n\t" \
    "s_setprio 3\n\t" \
    "v_mfma_f32_32x32x16_bf16 %0, v[96:99], v[68:71], %0\n\t" \
    "s_setprio 0\n\t" \
    "ds_read_b128 v[96:99], %3 offset:16128\n\t" \
    "v_exp_f32 v36, v36\n\t" \
    "v_exp_f32 v37, v37\n\t" \
    "v_exp_f32 v38, v38\n\t" \
    "v_exp_f32 v39, v39\n\t" \
    "s_setprio 3\n\t" \
    "v_mfma_f32_32x32x8_bf16 v[48:63], v[82:83], %9, v[48:63]\n\t" \
    "s_setprio 0\n\t" \
    "v_cvt_pk_bf16_f32 v64, v32, v33\n\t" \
    "v_cvt_pk_bf16_f32 v65, v34, v35\n\t" \
    "v_cvt_pk_bf16_f32 v66, v36, v37\n\t" \
    "v_cvt_pk_bf16_f32 v67, v38, v39\n\t" \
    "v_exp_f32 v40, v40\n\t" \
    "v_exp_f32 v41, v41\n\t" \
    "v_exp_f32 v42, v42\n\t" \
    "v_exp_f32 v43, v43\n\t" \
    "s_setprio 3\n\t" \
    "v_mfma_f32_32x32x16_bf16 %0, v[84:87], v[64:67], %0\n\t" \
    "s_setprio 0\n\t" \
    "ds_read_b128 v[84:87], %3 offset:18432\n\t" \
    "v_exp_f32 v44, v44\n\t" \
    "v_exp_f32 v45, v45\n\t" \
    "v_exp_f32 v46, v46\n\t" \
    "v_exp_f32 v47, v47\n\t" \
    "v_cvt_pk_bf16_f32 v68, v40, v41\n\t" \
    "v_cvt_pk_bf16_f32 v69, v42, v43\n\t" \
    "v_cvt_pk_bf16_f32 v70, v44, v45\n\t" \
    "v_cvt_pk_bf16_f32 v71, v46, v47\n\t" \
    "s_waitcnt lgkmcnt(1)\n\t" \
    "s_setprio 3\n\t" \
    "v_mfma_f32_32x32x16_bf16 v[32:47], v[72:75], %8, 0\n\t" \
    "s_setprio 0\n\t" \
    "v_exp_f32 v48, v48\n\t" \
    "ds_read_b128 v[76:79], %1 offset:21504\n\t" \
    "v_exp_f32 v49, v49\n\t" \
    "ds_read_b64 v[82:83], %2 offset:21504\n\t" \
    "v_exp_f32 v50, v50\n\t" \
    "v_exp_f32 v51, v51\n\t" \
    "s_setprio 3\n\t" \
    "v_mfma_f32_32x32x16_bf16 %0, v[88:91], v[68:71], %0\n\t" \
    "s_setprio 0\n\t" \
    "ds_read_b128 v[88:91], %3 offset:19200\n\t" \
    "v_exp_f32 v52, v52\n\t" \
    "v_exp_f32 v53, v53\n\t" \
    "v_exp_f32 v54, v54\n\t" \
    "v_exp_f32 v55, v55\n\t" \
    "s_setprio 3\n\t" \
    "v_mfma_f32_32x32x8_bf16 v[32:47], v[80:81], %9, v[32:47]\n\t" \
    "s_setprio 0\n\t" \
    "v_cvt_pk_bf16_f32 v64, v48, v49\n\t" \
    "v_cvt_pk_bf16_f32 v65, v50, v51\n\t" \
    "v_cvt_pk_bf16_f32 v66, v52, v53\n\t" \
    "v_cvt_pk_bf16_f32 v67, v54, v55\n\t" \
    "v_exp_f32 v56, v56\n\t" \
    "v_exp_f32 v57, v57\n\t" \
    "v_exp_f32 v58, v58\n\t" \
    "v_exp_f32 v59, v59\n\t" \
    "s_setprio 3\n\t" \
    "v_mfma_f32_32x32x16_bf16 %0, v[92:95], v[64:67], %0\n\t" \
    "s_setprio 0\n\t" \
    "ds_read_b128 v[92:95], %3 offset:21504\n\t" \
    "v_exp_f32 v60, v60\n\t" \
    "v_exp_f32 v61, v61\n\t" \
    "v_exp_f32 v62, v62\n\t" \
    "v_exp_f32 v63, v63\n\t" \
    "v_cvt_pk_bf16_f32 v68, v56, v57\n\t" \
    "v_cvt_pk_bf16_f32 v69, v58, v59\n\t" \
    "v_cvt_pk_bf16_f32 v70, v60, v61\n\t" \
    "v_cvt_pk_bf16_f32 v71, v62, v63\n\t" \
    "s_nop 1\n\t" \
    "s_setprio 3\n\t" \
    "v_mfma_f32_32x32x16_bf16 %0, v[96:99], v[68:71], %0\n\t" \
    "s_setprio 0\n\t" \
    "ds_read_b128 v[96:99], %3 offset:22272\n\t" \
    "s_add_u32 %4, %4, 0x6000\n\t" \
    "s_add_u32 %6, %6, 0x6000\n\t" \
    "s_cmp_eq_u32 %6, 0x12000\n\t" \
    "s_cselect_b32 %6, 0, %6\n\t" \
    "s_sub_u32 %7, %7, 1\n\t" \
    "s_cmp_lg_u32 %7, 0\n\t" \
    "s_cbranch_scc1 1b\n\t" \
    "s_waitcnt vmcnt(0) lgkmcnt(0)\n\t" \
    "s_nop 15"
#define ATT_CLOBBERS "v32", "v33", "v34", "v35", "v36", "v37", "v38", "v39", "v40", "v41", "v42", "v43", "v44", "v45", "v46", "v47", "v48", "v49", "v50", "v51", "v52", "v53", "v54", "v55", "v56", "v57", "v58", "v59", "v60", "v61", "v62", "v63", "v64", "v65", "v66", "v67", "v68", "v69", "v70", "v71", "v72", "v73", "v74", "v75", "v76", "v77", "v78", "v79", "v80", "v81", "v82", "v83", "v84", "v85", "v86", "v87", "v88", "v89", "v90", "v91", "v92", "v93", "v94", "v95", "v96", "v97", "v98", "v99"
DEV void attn_role(char* ws, int item, lds_ptr lds) {
    constexpr int SK = 1;
    const int tid = threadIdx.x, lane = tid & 63, w = __builtin_amdgcn_readfirstlane(tid >> 6);
    const int r = lane & 31, h = lane >> 5;
    const int head = (item & 7) * 4 + (item >> 6), qb = (item >> 3) & 7;
    const char* kvh = ws + WS_KV + (size_t)head * 64 * 3072;
    i32x4 skv = make_srd(kvh);
    skv[2] = 64 * 3072;
    const unsigned l0 = (unsigned)(uintptr_t)lds;
    const unsigned vo = lane * 16;
    STAMP(0);
    __builtin_amdgcn_s_setprio(3);
    if (w < 6) dma16(skv, vo, (unsigned)w * 1024u, l0 + 3 * AT_CHUNK + (unsigned)w * 1024u);
#pragma unroll
    for (int c = 0; c < 2; ++c)
#pragma unroll
        for (int j = 0; j < 3; ++j) dma16(skv, vo, (unsigned)(6144 + c * AT_CHUNK + (3 * w + j) * 1024), l0 + (unsigned)(c * AT_CHUNK + (3 * w + j) * 1024));
    const int tile = qb * 8 + w;
    const char* qp = ws + WS_QF + (size_t)(head * 64 + tile) * 1280;
    const bf16x8 q1 = *(const bf16x8*)(qp + lane * 16);
    s16x4 q2 = *(const s16x4*)(qp + 1024 + r * 8);
    if (h) q2 = (s16x4){0, 0, 0, 0};
    const int vrow = r < 21 ? r : 21;
    const unsigned kbase = l0 + lane * 16, k2base = l0 + 1024 + lane * 8, vbase = l0 + 1536 + h * 384 + vrow * 16;
    WAIT_VM_BAR(6);
    STAMP(1);
    {
        const bf16x8 k1 = lds_ld128((lds_cptr)lds + 3 * AT_CHUNK + lane * 16); const s16x4 k2 = lds_ld64((lds_cptr)lds + 3 * AT_CHUNK + 1024 + lane * 8);
        f32x16 s = mfma16(k1, q1, f32x16{});
        s = mfma8(k2, q2, s);
        float tm = s[0];
#pragma unroll
        for (int i = 1; i < 16; ++i) tm = fmaxf(tm, s[i]);
        tm = fmaxf(tm, __shfl_xor(tm, 32));
        const unsigned nb = pk2(-tm, 0.f) & 0xffffu;
        if (h) q2[0] = (short)nb;
    }
    const float gq = ((const float*)(ws + WS_G))[tile * 32 + r];
    asm volatile("" :: "v"(gq));
    f32x16 o = f32x16{};
    __builtin_amdgcn_s_setprio(A_PRIO);
    STAMP(2);
    {
        unsigned ka = kbase + 3 * AT_CHUNK, kb = k2base + 3 * AT_CHUNK, va = vbase + 3 * AT_CHUNK;
        unsigned soff = 6144 + 2 * AT_CHUNK + w * 3072, ldst, cur = 0, cnt = 8;
        const unsigned wbase = l0 + w * 3072;
        asm volatile(ATT_LOOP_ASM
                     : "+v"(o), "+v"(ka), "+v"(kb), "+v"(va), "+s"(soff), "=&s"(ldst), "+s"(cur), "+s"(cnt)
                     : "v"(q1), "v"(q2), "v"(vo), "s"(skv), "v"(kbase), "v"(k2base), "v"(vbase), "s"(wbase)
                     : "memory", "scc", ATT_CLOBBERS);
    }
    STAMP(3);
    float l = __shfl(o[8], r + 32);
    bool ok = (l > 9.094947e-13f) && (l < 1.8446744e19f);
#pragma unroll
    for (int i = 0; i < 12; ++i) ok = ok && (fabsf(o[i]) < INFINITY || (h && i >= 8));
    if (!__all(ok)) {
        if (h) q2[0] = 0;
        attn_slow(kvh, q1, q2, lane, o);
        l = __shfl(o[8], r + 32);
    }
    const float sc = gq * __builtin_amdgcn_rcpf(l);
    const __amdgpu_buffer_rsrc_t ro = make_rsrc(ws + WS_OF);
    const unsigned ob = (unsigned)tile * 40u * 1024u;
    u32x4 m = {pk2(o[0] * sc, o[1] * sc), pk2(o[2] * sc, o[3] * sc), pk2(o[4] * sc, o[5] * sc), pk2(o[6] * sc, o[7] * sc)};
    st16_sc1(ro, ob + (unsigned)(head * 1024 + lane * 16), m);
    if (h == 0) {
        u32x2 e = {pk2(o[8] * sc, o[9] * sc), pk2(o[10] * sc, o[11] * sc)};
        st8_sc1(ro, ob + (unsigned)((32 + (head >> 2)) * 1024 + (((head >> 1) & 1) * 32 + r) * 16 + (head & 1) * 8), e);
    }
    publish(ws, 16 + qb);
    STAMP(4);
}

constexpr int F1_STAGE = 40960;

DEV float gelu_tanh(float x) {
    const float c0 = -2.0f * 0.7978845608028654f * 1.4426950408889634f, c1 = c0 * 0.044715f;
    const float e = __builtin_amdgcn_exp2f(x * __builtin_fmaf(x * x, c1, c0));
    return x * __builtin_amdgcn_rcpf(1.0f + e);
}

DEV void bufld16(bf16x8& dst, const i32x4& srd, unsigned voff, unsigned soff) {
    asm volatile(PADS "buffer_load_dwordx4 %0, %1, %2, %3 offen" : "=v"(dst) : "v"(voff), "s"(srd), "s"(RFL(soff)) : "memory");
}
DEV void bufld16_o(bf16x8& dst, const i32x4& srd, unsigned voff, unsigned soff) {
    asm volatile(PADS "buffer_load_dwordx4 %0, %1, %2, %3 offen sc1" : "=v"(dst) : "v"(voff), "s"(srd), "s"(RFL(soff)) : "memory");
}
#define WAIT_VM_2(n, a, b) asm volatile("s_waitcnt vmcnt(" #n ")" : "+v"(a), "+v"(b) :: "memory")

template <int I, int N, class F> DEV void static_for(F&& f) { if constexpr (I < N) { f(std::integral_constant<int, I>{}); static_for<I + 1, N>(f); } }
template <int N> DEV void wait_vm() { asm volatile("s_waitcnt vmcnt(%0)" :: "n"(N) : "memory"); }
template <int N> DEV void wait_vm_bar() { asm volatile("s_waitcnt vmcnt(%0)\n\ts_barrier" :: "n"(N) : "memory"); }
template <int N> DEV void wait_vm_bar_pin(bf16x8& a) { asm volatile("s_waitcnt vmcnt(%1)\n\ts_barrier" : "+v"(a) : "n"(N) : "memory"); }

constexpr int P2_NF = 24, P2_NS = 34, P2_PD = 3, P2_AD = 6;
constexpr int p2_allowed(int s, bool up) {
    int kind[256] = {}, idx[256] = {}, n = 0;
    const int nw = up ? 2 : 3;
    auto A = [&](int k) { kind[n] = 0; idx[n] = k; ++n; };
    auto W = [&](int k) { for (int i = 0; i < nw; ++i) { kind[n] = 1; idx[n] = k; ++n; } };
    for (int k = 0; k < P2_PD; ++k) W(k);
    for (int k = 0; k < P2_AD; ++k) A(k);
    for (int u = 0; u <= s; ++u) {
        if (u == P2_NF) for (int k = P2_NF; k < P2_NF + P2_AD; ++k) A(k);
        if (u == s) break;
        if (u + P2_PD < P2_NS) W(u + P2_PD);
        if (u + P2_AD < P2_NS && (u + P2_AD < P2_NF || u >= P2_NF)) A(u + P2_AD);
    }
    int last = -1;
    for (int i = 0; i < n; ++i) if (idx[i] <= s) last = i;
    return n - 1 - last;
}

constexpr int f1_allowed1(int t, bool lower) {
    int kind[160] = {}, idx[160] = {}, n = 0;
    auto X = [&](int k) { kind[n] = 0; idx[n] = k; ++n; };
    auto W = [&](int k) { const int c = (((k & 1) == 0) == lower) ? 2 : 1; for (int i = 0; i < c; ++i) { kind[n] = 1; idx[n] = k; ++n; } };
    X(0); X(1); W(0); W(1); X(2); X(3); W(2); X(4); X(5); W(3); W(4);
    for (int u = 0; u < t; ++u) { if (u + 5 < 20) W(u + 5); if (2 * u + 7 < 40) { X(2 * u + 6); X(2 * u + 7); } }
    int last = -1;
    for (int i = 0; i < n; ++i) {
        const bool need = t < 0 ? (kind[i] == 1 && idx[i] == 0) : ((kind[i] == 0 && idx[i] <= 2 * t + 1) || (kind[i] == 1 && idx[i] <= t + 1));
        if (need) last = i;
    }
    return n - 1 - last;
}
constexpr int f1_allowed(int t) { return f1_allowed1(t, true) < f1_allowed1(t, false) ? f1_allowed1(t, true) : f1_allowed1(t, false); }

DEV void g_role(char* ws, char* out, int item, lds_ptr lds) {
    constexpr int SK = 1;
    const int tid = threadIdx.x, lane = tid & 63, w = __builtin_amdgcn_readfirstlane(tid >> 6);
    const int r = lane & 31, h = lane >> 5;
    const int xcd = item & 7, j = item >> 3;
    const unsigned l0 = (unsigned)(uintptr_t)lds;
    STAMP(0);
    __builtin_amdgcn_s_setprio(G_PRIO);
    {
        const int tq = w >> 1, fh = w & 1;
        const int mt = (xcd & 1) * 8 + (j & 7), ft = (xcd >> 1) * 4 + (j >> 3);
        const i32x4 sxf = make_srd(ws + WS_XF), swf = make_srd(ws + WS_WF);
        const unsigned sxb = (unsigned)((mt * 4 + tq) * 40) * 1024u, va = lane * 16;
        auto issue_w = [&](int t) {
            const unsigned lb = l0 + (unsigned)(t % 6) * 12288u;
            auto piece = [&](int p) { const int ks = 2 * t + (p >= 6), fg = ft * 6 + (p >= 6 ? p - 6 : p); dma16(swf, va, (unsigned)(ks * 96 + fg) * 1024u, lb + (unsigned)p * 1024u); };
            if (((t & 1) == 0) == (w < 4)) { piece(2 * (w & 3)); piece(2 * (w & 3) + 1); } else piece(8 + (w & 3));
        };
        const lds_cptr wa = (lds_cptr)lds + fh * 3072 + lane * 16;
        f32x16 acc[3];
#pragma unroll
        for (int a = 0; a < 3; ++a) acc[a] = f32x16{};
        auto run1 = [&](auto upc) {
            constexpr bool UP = decltype(upc)::value;
            bf16x8 xf[8], wf[2][3];
            auto issue_x = [&](int ks) { bufld16(xf[ks & 7], sxf, va, sxb + (unsigned)ks * 1024u); };
            auto ldw = [&](int buf, int t, int s) {
#pragma unroll
                for (int a = 0; a < 3; ++a) wf[buf][a] = lds_ld128(wa + (t % 6) * 12288 + s * 6144 + a * 1024);
            };
            issue_x(0); issue_x(1); issue_w(0); issue_w(1);
            issue_x(2); issue_x(3); issue_w(2);
            issue_x(4); issue_x(5); issue_w(3); issue_w(4);
            wait_vm_bar<f1_allowed(-1)>();
            ldw(0, 0, 0);
            auto stage = [&](auto tc) {
                constexpr int t = decltype(tc)::value;
                asm volatile("s_waitcnt vmcnt(%2) lgkmcnt(0)\n\ts_barrier" : "+v"(xf[(2 * t) & 7]), "+v"(xf[(2 * t + 1) & 7]) : "n"(f1_allowed(t)) : "memory");
                auto feed = [&]() {
                    if constexpr (t + 5 < 20) issue_w(t + 5);
                    if constexpr (2 * t + 7 < 40) { issue_x(2 * t + 6); issue_x(2 * t + 7); }
                };
                if constexpr (UP) feed();
                ldw(1, t, 1);
                SBAR();
#pragma unroll
                for (int a = 0; a < 3; ++a) acc[a] = mfma16(wf[0][a], xf[(2 * t) & 7], acc[a]);
                SBAR();
                if constexpr (t < 19) ldw(0, t + 1, 0);
                SBAR();
#pragma unroll
                for (int a = 0; a < 3; ++a) acc[a] = mfma16(wf[1][a], xf[(2 * t + 1) & 7], acc[a]);
                SBAR();
                if constexpr (!UP) feed();
                SBAR();
            };
            static_for<0, 20>(stage);
        };
        if (w >= 4) run1(std::true_type{}); else run1(std::false_type{});
        STAMP(1);
        const __amdgpu_buffer_rsrc_t rh = make_rsrc(ws + WS_HF);
#pragma unroll
        for (int a = 0; a < 3; ++a) {
            f32x16 hv;
#pragma unroll
            for (int i = 0; i < 8; ++i) hv[i] = gelu_tanh(acc[a][i]) * acc[a][8 + i];
            const int tile = mt * 4 + tq, fs = ft * 6 + fh * 3 + a;
            st16_sc1(rh, (unsigned)((tile * 96 + fs) * 1024 + lane * 16), __builtin_bit_cast(u32x4, pack8(hv, 0)));
        }
        publish(ws, mt);
        STAMP(2);
    }
    const int reg = j >> 2, sub = j & 3, kg = w & 3;
    const bool up = w >= 4;
    const int rgp = (xcd & 3) * 4 + (reg & 3), cgp = (xcd >> 2) * 2 + (reg >> 2);
    const int rp = sub >> 1, cp = sub & 1;
    const int t0 = rgp * 4 + 2 * rp + cp, t1 = rgp * 4 + 2 * rp + (cp ^ 1);
    const int c0 = cgp * 128 + cp * 64, c4 = 512 + cgp * 32;
    const int ct0 = cgp * 4 + cp * 2, ct4 = 16 + cgp;
    const int tme = up ? t1 : t0;
    const i32x4 swf = make_srd(ws + WS_WF), sh = make_srd(ws + WS_HF), so_ = make_srd(ws + WS_OF);
    const unsigned va = lane * 16;
    f32x16 acc[3];
#pragma unroll
    for (int i = 0; i < 3; ++i) acc[i] = f32x16{};
    const float gm = 1.f - ((const float*)(ws + WS_G))[tme * 32 + r];
    asm volatile("" :: "v"(gm));
    auto run = [&](auto upc) {
        constexpr bool UP = decltype(upc)::value;
        bf16x8 af[P2_AD], wf[P2_PD][3];
        auto issue_w = [&](int s) {
            const unsigned fb = s < P2_NF ? (unsigned)(WF_WD + (kg * 24 + s) * 20) * 1024u : (unsigned)(WF_WO + (kg * 10 + s - P2_NF) * 20) * 1024u;
            bufld16(wf[s % P2_PD][0], swf, va, fb + (unsigned)ct0 * 1024u); bufld16(wf[s % P2_PD][1], swf, va, fb + (unsigned)(ct0 + 1) * 1024u);
            if constexpr (!UP) bufld16(wf[s % P2_PD][2], swf, va, fb + (unsigned)ct4 * 1024u);
        };
        auto issue_a = [&](int s) {
            if (s < P2_NF) bufld16(af[s % P2_AD], sh, va, (unsigned)(tme * 96 + kg * 24 + s) * 1024u);
            else bufld16_o(af[s % P2_AD], so_, va, (unsigned)(tme * 40 + kg * 10 + s - P2_NF) * 1024u);
        };
#pragma unroll
        for (int s = 0; s < P2_PD; ++s) issue_w(s);
        consume(ws, rgp, 16);
        STAMP(3);
#pragma unroll
        for (int s = 0; s < P2_AD; ++s) issue_a(s);
        auto step = [&](auto sc) {
            constexpr int s = decltype(sc)::value;
            if constexpr (s == P2_NF) {
                STAMP(4);
                consume<false>(ws, 16 + (rgp >> 1), 32);
                STAMP(5);
#pragma unroll
                for (int t = P2_NF; t < P2_NF + P2_AD; ++t) issue_a(t);
            }
            if constexpr (UP) asm volatile("s_waitcnt vmcnt(%3)" : "+v"(af[s % P2_AD]), "+v"(wf[s % P2_PD][0]), "+v"(wf[s % P2_PD][1]) : "n"(p2_allowed(s, UP)) : "memory");
            else asm volatile("s_waitcnt vmcnt(%4)" : "+v"(af[s % P2_AD]), "+v"(wf[s % P2_PD][0]), "+v"(wf[s % P2_PD][1]), "+v"(wf[s % P2_PD][2]) : "n"(p2_allowed(s, UP)) : "memory");
            acc[0] = mfma16(wf[s % P2_PD][0], af[s % P2_AD], acc[0]); acc[1] = mfma16(wf[s % P2_PD][1], af[s % P2_AD], acc[1]);
            if constexpr (!UP) acc[2] = mfma16(wf[s % P2_PD][2], af[s % P2_AD], acc[2]);
            SBAR();
            if constexpr (s + P2_PD < P2_NS) issue_w(s + P2_PD);
            if constexpr (s + P2_AD < P2_NS && (s + P2_AD < P2_NF || s >= P2_NF)) issue_a(s + P2_AD);
            if constexpr (s == P2_NF - 1) {
#pragma unroll
                for (int i = 0; i < 16; ++i) { acc[0][i] *= gm; acc[1][i] *= gm; acc[2][i] *= gm; }
            }
            SBAR();
        };
        static_for<0, P2_NS>(step);
    };
    if (up) run(std::true_type{}); else run(std::false_type{});
    STAMP(6);
    typedef __attribute__((ext_vector_type(4))) float f32x4;
    typedef __attribute__((address_space(3))) f32x4* lds_f4;
    __syncthreads();
    {
        lds_ptr mine = lds + kg * 20480 + r * 128;
#pragma unroll
        for (int i = 0; i < 3; ++i) {
            if (i == 2 && up) break;
            const int tl = up ? 2 + i : (i == 2 ? 4 : i);
#pragma unroll
            for (int g = 0; g < 4; ++g) {
                f32x4 v = {acc[i][4 * g], acc[i][4 * g + 1], acc[i][4 * g + 2], acc[i][4 * g + 3]};
                *(lds_f4)(mine + tl * 4096 + (((2 * g + h) ^ (r & 7)) * 16)) = v;
            }
        }
    }
    __syncthreads();
#pragma unroll
    for (int it = 0; it < 2; ++it) {
        const int u = tid + 512 * it;
        if (u < 640) {
            const int tl = u >> 7, tok = (u & 127) >> 2, ch = u & 3;
            f32x4 lo = {0, 0, 0, 0}, hi = {0, 0, 0, 0};
#pragma unroll
            for (int ww = 0; ww < 4; ++ww) {
                lds_ptr p = lds + ww * 20480 + tl * 4096 + tok * 128;
                lo += *(lds_f4)(p + (((2 * ch) ^ (tok & 7)) * 16));
                hi += *(lds_f4)(p + (((2 * ch + 1) ^ (tok & 7)) * 16));
            }
            const int tt = (tl == 2 || tl == 3) ? t1 : t0, cc = tl == 4 ? c4 : c0 + (tl & 1) * 32;
            u32x4 o = {pk2(lo[0], lo[1]), pk2(lo[2], lo[3]), pk2(hi[0], hi[1]), pk2(hi[2], hi[3])};
            *(u32x4*)(out + (size_t)(tt * 32 + tok) * 1280 + (cc + ch * 8) * 2) = o;
        }
    }
    STAMP(7);
}

__global__ __launch_bounds__(512, 4) void k2_kernel(char* __restrict__ ws, char* __restrict__ out) {
    extern __shared__ __attribute__((aligned(16))) char lds_raw[];
    lds_ptr lds = (lds_ptr)lds_raw;
    if (blockIdx.x < 256) attn_role(ws, blockIdx.x, lds);
    else g_role(ws, out, blockIdx.x - 256, lds);
}

extern "C" void kernel_launch(void* const* d_in, const int* in_sizes, int n_in, void* d_out, int out_size, void* d_ws, size_t ws_size, hipStream_t stream) {
    const char* ax = (const char*)d_in[0];
    const char* wqkv = (const char*)d_in[1];
    const char* wo = (const char*)d_in[2];
    const char* wg = (const char*)d_in[4];
    const char* wu = (const char*)d_in[5];
    const char* wd = (const char*)d_in[6];
    const char* gw = (const char*)d_in[7];
    char* ws = (char*)d_ws;
    (void)hipFuncSetAttribute((const void*)k1_kernel, hipFuncAttributeMaxDynamicSharedMemorySize, K1_LDS);
    (void)hipFuncSetAttribute((const void*)k2_kernel, hipFuncAttributeMaxDynamicSharedMemorySize, 81920);
    k1_kernel<<<256, 512, K1_LDS, stream>>>(ax, wqkv, gw, wg, wu, wd, wo, ws);
    k2_kernel<<<512, 512, 81920, stream>>>(ws, (char*)d_out);
}
```

```cpp
#define A_PRIO 0
#define G_PRIO 0
#define O_SC1 1
#define POLL_SLEEP 8
#include <hip/hip_runtime.h>
#include <hip/hip_bf16.h>
#include <stdint.h>
#include <type_traits>

typedef __attribute__((ext_vector_type(8))) __bf16 bf16x8;
typedef __attribute__((ext_vector_type(8))) short s16x8;
typedef __attribute__((ext_vector_type(4))) short s16x4;
typedef __attribute__((ext_vector_type(2))) __bf16 bf16x2;
typedef __attribute__((ext_vector_type(2))) float f32x2;
typedef __attribute__((ext_vector_type(16))) float f32x16;
typedef __attribute__((ext_vector_type(4))) unsigned u32x4;
typedef __attribute__((ext_vector_type(2))) unsigned u32x2;
typedef __attribute__((ext_vector_type(4))) int i32x4;
typedef __attribute__((address_space(3))) const char* lds_cptr;
typedef __attribute__((address_space(3))) char* lds_ptr;

#define DEV __device__ __forceinline__
#define RL_AT 2
#define RL_DO 6
#define WARM_POL ""
#define WARM_AT 1

constexpr int T_ = 2048, D_ = 640, NH = 32, DH = 20, F_ = 1536;
constexpr float QSCALE = 0.22360679774997896f * 1.4426950408889634f;

constexpr size_t WS_QF = 0;
constexpr size_t WS_KV = WS_QF + (size_t)NH * 64 * 1280;
constexpr size_t WS_HF = WS_KV + (size_t)NH * 64 * 3072;
constexpr size_t WS_OF = WS_HF + (size_t)64 * 96 * 1024;
constexpr size_t WS_WF = WS_OF + (size_t)64 * 40 * 1024;
constexpr int WF_WD = 0, WF_WO = 1920, WF_N = 2720;
constexpr size_t WS_G  = WS_WF + (size_t)WF_N * 1024;
constexpr size_t WS_FLAG = WS_G + 2048 * 4;
constexpr int FLAG_LINES = 8;
constexpr size_t WS_END = WS_FLAG + FLAG_LINES * 256;

DEV i32x4 make_srd(const void* p) {
    i32x4 s;
    s[0] = __builtin_amdgcn_readfirstlane((int)(uint32_t)(uintptr_t)p);
    s[1] = __builtin_amdgcn_readfirstlane((int)(uint32_t)((uintptr_t)p >> 32)) & 0xffff;
    s[2] = 0x7fffffff;
    s[3] = 0x00020000;
    return s;
}
#define RFL(x) (x)
#define PADS
DEV void dma16(const i32x4& srd, unsigned voff, unsigned soff, unsigned ldsaddr) {
    asm volatile(PADS "s_mov_b32 m0, %3\n\ts_nop 0\n\tbuffer_load_dwordx4 %0, %1, %2 offen lds"
                 :: "v"(voff), "s"(srd), "s"(RFL(soff)), "s"(RFL(ldsaddr)) : "memory");
}
DEV void warm(unsigned& sink, const char* base, unsigned voff) {
    asm volatile("global_load_dword %0, %1, %2" : "=v"(sink) : "v"(voff), "s"(base) : "memory");
}
#define WAIT_VM(n) asm volatile("s_waitcnt vmcnt(" #n ")" ::: "memory")
#define WAIT_VM_BAR(n) asm volatile("s_waitcnt vmcnt(" #n ")\n\ts_barrier" ::: "memory")
#define SBAR() __builtin_amdgcn_sched_barrier(0)

DEV f32x16 mfma16(bf16x8 a, bf16x8 b, f32x16 c) { return __builtin_amdgcn_mfma_f32_32x32x16_bf16(a, b, c, 0, 0, 0); }
DEV f32x16 mfma8(s16x4 a, s16x4 b, f32x16 c) { return __builtin_amdgcn_mfma_f32_32x32x8bf16_1k(a, b, c, 0, 0, 0); }
DEV unsigned pk2(float lo, float hi) {
    f32x2 v = {lo, hi};
    bf16x2 r = __builtin_convertvector(v, bf16x2);
    return __builtin_bit_cast(unsigned, r);
}
DEV bf16x8 lds_ld128(lds_cptr p) { return *(const __attribute__((address_space(3))) bf16x8*)p; }
DEV s16x4 lds_ld64(lds_cptr p) { return *(const __attribute__((address_space(3))) s16x4*)p; }
DEV s16x4 lds_tr(lds_cptr p) { return __builtin_amdgcn_ds_read_tr16_b64_v4i16((__attribute__((address_space(3))) s16x4*)p); }
DEV bf16x8 cat8(s16x4 lo, s16x4 hi) {
    s16x8 r = __builtin_shufflevector(lo, hi, 0, 1, 2, 3, 4, 5, 6, 7);
    return __builtin_bit_cast(bf16x8, r);
}
DEV bf16x8 pack8(const f32x16& v, int base) {
    u32x4 r = {pk2(v[base], v[base + 1]), pk2(v[base + 2], v[base + 3]), pk2(v[base + 4], v[base + 5]), pk2(v[base + 6], v[base + 7])};
    return __builtin_bit_cast(bf16x8, r);
}

typedef __attribute__((address_space(1))) unsigned gu32;
#define RLX_AGENT __ATOMIC_RELAXED, __HIP_MEMORY_SCOPE_AGENT
DEV __amdgpu_buffer_rsrc_t make_rsrc(void* p) { return __builtin_amdgcn_make_buffer_rsrc(p, 0, 0x7fffffff, 0x00020000); }
DEV void st16_sc1(__amdgpu_buffer_rsrc_t rs, unsigned voff, u32x4 v) { __builtin_amdgcn_raw_buffer_store_b128(v, rs, voff, 0, 16); }
DEV void st8_sc1(__amdgpu_buffer_rsrc_t rs, unsigned voff, u32x2 v) { __builtin_amdgcn_raw_buffer_store_b64(v, rs, voff, 0, 16); }
#define K1_WT_AUX 16
DEV void st16_pol(__amdgpu_buffer_rsrc_t rs, unsigned voff, u32x4 v) { __builtin_amdgcn_raw_buffer_store_b128(v, rs, voff, 0, K1_WT_AUX); }
DEV void st8_pol(__amdgpu_buffer_rsrc_t rs, unsigned voff, u32x2 v) { __builtin_amdgcn_raw_buffer_store_b64(v, rs, voff, 0, K1_WT_AUX); }
DEV void publish(char* ws, int line) {
    asm volatile("s_waitcnt vmcnt(0)\n\ts_barrier" ::: "memory");
    if (threadIdx.x == 0) __hip_atomic_fetch_add((gu32*)(ws + WS_FLAG + line * 256), 1u, RLX_AGENT);
}
template <bool ACQ = true>
DEV void consume(char* ws, int line, unsigned target) {
    if (threadIdx.x < 64) {
        gu32* f = (gu32*)(ws + WS_FLAG + line * 256);
        for (unsigned spins = 0; spins < (1u << 20); ++spins) {
            if ((unsigned)__builtin_amdgcn_readfirstlane(__hip_atomic_load(f, RLX_AGENT)) >= target) break;
            __builtin_amdgcn_s_sleep(POLL_SLEEP);
        }
        if constexpr (ACQ) {
            __builtin_amdgcn_fence(__ATOMIC_ACQUIRE, "agent");
            asm volatile("s_waitcnt vmcnt(0)" ::: "memory");
        }
    }
    asm volatile("s_barrier" ::: "memory");
}

#define STAMP(i) do {} while (0)
constexpr int K1_XS = 16384, K1_WS = 20480, K1_WB = 3 * K1_XS, K1_GW = K1_WB + 5 * K1_WS, K1_LDS = K1_GW + 1280 + 64;

DEV float gelu_tanh(float x) {
    const float c0 = -2.0f * 0.7978845608028654f * 1.4426950408889634f, c1 = c0 * 0.044715f;
    const float e = __builtin_amdgcn_exp2f(x * __builtin_fmaf(x * x, c1, c0));
    return x * __builtin_amdgcn_rcpf(1.0f + e);
}
template <int I, int N, class F> DEV void static_for(F&& f) { if constexpr (I < N) { f(std::integral_constant<int, I>{}); static_for<I + 1, N>(f); } }
template <int N> DEV void wait_vm() { asm volatile("s_waitcnt vmcnt(%0)" :: "n"(N) : "memory"); }
template <int N> DEV void wait_vm_bar() { asm volatile("s_waitcnt vmcnt(%0)\n\ts_barrier" :: "n"(N) : "memory"); }

constexpr int k1_allowed(int t) {
    int kind[160] = {}, idx[160] = {}, n = 0;
    auto P = [&](int k, int i, int c) { for (int q = 0; q < c; ++q) { kind[n] = k; idx[n] = i; ++n; } };
    P(0, 0, 4); P(1, 0, 5); P(1, 1, 5); P(0, 1, 4); P(1, 2, 5); P(1, 3, 5);
    for (int u = 0; u < t; ++u) { if ((u & 1) == 0 && u / 2 + 2 < 10) P(0, u / 2 + 2, 4); if (u + 4 < 20) P(1, u + 4, 5); if (u == 16) P(2, 0, 4); }
    int last = -1;
    for (int i = 0; i < n; ++i) {
        const bool need = (kind[i] == 0 && idx[i] <= ((t + 1) >> 1)) || (kind[i] == 1 && idx[i] <= t + 1);
        if (need) last = i;
    }
    return n - 1 - last;
}

template <int MODE>
DEV void k1_gemm(const char* x, const char* wqkv, const char* gw, const char* wg, const char* wu, const char* wd, const char* wo, char* ws, int mt, int j, lds_ptr lds) {
    constexpr int SK = 0;
    const int tid = threadIdx.x, lane = tid & 63, w8 = __builtin_amdgcn_readfirstlane(tid >> 6), w = w8 & 3;
    const int wt = w >> 1, ww = w & 1, r = lane & 31, h = lane >> 5;
    const int m0 = mt * 128, n0 = (j < 15 ? j : 14) * 128, f0 = j * 96;
    const unsigned l0 = (unsigned)(uintptr_t)lds;
    STAMP(0);
    if (w8 >= 4) {
        const i32x4 sx = make_srd(x), sw = make_srd(wqkv), sg = make_srd(wg), su = make_srd(wu);
        const unsigned xq = lane >> 4;
        const unsigned vx0 = (unsigned)(lane >> 3) * 1280u + (unsigned)(((lane & 7) ^ xq) * 16);
        const unsigned vx1 = (unsigned)(lane >> 3) * 1280u + (unsigned)(((lane & 7) ^ xq ^ 4) * 16);
        const unsigned vq = (unsigned)(lane >> 4) * 3840u + (unsigned)(((lane & 15) ^ ((lane >> 4) << 2)) * 16);
        unsigned vf[3];
#pragma unroll
        for (int i = 0; i < 3; ++i) { const int p = 3 * w + i, L = (p % 6) * 64 + lane, row = L / 12, c16 = L - row * 12; vf[i] = (unsigned)row * 3072u + (unsigned)((p < 6 ? c16 : (c16 ^ 2)) * 16); }
        const unsigned sxb = (unsigned)(m0 + 32 * w) * 1280u, sqb = (unsigned)(8 * w) * 3840u + (unsigned)n0 * 2u, sfb = (unsigned)f0 * 2u;
        auto issue_x = [&](int u) {
            const unsigned lb = l0 + (unsigned)(u % 3) * K1_XS;
#pragma unroll
            for (int i = 0; i < 4; ++i) dma16(sx, (i & 1) ? vx1 : vx0, sxb + (unsigned)i * 10240u + (unsigned)u * 128u, lb + (unsigned)(4 * w + i) * 1024u);
        };
        auto issue_w = [&](int t) {
            const unsigned lb = l0 + K1_WB + (unsigned)(t % 5) * K1_WS;
#pragma unroll
            for (int i = 0; i < 2; ++i) dma16(sw, vq, sqb + (unsigned)(t * 32 + 4 * i) * 3840u, lb + (unsigned)(2 * w + i) * 1024u);
            const unsigned so = sfb + (unsigned)t * (32u * 3072u);
            if (w < 2) {
#pragma unroll
                for (int i = 0; i < 3; ++i) dma16(sg, vf[i], so, lb + 8192u + (unsigned)(3 * w + i) * 1024u);
            } else {
#pragma unroll
                for (int i = 0; i < 3; ++i) dma16(su, vf[i], so, lb + 8192u + (unsigned)(3 * w + i) * 1024u);
            }
        };
        const int gwv = (mt * 16 + j) * 4 + w;
        const int u0 = gwv, u1 = gwv + 1024 < 1360 ? gwv + 1024 : gwv;
        const unsigned wl = l0 + K1_WB + (unsigned)w * 4096u;
        const char* rsrc[4];
        auto prep = [&](int i) {
            int u = (i >> 1) ? u1 : u0;
            asm volatile("" : "+s"(u) :: "memory");
            const int row = 8 * (i & 1) + (lane >> 3), c16 = lane & 7;
            if (u < 960) { const int ks = u / 10, cq = u - ks * 10; rsrc[i] = wd + (size_t)(16 * ks + row) * 1280 + cq * 128 + c16 * 16; }
            else { const int q = u - 960, ks = q / 10, cq = q - ks * 10;
                   const int k = ks < 32 ? 20 * ks + row : 20 * (4 * (ks - 32) + 2 * ((row >> 2) & 1) + (row >> 3)) + 16 + (row & 3);
                   rsrc[i] = wo + (size_t)k * 1280 + cq * 128 + c16 * 16; }
            asm volatile("" : "+v"(rsrc[i]) :: "memory");
        };
        auto rl_issue = [&]() {
#pragma unroll
            for (int i = 0; i < 4; ++i) { const char* p = rsrc[i]; const unsigned d = wl + (unsigned)i * 1024u; asm volatile("s_mov_b32 m0, %1\n\ts_nop 0\n\tglobal_load_lds_dwordx4 %0, off" :: "v"(p), "s"(d) : "memory"); }
        };
        issue_x(0); issue_w(0); issue_w(1); issue_x(1); issue_w(2); issue_w(3);
        wait_vm_bar<k1_allowed(-1)>();
        auto top = [&](auto tc) {
            constexpr int t = decltype(tc)::value;
            wait_vm_bar<k1_allowed(t)>();
            if constexpr ((t & 1) == 0 && t / 2 + 2 < 10) issue_x(t / 2 + 2);
            if constexpr (t + 4 < 20) issue_w(t + 4);
            if constexpr (t >= 2 && t < 6) prep(t - 2);
            if constexpr (t == 16) rl_issue();
        };
        static_for<0, 19>(top);
        wait_vm<0>();
        {
            const int g = lane >> 4, nh = g & 1, q = (lane & 15) >> 2, p = lane & 3;
            lds_cptr base = (lds_cptr)lds + K1_WB + w * 4096;
            const __amdgpu_buffer_rsrc_t rw = make_rsrc(ws + WS_WF);
            bf16x8 v[4];
#pragma unroll
            for (int i = 0; i < 4; ++i) { lds_cptr pp = base + (i >> 1) * 2048 + (4 * h + q) * 128 + (i & 1) * 64 + nh * 32 + p * 8; v[i] = cat8(lds_tr(pp), lds_tr(pp + 1024)); }
#pragma unroll
            for (int i = 0; i < 4; ++i) *(bf16x8*)(ws + WS_WF + (size_t)(2 * ((i >> 1) ? u1 : u0) + (i & 1)) * 1024 + lane * 16) = v[i];
            (void)rw;
        }
        return;
    }
    if constexpr (MODE == 2) {
        if (tid < 80) *(__attribute__((address_space(3))) u32x4*)(lds + K1_GW + tid * 16) = *(const u32x4*)(gw + tid * 16);
        else if (tid == 80) *(__attribute__((address_space(3))) u32x4*)(lds + K1_GW + 1280) = (u32x4){0u, 0u, 0u, 0u};
    }
    lds_cptr xa[4], qa[2], fa[3];
#pragma unroll
    for (int s = 0; s < 4; ++s) xa[s] = (lds_cptr)lds + (wt * 64 + r) * 128 + (((2 * s + h) ^ ((r >> 1) & 7)) * 16);
    {
        const int g = lane >> 4, nh = g & 1, q = (lane & 15) >> 2, p = lane & 3;
#pragma unroll
        for (int ct = 0; ct < 2; ++ct) {
            const int slot = ww * 8 + ct * 4 + 2 * nh + (p >> 1);
            qa[ct] = (lds_cptr)lds + K1_WB + (8 * h + q) * 256 + ((slot ^ (q << 2)) * 16) + 8 * (p & 1);
        }
#pragma unroll
        for (int a = 0; a < 3; ++a) {
            const int aa = ww * 3 + a, c16 = 2 * (nh ? (aa ^ 1) : aa) + (p >> 1);
            fa[a] = (lds_cptr)lds + K1_WB + 8192 + nh * 6144 + (8 * h + q) * 192 + c16 * 16 + 8 * (p & 1);
        }
    }
    const lds_cptr ga = (lds_cptr)lds + K1_GW + (r == 0 ? h * 16 : 1280);
    const int gstep = r == 0 ? 32 : 0;
    f32x16 aq[2][2], af[3][2];
#pragma unroll
    for (int i = 0; i < 2; ++i) { aq[i][0] = f32x16{}; aq[i][1] = f32x16{}; }
#pragma unroll
    for (int a = 0; a < 3; ++a) { af[a][0] = f32x16{}; af[a][1] = f32x16{}; }
    bf16x8 xf[2][2], qf[2][2], ff[2][3];
    auto rd_x = [&](int b, int t, int s, int tt) { xf[b][tt] = lds_ld128(xa[2 * (t & 1) + s] + ((t >> 1) % 3) * K1_XS + tt * 4096); };
    auto rd_q = [&](int b, int t, int s, int ct) {
        if constexpr (MODE == 2) { if (ct == 0) qf[b][0] = lds_ld128(ga + (2 * t + s) * gstep); }
        else qf[b][ct] = cat8(lds_tr(qa[ct] + (t % 5) * K1_WS + s * 4096), lds_tr(qa[ct] + (t % 5) * K1_WS + s * 4096 + 1024));
    };
    auto rd_f = [&](int b, int t, int s, int a) { ff[b][a] = cat8(lds_tr(fa[a] + (t % 5) * K1_WS + s * 3072), lds_tr(fa[a] + (t % 5) * K1_WS + s * 3072 + 768)); };
    asm volatile("s_waitcnt lgkmcnt(0)\n\ts_barrier" ::: "memory");
    STAMP(1);
    rd_x(0, 0, 0, 0); rd_x(0, 0, 0, 1); rd_q(0, 0, 0, 0); rd_q(0, 0, 0, 1); rd_f(0, 0, 0, 0); rd_f(0, 0, 0, 1); rd_f(0, 0, 0, 2);
    auto stage = [&](auto tc) {
        constexpr int t = decltype(tc)::value;
        if constexpr (t < 19) asm volatile("s_barrier" ::: "memory");
#pragma unroll
        for (int s = 0; s < 2; ++s) {
            const int c = s, nb = s ^ 1;
            const bool more = s == 0 || t < 19;
            const int tn = s == 0 ? t : t + 1, sn = s ^ 1;
            auto mq = [&](int ct, int tt) {
                if constexpr (MODE == 0) aq[ct][tt] = mfma16(qf[c][ct], xf[c][tt], aq[ct][tt]);
                else if constexpr (MODE == 1) aq[tt][ct] = mfma16(xf[c][tt], qf[c][ct], aq[tt][ct]);
                else { if (ct == 0) aq[0][tt] = mfma16(qf[c][0], xf[c][tt], aq[0][tt]); }
            };
            auto mf = [&](int a, int tt) { af[a][tt] = mfma16(ff[c][a], xf[c][tt], af[a][tt]); };
            mq(0, 0); SBAR(); if (more) { rd_x(nb, tn, sn, 0); rd_x(nb, tn, sn, 1); } SBAR();
            mq(0, 1); SBAR(); if (more) rd_q(nb, tn, sn, 0); SBAR();
            mq(1, 0); SBAR(); if (more) rd_q(nb, tn, sn, 1); SBAR();
            mq(1, 1); SBAR(); if (more) rd_f(nb, tn, sn, 0); SBAR();
            mf(0, 0); SBAR(); if (more) rd_f(nb, tn, sn, 1); SBAR();
            mf(0, 1); SBAR(); if (more) rd_f(nb, tn, sn, 2); SBAR();
            mf(1, 0); SBAR(); mf(1, 1); SBAR(); mf(2, 0); SBAR(); mf(2, 1); SBAR();
        }
    };
    static_for<0, 20>(stage);
    STAMP(2);
    if constexpr (MODE == 0) {
        const bool isq = j < 5;
        const int csec = (isq ? n0 : n0 - 640) + ww * 64;
        char* base = ws + (isq ? WS_QF : WS_KV);
        const int tstride = isq ? 1280 : 3072;
        const float sc = isq ? QSCALE : 1.0f;
#pragma unroll
        for (int ct = 0; ct < 2; ++ct)
#pragma unroll
            for (int g = 0; g < 4; ++g) {
                const int qd = (csec + ct * 32 + 8 * g + 4 * h) >> 2, head = qd / 5, dq = qd - head * 5;
                const int inoff = dq < 4 ? (dq >> 1) * 512 + r * 16 + (dq & 1) * 8 : 1024 + r * 8;
#pragma unroll
                for (int tt = 0; tt < 2; ++tt) {
                    const int tile = mt * 4 + wt * 2 + tt;
                    char* p = base + (size_t)(head * 64 + tile) * tstride + inoff;
                    const f32x16& a = aq[ct][tt];
                    u32x2 v = {pk2(a[4 * g] * sc, a[4 * g + 1] * sc), pk2(a[4 * g + 2] * sc, a[4 * g + 3] * sc)};
                    *(u32x2*)p = v;
                    if (!isq && dq == 4) *(u32x2*)(p + 256) = (u32x2){0x00003f80u, 0u};
                }
            }
    } else if constexpr (MODE == 1) {
#pragma unroll
        for (int ct = 0; ct < 2; ++ct) {
            const int col = n0 - 1280 + ww * 64 + ct * 32 + r, head = col / 20, d = col - head * 20;
#pragma unroll
            for (int tt = 0; tt < 2; ++tt) {
                const int tile = mt * 4 + wt * 2 + tt;
                char* p = ws + WS_KV + (size_t)(head * 64 + tile) * 3072 + 1536 + h * 384 + d * 16;
#pragma unroll
                for (int s = 0; s < 2; ++s) {
                    *(bf16x8*)(p + s * 768) = pack8(aq[tt][ct], 8 * s);
                    if (d == 0) *(u32x4*)(p + s * 768 + 320) = (u32x4){0x3f803f80u, 0x3f803f80u, 0x3f803f80u, 0x3f803f80u};
                    if (d == 1) *(u32x4*)(p + s * 768 + 320) = (u32x4){0u, 0u, 0u, 0u};
                }
            }
        }
    } else {
        if (ww == 0 && h == 0) {
#pragma unroll
            for (int tt = 0; tt < 2; ++tt) ((float*)(ws + WS_G))[m0 + wt * 64 + tt * 32 + r] = __builtin_amdgcn_rcpf(1.0f + __builtin_amdgcn_exp2f(-1.4426950408889634f * aq[0][tt][0]));
        }
        if (mt == 0 && tid < FLAG_LINES) *(unsigned*)(ws + WS_FLAG + tid * 256) = 0u;
    }
#pragma unroll
    for (int a = 0; a < 3; ++a)
#pragma unroll
        for (int tt = 0; tt < 2; ++tt) {
            f32x16 hv;
#pragma unroll
            for (int i = 0; i < 8; ++i) hv[i] = gelu_tanh(af[a][tt][i]) * af[a][tt][8 + i];
            const int tile = mt * 4 + wt * 2 + tt, fs = j * 6 + ww * 3 + a;
            *(bf16x8*)(ws + WS_HF + (size_t)(tile * 96 + fs) * 1024 + lane * 16) = pack8(hv, 0);
        }
    STAMP(3);
}

__global__ __launch_bounds__(512, 2) void k1_kernel(const char* __restrict__ x, const char* __restrict__ wqkv, const char* __restrict__ gw, const char* __restrict__ wg, const char* __restrict__ wu,
                                                   const char* __restrict__ wd, const char* __restrict__ wo, char* __restrict__ ws) {
    extern __shared__ __attribute__((aligned(16))) char lds_raw[];
    lds_ptr lds = (lds_ptr)lds_raw;
    const int b = blockIdx.x, xcd = b & 7, jj = b >> 3;
    const int mt = (xcd & 1) * 8 + (jj & 7), j = (xcd >> 1) * 4 + (jj >> 3);
    if (j < 10) k1_gemm<0>(x, wqkv, gw, wg, wu, wd, wo, ws, mt, j, lds);
    else if (j < 15) k1_gemm<1>(x, wqkv, gw, wg, wu, wd, wo, ws, mt, j, lds);
    else k1_gemm<2>(x, wqkv, gw, wg, wu, wd, wo, ws, mt, j, lds);
}

constexpr int AT_CHUNK = 24576;

DEV void attn_slow(const char* kvh, bf16x8 q1, s16x4 q2, int lane, f32x16& o) {
    const int r = lane & 31, h = lane >> 5;
    const int vrow = r < 21 ? r : 21;
    float m = -INFINITY;
    o = f32x16{};
    for (int t = 0; t < 64; ++t) {
        const char* p = kvh + t * 3072;
        const bf16x8 k1 = *(const bf16x8*)(p + lane * 16);
        const s16x4 k2 = *(const s16x4*)(p + 1024 + lane * 8);
        const bf16x8 v0 = *(const bf16x8*)(p + 1536 + h * 384 + vrow * 16);
        const bf16x8 v1 = *(const bf16x8*)(p + 2304 + h * 384 + vrow * 16);
        f32x16 s = mfma16(k1, q1, f32x16{});
        s = mfma8(k2, q2, s);
        float tm = s[0];
#pragma unroll
        for (int i = 1; i < 16; ++i) tm = fmaxf(tm, s[i]);
        tm = fmaxf(tm, __shfl_xor(tm, 32));
        const float mn = fmaxf(m, tm);
        const float alpha = __builtin_amdgcn_exp2f(m - mn);
        m = mn;
#pragma unroll
        for (int i = 0; i < 16; ++i) { s[i] = __builtin_amdgcn_exp2f(s[i] - mn); o[i] *= alpha; }
        o = mfma16(v0, pack8(s, 0), o);
        o = mfma16(v1, pack8(s, 8), o);
    }
}

#define ATT_LOOP_ASM \
    "ds_read_b128 v[72:75], %1\n\t" \
    "ds_read_b64 v[80:81], %2\n\t" \
    "ds_read_b128 v[76:79], %1 offset:3072\n\t" \
    "ds_read_b64 v[82:83], %2 offset:3072\n\t" \
    "ds_read_b128 v[84:87], %3\n\t" \
    "ds_read_b128 v[88:91], %3 offset:768\n\t" \
    "ds_read_b128 v[92:95], %3 offset:3072\n\t" \
    "ds_read_b128 v[96:99], %3 offset:3840\n\t" \
    "s_waitcnt lgkmcnt(6)\n\t" \
    "v_mfma_f32_32x32x16_bf16 v[32:47], v[72:75], %8, 0\n\t" \
    "v_mfma_f32_32x32x8_bf16 v[32:47], v[80:81], %9, v[32:47]\n\t" \
    "s_nop 15\n\t" \
    "1:\n\t" \
    "s_waitcnt vmcnt(3) lgkmcnt(0)\n\t" \
    "s_barrier\n\t" \
    "v_add_u32 %1, %6, %12\n\t" \
    "v_add_u32 %2, %6, %13\n\t" \
    "v_add_u32 %3, %6, %14\n\t" \
    "s_sub_u32 %5, %6, 0x6000\n\t" \
    "s_cmp_eq_u32 %6, 0\n\t" \
    "s_cselect_b32 %5, 0xc000, %5\n\t" \
    "s_add_u32 %5, %5, %15\n\t" \
    "s_setprio 3\n\t" \
    "v_mfma_f32_32x32x16_bf16 v[48:63], v[76:79], %8, 0\n\t" \
    "s_setprio 0\n\t" \
    "v_exp_f32 v32, v32\n\t" \
    "ds_read_b128 v[72:75], %1 offset:0\n\t" \
    "v_exp_f32 v33, v33\n\t" \
    "ds_read_b64 v[80:81], %2 offset:0\n\t" \
    "v_exp_f32 v34, v34\n\t" \
    "v_exp_f32 v35, v35\n\t" \
    "v_exp_f32 v36, v36\n\t" \
    "s_mov_b32 m0, %5\n\t" \
    "v_exp_f32 v37, v37\n\t" \
    "s_setprio 3\n\t" \
    "buffer_load_dwordx4 %10, %11, %4 offen offset:0 lds\n\t" \
    "s_setprio 0\n\t" \
    "v_exp_f32 v38, v38\n\t" \
    "v_exp_f32 v39, v39\n\t" \
    "s_setprio 3\n\t" \
    "v_mfma_f32_32x32x8_bf16 v[48:63], v[82:83], %9, v[48:63]\n\t" \
    "s_setprio 0\n\t" \
    "v_cvt_pk_bf16_f32 v64, v32, v33\n\t" \
    "v_cvt_pk_bf16_f32 v65, v34, v35\n\t" \
    "v_cvt_pk_bf16_f32 v66, v36, v37\n\t" \
    "v_cvt_pk_bf16_f32 v67, v38, v39\n\t" \
    "v_exp_f32 v40, v40\n\t" \
    "v_exp_f32 v41, v41\n\t" \
    "v_exp_f32 v42, v42\n\t" \
    "v_exp_f32 v43, v43\n\t" \
    "s_setprio 3\n\t" \
    "v_mfma_f32_32x32x16_bf16 %0, v[84:87], v[64:67], %0\n\t" \
    "s_setprio 0\n\t" \
    "ds_read_b128 v[84:87], %3 offset:0\n\t" \
    "v_exp_f32 v44, v44\n\t" \
    "v_exp_f32 v45, v45\n\t" \
    "v_exp_f32 v46, v46\n\t" \
    "v_exp_f32 v47, v47\n\t" \
    "v_cvt_pk_bf16_f32 v68, v40, v41\n\t" \
    "v_cvt_pk_bf16_f32 v69, v42, v43\n\t" \
    "v_cvt_pk_bf16_f32 v70, v44, v45\n\t" \
    "v_cvt_pk_bf16_f32 v71, v46, v47\n\t" \
    "s_waitcnt lgkmcnt(1)\n\t" \
    "s_setprio 3\n\t" \
    "v_mfma_f32_32x32x16_bf16 v[32:47], v[72:75], %8, 0\n\t" \
    "s_setprio 0\n\t" \
    "v_exp_f32 v48, v48\n\t" \
    "ds_read_b128 v[76:79], %1 offset:3072\n\t" \
    "v_exp_f32 v49, v49\n\t" \
    "ds_read_b64 v[82:83], %2 offset:3072\n\t" \
    "v_exp_f32 v50, v50\n\t" \
    "v_exp_f32 v51, v51\n\t" \
    "s_setprio 3\n\t" \
    "v_mfma_f32_32x32x16_bf16 %0, v[88:91], v[68:71], %0\n\t" \
    "s_setprio 0\n\t" \
    "ds_read_b128 v[88:91], %3 offset:768\n\t" \
    "v_exp_f32 v52, v52\n\t" \
    "v_exp_f32 v53, v53\n\t" \
    "s_setprio 3\n\t" \
    "buffer_load_dwordx4 %10, %11, %4 offen offset:1024 lds\n\t" \
    "s_setprio 0\n\t" \
    "v_exp_f32 v54, v54\n\t" \
    "v_exp_f32 v55, v55\n\t" \
    "s_setprio 3\n\t" \
    "v_mfma_f32_32x32x8_bf16 v[32:47], v[80:81], %9, v[32:47]\n\t" \
    "s_setprio 0\n\t" \
    "v_cvt_pk_bf16_f32 v64, v48, v49\n\t" \
    "v_cvt_pk_bf16_f32 v65, v50, v51\n\t" \
    "v_cvt_pk_bf16_f32 v66, v52, v53\n\t" \
    "v_cvt_pk_bf16_f32 v67, v54, v55\n\t" \
    "v_exp_f32 v56, v56\n\t" \
    "v_exp_f32 v57, v57\n\t" \
    "v_exp_f32 v58, v58\n\t" \
    "v_exp_f32 v59, v59\n\t" \
    "s_setprio 3\n\t" \
    "v_mfma_f32_32x32x16_bf16 %0, v[92:95], v[64:67], %0\n\t" \
    "s_setprio 0\n\t" \
    "ds_read_b128 v[92:95], %3 offset:3072\n\t" \
    "v_exp_f32 v60, v60\n\t" \
    "v_exp_f32 v61, v61\n\t" \
    "v_exp_f32 v62, v62\n\t" \
    "v_exp_f32 v63, v63\n\t" \
    "v_cvt_pk_bf16_f32 v68, v56, v57\n\t" \
    "v_cvt_pk_bf16_f32 v69, v58, v59\n\t" \
    "v_cvt_pk_bf16_f32 v70, v60, v61\n\t" \
    "v_cvt_pk_bf16_f32 v71, v62, v63\n\t" \
    "s_waitcnt lgkmcnt(1)\n\t" \
    "s_setprio 3\n\t" \
    "v_mfma_f32_32x32x16_bf16 v[48:63], v[76:79], %8, 0\n\t" \
    "s_setprio 0\n\t" \
    "v_exp_f32 v32, v32\n\t" \
    "ds_read_b128 v[72:75], %1 offset:6144\n\t" \
    "v_exp_f32 v33, v33\n\t" \
    "ds_read_b64 v[80:81], %2 offset:6144\n\t" \
    "v_exp_f32 v34, v34\n\t" \
    "v_exp_f32 v35, v35\n\t" \
    "s_setprio 3\n\t" \
    "v_mfma_f32_32x32x16_bf16 %0, v[96:99], v[68:71], %0\n\t" \
    "s_setprio 0\n\t" \
    "ds_read_b128 v[96:99], %3 offset:3840\n\t" \
    "v_exp_f32 v36, v36\n\t" \
    "v_exp_f32 v37, v37\n\t" \
    "s_setprio 3\n\t" \
    "buffer_load_dwordx4 %10, %11, %4 offen offset:2048 lds\n\t" \
    "s_setprio 0\n\t" \
    "v_exp_f32 v38, v38\n\t" \
    "v_exp_f32 v39, v39\n\t" \
    "s_setprio 3\n\t" \
    "v_mfma_f32_32x32x8_bf16 v[48:63], v[82:83], %9, v[48:63]\n\t" \
    "s_setprio 0\n\t" \
    "v_cvt_pk_bf16_f32 v64, v32, v33\n\t" \
    "v_cvt_pk_bf16_f32 v65, v34, v35\n\t" \
    "v_cvt_pk_bf16_f32 v66, v36, v37\n\t" \
    "v_cvt_pk_bf16_f32 v67, v38, v39\n\t" \
    "v_exp_f32 v40, v40\n\t" \
    "v_exp_f32 v41, v41\n\t" \
    "v_exp_f32 v42, v42\n\t" \
    "v_exp_f32 v43, v43\n\t" \
    "s_setprio 3\n\t" \
    "v_mfma_f32_32x32x16_bf16 %0, v[84:87], v[64:67], %0\n\t" \
    "s_setprio 0\n\t" \
    "ds_read_b128 v[84:87], %3 offset:6144\n\t" \
    "v_exp_f32 v44, v44\n\t" \
    "v_exp_f32 v45, v45\n\t" \
    "v_exp_f32 v46, v46\n\t" \
    "v_exp_f32 v47, v47\n\t" \
    "v_cvt_pk_bf16_f32 v68, v40, v41\n\t" \
    "v_cvt_pk_bf16_f32 v69, v42, v43\n\t" \
    "v_cvt_pk_bf16_f32 v70, v44, v45\n\t" \
    "v_cvt_pk_bf16_f32 v71, v46, v47\n\t" \
    "s_waitcnt lgkmcnt(1)\n\t" \
    "s_setprio 3\n\t" \
    "v_mfma_f32_32x32x16_bf16 v[32:47], v[72:75], %8, 0\n\t" \
    "s_setprio 0\n\t" \
    "v_exp_f32 v48, v48\n\t" \
    "ds_read_b128 v[76:79], %1 offset:9216\n\t" \
    "v_exp_f32 v49, v49\n\t" \
    "ds_read_b64 v[82:83], %2 offset:9216\n\t" \
    "v_exp_f32 v50, v50\n\t" \
    "v_exp_f32 v51, v51\n\t" \
    "s_setprio 3\n\t" \
    "v_mfma_f32_32x32x16_bf16 %0, v[88:91], v[68:71], %0\n\t" \
    "s_setprio 0\n\t" \
    "ds_read_b128 v[88:91], %3 offset:6912\n\t" \
    "v_exp_f32 v52, v52\n\t" \
    "v_exp_f32 v53, v53\n\t" \
    "v_exp_f32 v54, v54\n\t" \
    "v_exp_f32 v55, v55\n\t" \
    "s_setprio 3\n\t" \
    "v_mfma_f32_32x32x8_bf16 v[32:47], v[80:81], %9, v[32:47]\n\t" \
    "s_setprio 0\n\t" \
    "v_cvt_pk_bf16_f32 v64, v48, v49\n\t" \
    "v_cvt_pk_bf16_f32 v65, v50, v51\n\t" \
    "v_cvt_pk_bf16_f32 v66, v52, v53\n\t" \
    "v_cvt_pk_bf16_f32 v67, v54, v55\n\t" \
    "v_exp_f32 v56, v56\n\t" \
    "v_exp_f32 v57, v57\n\t" \
    "v_exp_f32 v58, v58\n\t" \
    "v_exp_f32 v59, v59\n\t" \
    "s_setprio 3\n\t" \
    "v_mfma_f32_32x32x16_bf16 %0, v[92:95], v[64:67], %0\n\t" \
    "s_setprio 0\n\t" \
    "ds_read_b128 v[92:95], %3 offset:9216\n\t" \
    "v_exp_f32 v60, v60\n\t" \
    "v_exp_f32 v61, v61\n\t" \
    "v_exp_f32 v62, v62\n\t" \
    "v_exp_f32 v63, v63\n\t" \
    "v_cvt_pk_bf16_f32 v68, v56, v57\n\t" \
    "v_cvt_pk_bf16_f32 v69, v58, v59\n\t" \
    "v_cvt_pk_bf16_f32 v70, v60, v61\n\t" \
    "v_cvt_pk_bf16_f32 v71, v62, v63\n\t" \
    "s_waitcnt lgkmcnt(1)\n\t" \
    "s_setprio 3\n\t" \
    "v_mfma_f32_32x32x16_bf16 v[48:63], v[76:79], %8, 0\n\t" \
    "s_setprio 0\n\t" \
    "v_exp_f32 v32, v32\n\t" \
    "ds_read_b128 v[72:75], %1 offset:12288\n\t" \
    "v_exp_f32 v33, v33\n\t" \
    "ds_read_b64 v[80:81], %2 offset:12288\n\t" \
    "v_exp_f32 v34, v34\n\t" \
    "v_exp_f32 v35, v35\n\t" \
    "s_setprio 3\n\t" \
    "v_mfma_f32_32x32x16_bf16 %0, v[96:99], v[68:71], %0\n\t" \
    "s_setprio 0\n\t" \
    "ds_read_b128 v[96:99], %3 offset:9984\n\t" \
    "v_exp_f32 v36, v36\n\t" \
    "v_exp_f32 v37, v37\n\t" \
    "v_exp_f32 v38, v38\n\t" \
    "v_exp_f32 v39, v39\n\t" \
    "s_setprio 3\n\t" \
    "v_mfma_f32_32x32x8_bf16 v[48:63], v[82:83], %9, v[48:63]\n\t" \
    "s_setprio 0\n\t" \
    "v_cvt_pk_bf16_f32 v64, v32, v33\n\t" \
    "v_cvt_pk_bf16_f32 v65, v34, v35\n\t" \
    "v_cvt_pk_bf16_f32 v66, v36, v37\n\t" \
    "v_cvt_pk_bf16_f32 v67, v38, v39\n\t" \
    "v_exp_f32 v40, v40\n\t" \
    "v_exp_f32 v41, v41\n\t" \
    "v_exp_f32 v42, v42\n\t" \
    "v_exp_f32 v43, v43\n\t" \
    "s_setprio 3\n\t" \
    "v_mfma_f32_32x32x16_bf16 %0, v[84:87], v[64:67], %0\n\t" \
    "s_setprio 0\n\t" \
    "ds_read_b128 v[84:87], %3 offset:12288\n\t" \
    "v_exp_f32 v44, v44\n\t" \
    "v_exp_f32 v45, v45\n\t" \
    "v_exp_f32 v46, v46\n\t" \
    "v_exp_f32 v47, v47\n\t" \
    "v_cvt_pk_bf16_f32 v68, v40, v41\n\t" \
    "v_cvt_pk_bf16_f32 v69, v42, v43\n\t" \
    "v_cvt_pk_bf16_f32 v70, v44, v45\n\t" \
    "v_cvt_pk_bf16_f32 v71, v46, v47\n\t" \
    "s_waitcnt lgkmcnt(1)\n\t" \
    "s_setprio 3\n\t" \
    "v_mfma_f32_32x32x16_bf16 v[32:47], v[72:75], %8, 0\n\t" \
    "s_setprio 0\n\t" \
    "v_exp_f32 v48, v48\n\t" \
    "ds_read_b128 v[76:79], %1 offset:15360\n\t" \
    "v_exp_f32 v49, v49\n\t" \
    "ds_read_b64 v[82:83], %2 offset:15360\n\t" \
    "v_exp_f32 v50, v50\n\t" \
    "v_exp_f32 v51, v51\n\t" \
    "s_setprio 3\n\t" \
    "v_mfma_f32_32x32x16_bf16 %0, v[88:91], v[68:71], %0\n\t" \
    "s_setprio 0\n\t" \
    "ds_read_b128 v[88:91], %3 offset:13056\n\t" \
    "v_exp_f32 v52, v52\n\t" \
    "v_exp_f32 v53, v53\n\t" \
    "v_exp_f32 v54, v54\n\t" \
    "v_exp_f32 v55, v55\n\t" \
    "s_setprio 3\n\t" \
    "v_mfma_f32_32x32x8_bf16 v[32:47], v[80:81], %9, v[32:47]\n\t" \
    "s_setprio 0\n\t" \
    "v_cvt_pk_bf16_f32 v64, v48, v49\n\t" \
    "v_cvt_pk_bf16_f32 v65, v50, v51\n\t" \
    "v_cvt_pk_bf16_f32 v66, v52, v53\n\t" \
    "v_cvt_pk_bf16_f32 v67, v54, v55\n\t" \
    "v_exp_f32 v56, v56\n\t" \
    "v_exp_f32 v57, v57\n\t" \
    "v_exp_f32 v58, v58\n\t" \
    "v_exp_f32 v59, v59\n\t" \
    "s_setprio 3\n\t" \
    "v_mfma_f32_32x32x16_bf16 %0, v[92:95], v[64:67], %0\n\t" \
    "s_setprio 0\n\t" \
    "ds_read_b128 v[92:95], %3 offset:15360\n\t" \
    "v_exp_f32 v60, v60\n\t" \
    "v_exp_f32 v61, v61\n\t" \
    "v_exp_f32 v62, v62\n\t" \
    "v_exp_f32 v63, v63\n\t" \
    "v_cvt_pk_bf16_f32 v68, v56, v57\n\t" \
    "v_cvt_pk_bf16_f32 v69, v58, v59\n\t" \
    "v_cvt_pk_bf16_f32 v70, v60, v61\n\t" \
    "v_cvt_pk_bf16_f32 v71, v62, v63\n\t" \
    "s_waitcnt lgkmcnt(1)\n\t" \
    "s_setprio 3\n\t" \
    "v_mfma_f32_32x32x16_bf16 v[48:63], v[76:79], %8, 0\n\t" \
    "s_setprio 0\n\t" \
    "v_exp_f32 v32, v32\n\t" \
    "ds_read_b128 v[72:75], %1 offset:18432\n\t" \
    "v_exp_f32 v33, v33\n\t" \
    "ds_read_b64 v[80:81], %2 offset:18432\n\t" \
    "v_exp_f32 v34, v34\n\t" \
    "v_exp_f32 v35, v35\n\t" \
    "s_setprio 3\n\t" \
    "v_mfma_f32_32x32x16_bf16 %0, v[96:99], v[68:71], %0\n\t" \
    "s_setprio 0\n\t" \
    "ds_read_b128 v[96:99], %3 offset:16128\n\t" \
    "v_exp_f32 v36, v36\n\t" \
    "v_exp_f32 v37, v37\n\t" \
    "v_exp_f32 v38, v38\n\t" \
    "v_exp_f32 v39, v39\n\t" \
    "s_setprio 3\n\t" \
    "v_mfma_f32_32x32x8_bf16 v[48:63], v[82:83], %9, v[48:63]\n\t" \
    "s_setprio 0\n\t" \
    "v_cvt_pk_bf16_f32 v64, v32, v33\n\t" \
    "v_cvt_pk_bf16_f32 v65, v34, v35\n\t" \
    "v_cvt_pk_bf16_f32 v66, v36, v37\n\t" \
    "v_cvt_pk_bf16_f32 v67, v38, v39\n\t" \
    "v_exp_f32 v40, v40\n\t" \
    "v_exp_f32 v41, v41\n\t" \
    "v_exp_f32 v42, v42\n\t" \
    "v_exp_f32 v43, v43\n\t" \
    "s_setprio 3\n\t" \
    "v_mfma_f32_32x32x16_bf16 %0, v[84:87], v[64:67], %0\n\t" \
    "s_setprio 0\n\t" \
    "ds_read_b128 v[84:87], %3 offset:18432\n\t" \
    "v_exp_f32 v44, v44\n\t" \
    "v_exp_f32 v45, v45\n\t" \
    "v_exp_f32 v46, v46\n\t" \
    "v_exp_f32 v47, v47\n\t" \
    "v_cvt_pk_bf16_f32 v68, v40, v41\n\t" \
    "v_cvt_pk_bf16_f32 v69, v42, v43\n\t" \
    "v_cvt_pk_bf16_f32 v70, v44, v45\n\t" \
    "v_cvt_pk_bf16_f32 v71, v46, v47\n\t" \
    "s_waitcnt lgkmcnt(1)\n\t" \
    "s_setprio 3\n\t" \
    "v_mfma_f32_32x32x16_bf16 v[32:47], v[72:75], %8, 0\n\t" \
    "s_setprio 0\n\t" \
    "v_exp_f32 v48, v48\n\t" \
    "ds_read_b128 v[76:79], %1 offset:21504\n\t" \
    "v_exp_f32 v49, v49\n\t" \
    "ds_read_b64 v[82:83], %2 offset:21504\n\t" \
    "v_exp_f32 v50, v50\n\t" \
    "v_exp_f32 v51, v51\n\t" \
    "s_setprio 3\n\t" \
    "v_mfma_f32_32x32x16_bf16 %0, v[88:91], v[68:71], %0\n\t" \
    "s_setprio 0\n\t" \
    "ds_read_b128 v[88:91], %3 offset:19200\n\t" \
    "v_exp_f32 v52, v52\n\t" \
    "v_exp_f32 v53, v53\n\t" \
    "v_exp_f32 v54, v54\n\t" \
    "v_exp_f32 v55, v55\n\t" \
    "s_setprio 3\n\t" \
    "v_mfma_f32_32x32x8_bf16 v[32:47], v[80:81], %9, v[32:47]\n\t" \
    "s_setprio 0\n\t" \
    "v_cvt_pk_bf16_f32 v64, v48, v49\n\t" \
    "v_cvt_pk_bf16_f32 v65, v50, v51\n\t" \
    "v_cvt_pk_bf16_f32 v66, v52, v53\n\t" \
    "v_cvt_pk_bf16_f32 v67, v54, v55\n\t" \
    "v_exp_f32 v56, v56\n\t" \
    "v_exp_f32 v57, v57\n\t" \
    "v_exp_f32 v58, v58\n\t" \
    "v_exp_f32 v59, v59\n\t" \
    "s_setprio 3\n\t" \
    "v_mfma_f32_32x32x16_bf16 %0, v[92:95], v[64:67], %0\n\t" \
    "s_setprio 0\n\t" \
    "ds_read_b128 v[92:95], %3 offset:21504\n\t" \
    "v_exp_f32 v60, v60\n\t" \
    "v_exp_f32 v61, v61\n\t" \
    "v_exp_f32 v62, v62\n\t" \
    "v_exp_f32 v63, v63\n\t" \
    "v_cvt_pk_bf16_f32 v68, v56, v57\n\t" \
    "v_cvt_pk_bf16_f32 v69, v58, v59\n\t" \
    "v_cvt_pk_bf16_f32 v70, v60, v61\n\t" \
    "v_cvt_pk_bf16_f32 v71, v62, v63\n\t" \
    "s_nop 1\n\t" \
    "s_setprio 3\n\t" \
    "v_mfma_f32_32x32x16_bf16 %0, v[96:99], v[68:71], %0\n\t" \
    "s_setprio 0\n\t" \
    "ds_read_b128 v[96:99], %3 offset:22272\n\t" \
    "s_add_u32 %4, %4, 0x6000\n\t" \
    "s_add_u32 %6, %6, 0x6000\n\t" \
    "s_cmp_eq_u32 %6, 0x12000\n\t" \
    "s_cselect_b32 %6, 0, %6\n\t" \
    "s_sub_u32 %7, %7, 1\n\t" \
    "s_cmp_lg_u32 %7, 0\n\t" \
    "s_cbranch_scc1 1b\n\t" \
    "s_waitcnt vmcnt(0) lgkmcnt(0)\n\t" \
    "s_nop 15"
#define ATT_CLOBBERS "v32", "v33", "v34", "v35", "v36", "v37", "v38", "v39", "v40", "v41", "v42", "v43", "v44", "v45", "v46", "v47", "v48", "v49", "v50", "v51", "v52", "v53", "v54", "v55", "v56", "v57", "v58", "v59", "v60", "v61", "v62", "v63", "v64", "v65", "v66", "v67", "v68", "v69", "v70", "v71", "v72", "v73", "v74", "v75", "v76", "v77", "v78", "v79", "v80", "v81", "v82", "v83", "v84", "v85", "v86", "v87", "v88", "v89", "v90", "v91", "v92", "v93", "v94", "v95", "v96", "v97", "v98", "v99"
DEV void attn_role(char* ws, int item, lds_ptr lds) {
    constexpr int SK = 1;
    const int tid = threadIdx.x, lane = tid & 63, w = __builtin_amdgcn_readfirstlane(tid >> 6);
    const int r = lane & 31, h = lane >> 5;
    const int head = (item & 7) * 4 + (item >> 6), qb = (item >> 3) & 7;
    const char* kvh = ws + WS_KV + (size_t)head * 64 * 3072;
    i32x4 skv = make_srd(kvh);
    skv[2] = 64 * 3072;
    const unsigned l0 = (unsigned)(uintptr_t)lds;
    const unsigned vo = lane * 16;
    STAMP(0);
    __builtin_amdgcn_s_setprio(3);
    if (w < 6) dma16(skv, vo, (unsigned)w * 1024u, l0 + 3 * AT_CHUNK + (unsigned)w * 1024u);
#pragma unroll
    for (int c = 0; c < 2; ++c)
#pragma unroll
        for (int j = 0; j < 3; ++j) dma16(skv, vo, (unsigned)(6144 + c * AT_CHUNK + (3 * w + j) * 1024), l0 + (unsigned)(c * AT_CHUNK + (3 * w + j) * 1024));
    const int tile = qb * 8 + w;
    const char* qp = ws + WS_QF + (size_t)(head * 64 + tile) * 1280;
    const bf16x8 q1 = *(const bf16x8*)(qp + lane * 16);
    s16x4 q2 = *(const s16x4*)(qp + 1024 + r * 8);
    if (h) q2 = (s16x4){0, 0, 0, 0};
    const int vrow = r < 21 ? r : 21;
    const unsigned kbase = l0 + lane * 16, k2base = l0 + 1024 + lane * 8, vbase = l0 + 1536 + h * 384 + vrow * 16;
    WAIT_VM_BAR(6);
    STAMP(1);
    {
        const bf16x8 k1 = lds_ld128((lds_cptr)lds + 3 * AT_CHUNK + lane * 16); const s16x4 k2 = lds_ld64((lds_cptr)lds + 3 * AT_CHUNK + 1024 + lane * 8);
        f32x16 s = mfma16(k1, q1, f32x16{});
        s = mfma8(k2, q2, s);
        float tm = s[0];
#pragma unroll
        for (int i = 1; i < 16; ++i) tm = fmaxf(tm, s[i]);
        tm = fmaxf(tm, __shfl_xor(tm, 32));
        const unsigned nb = pk2(-tm, 0.f) & 0xffffu;
        if (h) q2[0] = (short)nb;
    }
    const float gq = ((const float*)(ws + WS_G))[tile * 32 + r];
    asm volatile("" :: "v"(gq));
    f32x16 o = f32x16{};
    __builtin_amdgcn_s_setprio(A_PRIO);
    STAMP(2);
    {
        unsigned ka = kbase + 3 * AT_CHUNK, kb = k2base + 3 * AT_CHUNK, va = vbase + 3 * AT_CHUNK;
        unsigned soff = 6144 + 2 * AT_CHUNK + w * 3072, ldst, cur = 0, cnt = 8;
        const unsigned wbase = l0 + w * 3072;
        asm volatile(ATT_LOOP_ASM
                     : "+v"(o), "+v"(ka), "+v"(kb), "+v"(va), "+s"(soff), "=&s"(ldst), "+s"(cur), "+s"(cnt)
                     : "v"(q1), "v"(q2), "v"(vo), "s"(skv), "v"(kbase), "v"(k2base), "v"(vbase), "s"(wbase)
                     : "memory", "scc", ATT_CLOBBERS);
    }
    STAMP(3);
    float l = __shfl(o[8], r + 32);
    bool ok = (l > 9.094947e-13f) && (l < 1.8446744e19f);
#pragma unroll
    for (int i = 0; i < 12; ++i) ok = ok && (fabsf(o[i]) < INFINITY || (h && i >= 8));
    if (!__all(ok)) {
        if (h) q2[0] = 0;
        attn_slow(kvh, q1, q2, lane, o);
        l = __shfl(o[8], r + 32);
    }
    const float sc = gq * __builtin_amdgcn_rcpf(l);
    const __amdgpu_buffer_rsrc_t ro = make_rsrc(ws + WS_OF);
    const unsigned ob = (unsigned)tile * 40u * 1024u;
    u32x4 m = {pk2(o[0] * sc, o[1] * sc), pk2(o[2] * sc, o[3] * sc), pk2(o[4] * sc, o[5] * sc), pk2(o[6] * sc, o[7] * sc)};
    st16_sc1(ro, ob + (unsigned)(head * 1024 + lane * 16), m);
    if (h == 0) {
        u32x2 e = {pk2(o[8] * sc, o[9] * sc), pk2(o[10] * sc, o[11] * sc)};
        st8_sc1(ro, ob + (unsigned)((32 + (head >> 2)) * 1024 + (((head >> 1) & 1) * 32 + r) * 16 + (head & 1) * 8), e);
    }
    publish(ws, qb);
    STAMP(4);
}

DEV void bufld16(bf16x8& dst, const i32x4& srd, unsigned voff, unsigned soff) {
    asm volatile(PADS "buffer_load_dwordx4 %0, %1, %2, %3 offen" : "=v"(dst) : "v"(voff), "s"(srd), "s"(RFL(soff)) : "memory");
}
DEV void bufld16_o(bf16x8& dst, const i32x4& srd, unsigned voff, unsigned soff) {
    asm volatile(PADS "buffer_load_dwordx4 %0, %1, %2, %3 offen sc1" : "=v"(dst) : "v"(voff), "s"(srd), "s"(RFL(soff)) : "memory");
}
#define WAIT_VM_2(n, a, b) asm volatile("s_waitcnt vmcnt(" #n ")" : "+v"(a), "+v"(b) :: "memory")

constexpr int P2_NF = 24, P2_NS = 34, P2_PD = 3, P2_AD = 6;
constexpr int p2_allowed(int s, bool up) {
    int kind[256] = {}, idx[256] = {}, n = 0;
    const int nw = up ? 2 : 3;
    auto A = [&](int k) { kind[n] = 0; idx[n] = k; ++n; };
    auto W = [&](int k) { for (int i = 0; i < nw; ++i) { kind[n] = 1; idx[n] = k; ++n; } };
    for (int k = 0; k < P2_PD; ++k) W(k);
    for (int k = 0; k < P2_AD; ++k) A(k);
    for (int u = 0; u <= s; ++u) {
        if (u == P2_NF) for (int k = P2_NF; k < P2_NF + P2_AD; ++k) A(k);
        if (u == s) break;
        if (u + P2_PD < P2_NS) W(u + P2_PD);
        if (u + P2_AD < P2_NS && (u + P2_AD < P2_NF || u >= P2_NF)) A(u + P2_AD);
    }
    int last = -1;
    for (int i = 0; i < n; ++i) if (idx[i] <= s) last = i;
    return n - 1 - last;
}

DEV void g_role(char* ws, char* out, int item, lds_ptr lds) {
    constexpr int SK = 1;
    const int tid = threadIdx.x, lane = tid & 63, w = __builtin_amdgcn_readfirstlane(tid >> 6);
    const int r = lane & 31, h = lane >> 5;
    const int xcd = item & 7, j = item >> 3;
    const unsigned l0 = (unsigned)(uintptr_t)lds;
    STAMP(0);
    __builtin_amdgcn_s_setprio(G_PRIO);
    const int reg = j >> 2, sub = j & 3, kg = w & 3;
    const bool up = w >= 4;
    const int rgp = (xcd & 3) * 4 + (reg & 3), cgp = (xcd >> 2) * 2 + (reg >> 2);
    const int rp = sub >> 1, cp = sub & 1;
    const int t0 = rgp * 4 + 2 * rp + cp, t1 = rgp * 4 + 2 * rp + (cp ^ 1);
    const int c0 = cgp * 128 + cp * 64, c4 = 512 + cgp * 32;
    const int ct0 = cgp * 4 + cp * 2, ct4 = 16 + cgp;
    const int tme = up ? t1 : t0;
    const i32x4 swf = make_srd(ws + WS_WF), sh = make_srd(ws + WS_HF), so_ = make_srd(ws + WS_OF);
    const unsigned va = lane * 16;
    f32x16 acc[3];
#pragma unroll
    for (int i = 0; i < 3; ++i) acc[i] = f32x16{};
    const float gm = 1.f - ((const float*)(ws + WS_G))[tme * 32 + r];
    asm volatile("" :: "v"(gm));
    auto run = [&](auto upc) {
        constexpr bool UP = decltype(upc)::value;
        bf16x8 af[P2_AD], wf[P2_PD][3];
        auto issue_w = [&](int s) {
            const unsigned fb = s < P2_NF ? (unsigned)(WF_WD + (kg * 24 + s) * 20) * 1024u : (unsigned)(WF_WO + (kg * 10 + s - P2_NF) * 20) * 1024u;
            bufld16(wf[s % P2_PD][0], swf, va, fb + (unsigned)ct0 * 1024u); bufld16(wf[s % P2_PD][1], swf, va, fb + (unsigned)(ct0 + 1) * 1024u);
            if constexpr (!UP) bufld16(wf[s % P2_PD][2], swf, va, fb + (unsigned)ct4 * 1024u);
        };
        auto issue_a = [&](int s) {
            if (s < P2_NF) bufld16(af[s % P2_AD], sh, va, (unsigned)(tme * 96 + kg * 24 + s) * 1024u);
            else bufld16_o(af[s % P2_AD], so_, va, (unsigned)(tme * 40 + kg * 10 + s - P2_NF) * 1024u);
        };
#pragma unroll
        for (int s = 0; s < P2_PD; ++s) issue_w(s);
        STAMP(3);
#pragma unroll
        for (int s = 0; s < P2_AD; ++s) issue_a(s);
        auto step = [&](auto sc) {
            constexpr int s = decltype(sc)::value;
            if constexpr (s == P2_NF) {
                STAMP(4);
                consume<false>(ws, rgp >> 1, 32);
                __builtin_amdgcn_s_setprio(2);
                STAMP(5);
#pragma unroll
                for (int t = P2_NF; t < P2_NF + P2_AD; ++t) issue_a(t);
            }
            if constexpr (UP) asm volatile("s_waitcnt vmcnt(%3)" : "+v"(af[s % P2_AD]), "+v"(wf[s % P2_PD][0]), "+v"(wf[s % P2_PD][1]) : "n"(p2_allowed(s, UP)) : "memory");
            else asm volatile("s_waitcnt vmcnt(%4)" : "+v"(af[s % P2_AD]), "+v"(wf[s % P2_PD][0]), "+v"(wf[s % P2_PD][1]), "+v"(wf[s % P2_PD][2]) : "n"(p2_allowed(s, UP)) : "memory");
            acc[0] = mfma16(wf[s % P2_PD][0], af[s % P2_AD], acc[0]); acc[1] = mfma16(wf[s % P2_PD][1], af[s % P2_AD], acc[1]);
            if constexpr (!UP) acc[2] = mfma16(wf[s % P2_PD][2], af[s % P2_AD], acc[2]);
            SBAR();
            if constexpr (s + P2_PD < P2_NS) issue_w(s + P2_PD);
            if constexpr (s + P2_AD < P2_NS && (s + P2_AD < P2_NF || s >= P2_NF)) issue_a(s + P2_AD);
            if constexpr (s == P2_NF - 1) {
#pragma unroll
                for (int i = 0; i < 16; ++i) { acc[0][i] *= gm; acc[1][i] *= gm; acc[2][i] *= gm; }
            }
            SBAR();
        };
        static_for<0, P2_NS>(step);
    };
    if (up) run(std::true_type{}); else run(std::false_type{});
    STAMP(6);
    typedef __attribute__((ext_vector_type(4))) float f32x4;
    typedef __attribute__((address_space(3))) f32x4* lds_f4;
    __syncthreads();
    {
        lds_ptr mine = lds + kg * 20480 + r * 128;
#pragma unroll
        for (int i = 0; i < 3; ++i) {
            if (i == 2 && up) break;
            const int tl = up ? 2 + i : (i == 2 ? 4 : i);
#pragma unroll
            for (int g = 0; g < 4; ++g) {
                f32x4 v = {acc[i][4 * g], acc[i][4 * g + 1], acc[i][4 * g + 2], acc[i][4 * g + 3]};
                *(lds_f4)(mine + tl * 4096 + (((2 * g + h) ^ (r & 7)) * 16)) = v;
            }
        }
    }
    __syncthreads();
#pragma unroll
    for (int it = 0; it < 2; ++it) {
        const int u = tid + 512 * it;
        if (u < 640) {
            const int tl = u >> 7, tok = (u & 127) >> 2, ch = u & 3;
            f32x4 lo = {0, 0, 0, 0}, hi = {0, 0, 0, 0};
#pragma unroll
            for (int ww = 0; ww < 4; ++ww) {
                lds_ptr p = lds + ww * 20480 + tl * 4096 + tok * 128;
                lo += *(lds_f4)(p + (((2 * ch) ^ (tok & 7)) * 16));
                hi += *(lds_f4)(p + (((2 * ch + 1) ^ (tok & 7)) * 16));
            }
            const int tt = (tl == 2 || tl == 3) ? t1 : t0, cc = tl == 4 ? c4 : c0 + (tl & 1) * 32;
            u32x4 o = {pk2(lo[0], lo[1]), pk2(lo[2], lo[3]), pk2(hi[0], hi[1]), pk2(hi[2], hi[3])};
            *(u32x4*)(out + (size_t)(tt * 32 + tok) * 1280 + (cc + ch * 8) * 2) = o;
        }
    }
    STAMP(7);
}

__global__ __launch_bounds__(512, 4) void k2_kernel(char* __restrict__ ws, char* __restrict__ out) {
    extern __shared__ __attribute__((aligned(16))) char lds_raw[];
    lds_ptr lds = (lds_ptr)lds_raw;
    if (blockIdx.x < 256) attn_role(ws, blockIdx.x, lds);
    else g_role(ws, out, blockIdx.x - 256, lds);
}

extern "C" void kernel_launch(void* const* d_in, const int* in_sizes, int n_in, void* d_out, int out_size, void* d_ws, size_t ws_size, hipStream_t stream) {
    const char* ax = (const char*)d_in[0];
    const char* wqkv = (const char*)d_in[1];
    const char* wo = (const char*)d_in[2];
    const char* wg = (const char*)d_in[4];
    const char* wu = (const char*)d_in[5];
    const char* wd = (const char*)d_in[6];
    const char* gw = (const char*)d_in[7];
    char* ws = (char*)d_ws;
    (void)hipFuncSetAttribute((const void*)k1_kernel, hipFuncAttributeMaxDynamicSharedMemorySize, K1_LDS);
    (void)hipFuncSetAttribute((const void*)k2_kernel, hipFuncAttributeMaxDynamicSharedMemorySize, 81920);
    k1_kernel<<<256, 512, K1_LDS, stream>>>(ax, wqkv, gw, wg, wu, wd, wo, ws);
    k2_kernel<<<512, 512, 81920, stream>>>(ws, (char*)d_out);
}
```

```cpp
#define A_PRIO 1
#define G_PRIO 0
#define POLL_SLEEP 8
#include <hip/hip_runtime.h>
#include <hip/hip_bf16.h>
#include <stdint.h>
#include <type_traits>

typedef __attribute__((ext_vector_type(8))) __bf16 bf16x8;
typedef __attribute__((ext_vector_type(8))) short s16x8;
typedef __attribute__((ext_vector_type(4))) short s16x4;
typedef __attribute__((ext_vector_type(2))) __bf16 bf16x2;
typedef __attribute__((ext_vector_type(2))) float f32x2;
typedef __attribute__((ext_vector_type(16))) float f32x16;
typedef __attribute__((ext_vector_type(4))) unsigned u32x4;
typedef __attribute__((ext_vector_type(2))) unsigned u32x2;
typedef __attribute__((ext_vector_type(4))) int i32x4;
typedef __attribute__((address_space(3))) const char* lds_cptr;
typedef __attribute__((address_space(3))) char* lds_ptr;

#define DEV __device__ __forceinline__
#define PACE 8
#define PHMUL 1
#define RL_AT 2
#define RL_DO 6
#define WARM_POL ""
#define WARM_AT 1

constexpr int T_ = 2048, D_ = 640, NH = 32, DH = 20, F_ = 1536;
constexpr float QSCALE = 0.22360679774997896f * 1.4426950408889634f;

constexpr size_t WS_QF = 0;
constexpr size_t WS_KV = WS_QF + (size_t)NH * 64 * 1280;
constexpr size_t WS_HF = WS_KV + (size_t)NH * 64 * 3072;
constexpr size_t WS_OF = WS_HF + (size_t)64 * 96 * 1024;
constexpr size_t WS_WF = WS_OF + (size_t)64 * 40 * 1024;
constexpr int WF_WD = 0, WF_WO = 1920, WF_N = 2720;
constexpr size_t WS_G  = WS_WF + (size_t)WF_N * 1024;
constexpr size_t WS_FLAG = WS_G + 2048 * 4;
constexpr int FLAG_LINES = 8;
constexpr size_t WS_END = WS_FLAG + FLAG_LINES * 256;

DEV i32x4 make_srd(const void* p) {
    i32x4 s;
    s[0] = __builtin_amdgcn_readfirstlane((int)(uint32_t)(uintptr_t)p);
    s[1] = __builtin_amdgcn_readfirstlane((int)(uint32_t)((uintptr_t)p >> 32)) & 0xffff;
    s[2] = 0x7fffffff;
    s[3] = 0x00020000;
    return s;
}
#define RFL(x) (x)
#define PADS
DEV void dma16(const i32x4& srd, unsigned voff, unsigned soff, unsigned ldsaddr) {
    asm volatile(PADS "s_mov_b32 m0, %3\n\ts_nop 0\n\tbuffer_load_dwordx4 %0, %1, %2 offen lds"
                 :: "v"(voff), "s"(srd), "s"(RFL(soff)), "s"(RFL(ldsaddr)) : "memory");
}
DEV void warm(unsigned& sink, const char* base, unsigned voff) {
    asm volatile("global_load_dword %0, %1, %2" : "=v"(sink) : "v"(voff), "s"(base) : "memory");
}
#define WAIT_VM(n) asm volatile("s_waitcnt vmcnt(" #n ")" ::: "memory")
#define WAIT_VM_BAR(n) asm volatile("s_waitcnt vmcnt(" #n ")\n\ts_barrier" ::: "memory")
#define SBAR() __builtin_amdgcn_sched_barrier(0)

DEV f32x16 mfma16(bf16x8 a, bf16x8 b, f32x16 c) { return __builtin_amdgcn_mfma_f32_32x32x16_bf16(a, b, c, 0, 0, 0); }
DEV f32x16 mfma8(s16x4 a, s16x4 b, f32x16 c) { return __builtin_amdgcn_mfma_f32_32x32x8bf16_1k(a, b, c, 0, 0, 0); }
DEV unsigned pk2(float lo, float hi) {
    f32x2 v = {lo, hi};
    bf16x2 r = __builtin_convertvector(v, bf16x2);
    return __builtin_bit_cast(unsigned, r);
}
DEV bf16x8 lds_ld128(lds_cptr p) { return *(const __attribute__((address_space(3))) bf16x8*)p; }
DEV s16x4 lds_ld64(lds_cptr p) { return *(const __attribute__((address_space(3))) s16x4*)p; }
DEV s16x4 lds_tr(lds_cptr p) { return __builtin_amdgcn_ds_read_tr16_b64_v4i16((__attribute__((address_space(3))) s16x4*)p); }
DEV bf16x8 cat8(s16x4 lo, s16x4 hi) {
    s16x8 r = __builtin_shufflevector(lo, hi, 0, 1, 2, 3, 4, 5, 6, 7);
    return __builtin_bit_cast(bf16x8, r);
}
DEV bf16x8 pack8(const f32x16& v, int base) {
    u32x4 r = {pk2(v[base], v[base + 1]), pk2(v[base + 2], v[base + 3]), pk2(v[base + 4], v[base + 5]), pk2(v[base + 6], v[base + 7])};
    return __builtin_bit_cast(bf16x8, r);
}

typedef __attribute__((address_space(1))) unsigned gu32;
#define RLX_AGENT __ATOMIC_RELAXED, __HIP_MEMORY_SCOPE_AGENT
DEV __amdgpu_buffer_rsrc_t make_rsrc(void* p) { return __builtin_amdgcn_make_buffer_rsrc(p, 0, 0x7fffffff, 0x00020000); }
DEV void st16_sc1(__amdgpu_buffer_rsrc_t rs, unsigned voff, u32x4 v) { __builtin_amdgcn_raw_buffer_store_b128(v, rs, voff, 0, 16); }
DEV void st8_sc1(__amdgpu_buffer_rsrc_t rs, unsigned voff, u32x2 v) { __builtin_amdgcn_raw_buffer_store_b64(v, rs, voff, 0, 16); }
#define K1_WT_AUX 16
DEV void st16_pol(__amdgpu_buffer_rsrc_t rs, unsigned voff, u32x4 v) { __builtin_amdgcn_raw_buffer_store_b128(v, rs, voff, 0, K1_WT_AUX); }
DEV void st8_pol(__amdgpu_buffer_rsrc_t rs, unsigned voff, u32x2 v) { __builtin_amdgcn_raw_buffer_store_b64(v, rs, voff, 0, K1_WT_AUX); }
DEV void publish(char* ws, int line) {
    asm volatile("s_waitcnt vmcnt(0)\n\ts_barrier" ::: "memory");
    if (threadIdx.x == 0) __hip_atomic_fetch_add((gu32*)(ws + WS_FLAG + line * 256), 1u, RLX_AGENT);
}
template <bool ACQ = true>
DEV void consume(char* ws, int line, unsigned target) {
    if (threadIdx.x < 64) {
        gu32* f = (gu32*)(ws + WS_FLAG + line * 256);
        for (unsigned spins = 0; spins < (1u << 20); ++spins) {
            if ((unsigned)__builtin_amdgcn_readfirstlane(__hip_atomic_load(f, RLX_AGENT)) >= target) break;
            __builtin_amdgcn_s_sleep(POLL_SLEEP);
        }
        if constexpr (ACQ) {
            __builtin_amdgcn_fence(__ATOMIC_ACQUIRE, "agent");
            asm volatile("s_waitcnt vmcnt(0)" ::: "memory");
        }
    }
    asm volatile("s_barrier" ::: "memory");
}

#define STAMP(i) do {} while (0)
constexpr int K1_XS = 16384, K1_WS = 20480, K1_WB = 3 * K1_XS, K1_GW = K1_WB + 5 * K1_WS, K1_LDS = K1_GW + 1280 + 64;

DEV f32x2 gelu_mul2(f32x2 x, f32x2 u) {
    const float c0 = -2.0f * 0.7978845608028654f * 1.4426950408889634f, c1 = c0 * 0.044715f;
    const f32x2 t = x * __builtin_elementwise_fma(x * x, (f32x2){c1, c1}, (f32x2){c0, c0});
    f32x2 d = {__builtin_amdgcn_exp2f(t[0]), __builtin_amdgcn_exp2f(t[1])};
    d += (f32x2){1.0f, 1.0f};
    const f32x2 rc = {__builtin_amdgcn_rcpf(d[0]), __builtin_amdgcn_rcpf(d[1])};
    return (x * u) * rc;
}
DEV float gelu_tanh(float x) {
    const float c0 = -2.0f * 0.7978845608028654f * 1.4426950408889634f, c1 = c0 * 0.044715f;
    const float e = __builtin_amdgcn_exp2f(x * __builtin_fmaf(x * x, c1, c0));
    return x * __builtin_amdgcn_rcpf(1.0f + e);
}
template <int I, int N, class F> DEV void static_for(F&& f) { if constexpr (I < N) { f(std::integral_constant<int, I>{}); static_for<I + 1, N>(f); } }
template <int N> DEV void wait_vm() { asm volatile("s_waitcnt vmcnt(%0)" :: "n"(N) : "memory"); }
template <int N> DEV void wait_vm_bar() { asm volatile("s_waitcnt vmcnt(%0)\n\ts_barrier" :: "n"(N) : "memory"); }

constexpr int k1_allowed(int t) {
    int kind[160] = {}, idx[160] = {}, n = 0;
    auto P = [&](int k, int i, int c) { for (int q = 0; q < c; ++q) { kind[n] = k; idx[n] = i; ++n; } };
    P(0, 0, 4); P(1, 0, 5); P(1, 1, 5); P(0, 1, 4); P(1, 2, 5); P(1, 3, 5);
    for (int u = 0; u < t; ++u) { if ((u & 1) == 0 && u / 2 + 2 < 10) P(0, u / 2 + 2, 4); if (u + 4 < 20) P(1, u + 4, 5); if (u == 16) P(2, 0, 4); }
    int last = -1;
    for (int i = 0; i < n; ++i) {
        const bool need = (kind[i] == 0 && idx[i] <= ((t + 1) >> 1)) || (kind[i] == 1 && idx[i] <= t + 1);
        if (need) last = i;
    }
    return n - 1 - last;
}

template <int MODE>
DEV void k1_gemm(const char* x, const char* wqkv, const char* gw, const char* wg, const char* wu, const char* wd, const char* wo, char* ws, int mt, int j, lds_ptr lds) {
    constexpr int SK = 0;
    const int tid = threadIdx.x, lane = tid & 63, w8 = __builtin_amdgcn_readfirstlane(tid >> 6), w = w8 & 3;
    const int wt = w >> 1, ww = w & 1, r = lane & 31, h = lane >> 5;
    const int m0 = mt * 128, n0 = (j < 15 ? j : 14) * 128, f0 = j * 96;
    const unsigned l0 = (unsigned)(uintptr_t)lds;
    const int ph = (PHMUL * ((mt & 7) + 3 * (j & 3))) % 10;
    STAMP(0);
    if (w8 >= 4) {
        const i32x4 sx = make_srd(x), sw = make_srd(wqkv), sg = make_srd(wg), su = make_srd(wu);
        const unsigned xq = lane >> 4;
        const unsigned vx0 = (unsigned)(lane >> 3) * 1280u + (unsigned)(((lane & 7) ^ xq) * 16);
        const unsigned vx1 = (unsigned)(lane >> 3) * 1280u + (unsigned)(((lane & 7) ^ xq ^ 4) * 16);
        const unsigned vq = (unsigned)(lane >> 4) * 3840u + (unsigned)(((lane & 15) ^ ((lane >> 4) << 2)) * 16);
        unsigned vf[3];
#pragma unroll
        for (int i = 0; i < 3; ++i) { const int p = 3 * w + i, L = (p % 6) * 64 + lane, row = L / 12, c16 = L - row * 12; vf[i] = (unsigned)row * 3072u + (unsigned)((p < 6 ? c16 : (c16 ^ 2)) * 16); }
        const unsigned sxb = (unsigned)(m0 + 32 * w) * 1280u, sqb = (unsigned)(8 * w) * 3840u + (unsigned)n0 * 2u, sfb = (unsigned)f0 * 2u;
        auto issue_x = [&](int u) {
            const unsigned lb = l0 + (unsigned)(u % 3) * K1_XS;
            const int ur = u + ph < 10 ? u + ph : u + ph - 10;
#pragma unroll
            for (int i = 0; i < 4; ++i) dma16(sx, (i & 1) ? vx1 : vx0, sxb + (unsigned)i * 10240u + (unsigned)ur * 128u, lb + (unsigned)(4 * w + i) * 1024u);
        };
        auto issue_w = [&](int t) {
            const unsigned lb = l0 + K1_WB + (unsigned)(t % 5) * K1_WS;
            const int tr = t + 2 * ph < 20 ? t + 2 * ph : t + 2 * ph - 20;
#pragma unroll
            for (int i = 0; i < 2; ++i) dma16(sw, vq, sqb + (unsigned)(tr * 32 + 4 * i) * 3840u, lb + (unsigned)(2 * w + i) * 1024u);
            const unsigned so = sfb + (unsigned)tr * (32u * 3072u);
            if (w < 2) {
#pragma unroll
                for (int i = 0; i < 3; ++i) dma16(sg, vf[i], so, lb + 8192u + (unsigned)(3 * w + i) * 1024u);
            } else {
#pragma unroll
                for (int i = 0; i < 3; ++i) dma16(su, vf[i], so, lb + 8192u + (unsigned)(3 * w + i) * 1024u);
            }
        };
        const int gwv = (mt * 16 + j) * 4 + w;
        const int u0 = gwv, u1 = gwv + 1024 < 1360 ? gwv + 1024 : gwv;
        const unsigned wl = l0 + K1_WB + (unsigned)w * 4096u;
        const char* rsrc[4];
        auto prep = [&](int i) {
            int u = (i >> 1) ? u1 : u0;
            asm volatile("" : "+s"(u) :: "memory");
            const int row = 8 * (i & 1) + (lane >> 3), c16 = lane & 7;
            if (u < 960) { const int ks = u / 10, cq = u - ks * 10; rsrc[i] = wd + (size_t)(16 * ks + row) * 1280 + cq * 128 + c16 * 16; }
            else { const int q = u - 960, ks = q / 10, cq = q - ks * 10;
                   const int k = ks < 32 ? 20 * ks + row : 20 * (4 * (ks - 32) + 2 * ((row >> 2) & 1) + (row >> 3)) + 16 + (row & 3);
                   rsrc[i] = wo + (size_t)k * 1280 + cq * 128 + c16 * 16; }
            asm volatile("" : "+v"(rsrc[i]) :: "memory");
        };
        auto rl_issue = [&]() {
#pragma unroll
            for (int i = 0; i < 4; ++i) { const char* p = rsrc[i]; const unsigned d = wl + (unsigned)i * 1024u; asm volatile("s_mov_b32 m0, %1\n\ts_nop 0\n\tglobal_load_lds_dwordx4 %0, off" :: "v"(p), "s"(d) : "memory"); }
        };
        issue_x(0); issue_w(0);
        __builtin_amdgcn_s_sleep(PACE); issue_w(1);
        __builtin_amdgcn_s_sleep(PACE); issue_x(1); issue_w(2);
        __builtin_amdgcn_s_sleep(PACE); issue_w(3);
        wait_vm_bar<k1_allowed(-1)>();
        auto top = [&](auto tc) {
            constexpr int t = decltype(tc)::value;
            wait_vm_bar<k1_allowed(t)>();
            if constexpr ((t & 1) == 0 && t / 2 + 2 < 10) issue_x(t / 2 + 2);
            if constexpr (t + 4 < 20) issue_w(t + 4);
            if constexpr (t >= 2 && t < 6) prep(t - 2);
            if constexpr (t == 16) rl_issue();
        };
        static_for<0, 19>(top);
        wait_vm<0>();
        {
            const int g = lane >> 4, nh = g & 1, q = (lane & 15) >> 2, p = lane & 3;
            lds_cptr base = (lds_cptr)lds + K1_WB + w * 4096;
            const __amdgpu_buffer_rsrc_t rw = make_rsrc(ws + WS_WF);
            bf16x8 v[4];
#pragma unroll
            for (int i = 0; i < 4; ++i) { lds_cptr pp = base + (i >> 1) * 2048 + (4 * h + q) * 128 + (i & 1) * 64 + nh * 32 + p * 8; v[i] = cat8(lds_tr(pp), lds_tr(pp + 1024)); }
#pragma unroll
            for (int i = 0; i < 4; ++i) *(bf16x8*)(ws + WS_WF + (size_t)(2 * ((i >> 1) ? u1 : u0) + (i & 1)) * 1024 + lane * 16) = v[i];
            (void)rw;
        }
        return;
    }
    if constexpr (MODE == 2) {
        if (tid < 80) *(__attribute__((address_space(3))) u32x4*)(lds + K1_GW + tid * 16) = *(const u32x4*)(gw + tid * 16);
        else if (tid == 80) *(__attribute__((address_space(3))) u32x4*)(lds + K1_GW + 1280) = (u32x4){0u, 0u, 0u, 0u};
    }
    lds_cptr xa[4], qa[2], fa[3];
#pragma unroll
    for (int s = 0; s < 4; ++s) xa[s] = (lds_cptr)lds + (wt * 64 + r) * 128 + (((2 * s + h) ^ ((r >> 1) & 7)) * 16);
    {
        const int g = lane >> 4, nh = g & 1, q = (lane & 15) >> 2, p = lane & 3;
#pragma unroll
        for (int ct = 0; ct < 2; ++ct) {
            const int slot = ww * 8 + ct * 4 + 2 * nh + (p >> 1);
            qa[ct] = (lds_cptr)lds + K1_WB + (8 * h + q) * 256 + ((slot ^ (q << 2)) * 16) + 8 * (p & 1);
        }
#pragma unroll
        for (int a = 0; a < 3; ++a) {
            const int aa = ww * 3 + a, c16 = 2 * (nh ? (aa ^ 1) : aa) + (p >> 1);
            fa[a] = (lds_cptr)lds + K1_WB + 8192 + nh * 6144 + (8 * h + q) * 192 + c16 * 16 + 8 * (p & 1);
        }
    }
    const lds_cptr ga = (lds_cptr)lds + K1_GW + (r == 0 ? h * 16 : 1280);
    const int gstep = r == 0 ? 32 : 0;
    f32x16 aq[2][2], af[3][2];
#pragma unroll
    for (int i = 0; i < 2; ++i) { aq[i][0] = f32x16{}; aq[i][1] = f32x16{}; }
#pragma unroll
    for (int a = 0; a < 3; ++a) { af[a][0] = f32x16{}; af[a][1] = f32x16{}; }
    bf16x8 xf[2][2], qf[2][2], ff[2][3];
    auto rd_x = [&](int b, int t, int s, int tt) { xf[b][tt] = lds_ld128(xa[2 * (t & 1) + s] + ((t >> 1) % 3) * K1_XS + tt * 4096); };
    auto rd_q = [&](int b, int t, int s, int ct) {
        if constexpr (MODE == 2) { if (ct == 0) { const int tr = t + 2 * ph < 20 ? t + 2 * ph : t + 2 * ph - 20; qf[b][0] = lds_ld128(ga + (2 * tr + s) * gstep); } }
        else qf[b][ct] = cat8(lds_tr(qa[ct] + (t % 5) * K1_WS + s * 4096), lds_tr(qa[ct] + (t % 5) * K1_WS + s * 4096 + 1024));
    };
    auto rd_f = [&](int b, int t, int s, int a) { ff[b][a] = cat8(lds_tr(fa[a] + (t % 5) * K1_WS + s * 3072), lds_tr(fa[a] + (t % 5) * K1_WS + s * 3072 + 768)); };
    asm volatile("s_waitcnt lgkmcnt(0)\n\ts_barrier" ::: "memory");
    STAMP(1);
    rd_x(0, 0, 0, 0); rd_x(0, 0, 0, 1); rd_q(0, 0, 0, 0); rd_q(0, 0, 0, 1); rd_f(0, 0, 0, 0); rd_f(0, 0, 0, 1); rd_f(0, 0, 0, 2);
    auto stage = [&](auto tc) {
        constexpr int t = decltype(tc)::value;
        if constexpr (t < 19) asm volatile("s_barrier" ::: "memory");
#pragma unroll
        for (int s = 0; s < 2; ++s) {
            const int c = s, nb = s ^ 1;
            const bool more = s == 0 || t < 19;
            const int tn = s == 0 ? t : t + 1, sn = s ^ 1;
            auto mq = [&](int ct, int tt) {
                if constexpr (MODE == 0) aq[ct][tt] = mfma16(qf[c][ct], xf[c][tt], aq[ct][tt]);
                else if constexpr (MODE == 1) aq[tt][ct] = mfma16(xf[c][tt], qf[c][ct], aq[tt][ct]);
                else { if (ct == 0) aq[0][tt] = mfma16(qf[c][0], xf[c][tt], aq[0][tt]); }
            };
            auto mf = [&](int a, int tt) { af[a][tt] = mfma16(ff[c][a], xf[c][tt], af[a][tt]); };
            mq(0, 0); SBAR(); if (more) { rd_x(nb, tn, sn, 0); rd_x(nb, tn, sn, 1); } SBAR();
            mq(0, 1); SBAR(); if (more) rd_q(nb, tn, sn, 0); SBAR();
            mq(1, 0); SBAR(); if (more) rd_q(nb, tn, sn, 1); SBAR();
            mq(1, 1); SBAR(); if (more) rd_f(nb, tn, sn, 0); SBAR();
            mf(0, 0); SBAR(); if (more) rd_f(nb, tn, sn, 1); SBAR();
            mf(0, 1); SBAR(); if (more) rd_f(nb, tn, sn, 2); SBAR();
            mf(1, 0); SBAR(); mf(1, 1); SBAR(); mf(2, 0); SBAR(); mf(2, 1); SBAR();
        }
    };
    static_for<0, 20>(stage);
    STAMP(2);
    if constexpr (MODE == 0) {
        const bool isq = j < 5;
        const int csec = (isq ? n0 : n0 - 640) + ww * 64;
        char* base = ws + (isq ? WS_QF : WS_KV);
        const int tstride = isq ? 1280 : 3072;
        const float sc = isq ? QSCALE : 1.0f;
#pragma unroll
        for (int ct = 0; ct < 2; ++ct)
#pragma unroll
            for (int g = 0; g < 4; ++g) {
                const int qd = (csec + ct * 32 + 8 * g + 4 * h) >> 2, head = qd / 5, dq = qd - head * 5;
                const int inoff = dq < 4 ? (dq >> 1) * 512 + r * 16 + (dq & 1) * 8 : 1024 + r * 8;
#pragma unroll
                for (int tt = 0; tt < 2; ++tt) {
                    const int tile = mt * 4 + wt * 2 + tt;
                    char* p = base + (size_t)(head * 64 + tile) * tstride + inoff;
                    const f32x16& a = aq[ct][tt];
                    u32x2 v = {pk2(a[4 * g] * sc, a[4 * g + 1] * sc), pk2(a[4 * g + 2] * sc, a[4 * g + 3] * sc)};
                    *(u32x2*)p = v;
                    if (!isq && dq == 4) *(u32x2*)(p + 256) = (u32x2){0x00003f80u, 0u};
                }
            }
    } else if constexpr (MODE == 1) {
#pragma unroll
        for (int ct = 0; ct < 2; ++ct) {
            const int col = n0 - 1280 + ww * 64 + ct * 32 + r, head = col / 20, d = col - head * 20;
#pragma unroll
            for (int tt = 0; tt < 2; ++tt) {
                const int tile = mt * 4 + wt * 2 + tt;
                char* p = ws + WS_KV + (size_t)(head * 64 + tile) * 3072 + 1536 + h * 384 + d * 16;
#pragma unroll
                for (int s = 0; s < 2; ++s) {
                    *(bf16x8*)(p + s * 768) = pack8(aq[tt][ct], 8 * s);
                    if (d == 0) *(u32x4*)(p + s * 768 + 320) = (u32x4){0x3f803f80u, 0x3f803f80u, 0x3f803f80u, 0x3f803f80u};
                    if (d == 1) *(u32x4*)(p + s * 768 + 320) = (u32x4){0u, 0u, 0u, 0u};
                }
            }
        }
    } else {
        if (ww == 0 && h == 0) {
#pragma unroll
            for (int tt = 0; tt < 2; ++tt) ((float*)(ws + WS_G))[m0 + wt * 64 + tt * 32 + r] = __builtin_amdgcn_rcpf(1.0f + __builtin_amdgcn_exp2f(-1.4426950408889634f * aq[0][tt][0]));
        }
        if (mt == 0 && tid < FLAG_LINES) *(unsigned*)(ws + WS_FLAG + tid * 256) = 0u;
    }
#pragma unroll
    for (int a = 0; a < 3; ++a)
#pragma unroll
        for (int tt = 0; tt < 2; ++tt) {
            f32x16 hv;
#pragma unroll
            for (int i = 0; i < 8; i += 2) { const f32x2 v = gelu_mul2((f32x2){af[a][tt][i], af[a][tt][i + 1]}, (f32x2){af[a][tt][8 + i], af[a][tt][9 + i]}); hv[i] = v[0]; hv[i + 1] = v[1]; }
            const int tile = mt * 4 + wt * 2 + tt, fs = j * 6 + ww * 3 + a;
            *(bf16x8*)(ws + WS_HF + (size_t)(tile * 96 + fs) * 1024 + lane * 16) = pack8(hv, 0);
        }
    STAMP(3);
}

__global__ __launch_bounds__(512, 2) void k1_kernel(const char* __restrict__ x, const char* __restrict__ wqkv, const char* __restrict__ gw, const char* __restrict__ wg, const char* __restrict__ wu,
                                                   const char* __restrict__ wd, const char* __restrict__ wo, char* __restrict__ ws) {
    extern __shared__ __attribute__((aligned(16))) char lds_raw[];
    lds_ptr lds = (lds_ptr)lds_raw;
    const int b = blockIdx.x, xcd = b & 7, jj = b >> 3;
    const int mt = (xcd & 1) * 8 + (jj & 7), j = (xcd >> 1) * 4 + (jj >> 3);
    if (j < 10) k1_gemm<0>(x, wqkv, gw, wg, wu, wd, wo, ws, mt, j, lds);
    else if (j < 15) k1_gemm<1>(x, wqkv, gw, wg, wu, wd, wo, ws, mt, j, lds);
    else k1_gemm<2>(x, wqkv, gw, wg, wu, wd, wo, ws, mt, j, lds);
}

constexpr int AT_CHUNK = 24576;

DEV void attn_slow(const char* kvh, bf16x8 q1, s16x4 q2, int lane, f32x16& o) {
    const int r = lane & 31, h = lane >> 5;
    const int vrow = r < 21 ? r : 21;
    float m = -INFINITY;
    o = f32x16{};
    for (int t = 0; t < 64; ++t) {
        const char* p = kvh + t * 3072;
        const bf16x8 k1 = *(const bf16x8*)(p + lane * 16);
        const s16x4 k2 = *(const s16x4*)(p + 1024 + lane * 8);
        const bf16x8 v0 = *(const bf16x8*)(p + 1536 + h * 384 + vrow * 16);
        const bf16x8 v1 = *(const bf16x8*)(p + 2304 + h * 384 + vrow * 16);
        f32x16 s = mfma16(k1, q1, f32x16{});
        s = mfma8(k2, q2, s);
        float tm = s[0];
#pragma unroll
        for (int i = 1; i < 16; ++i) tm = fmaxf(tm, s[i]);
        tm = fmaxf(tm, __shfl_xor(tm, 32));
        const float mn = fmaxf(m, tm);
        const float alpha = __builtin_amdgcn_exp2f(m - mn);
        m = mn;
#pragma unroll
        for (int i = 0; i < 16; ++i) { s[i] = __builtin_amdgcn_exp2f(s[i] - mn); o[i] *= alpha; }
        o = mfma16(v0, pack8(s, 0), o);
        o = mfma16(v1, pack8(s, 8), o);
    }
}

#define ATT_LOOP_ASM \
    "ds_read_b128 v[72:75], %1\n\t" \
    "ds_read_b64 v[80:81], %2\n\t" \
    "ds_read_b128 v[76:79], %1 offset:3072\n\t" \
    "ds_read_b64 v[82:83], %2 offset:3072\n\t" \
    "ds_read_b128 v[84:87], %3\n\t" \
    "ds_read_b128 v[88:91], %3 offset:768\n\t" \
    "ds_read_b128 v[92:95], %3 offset:3072\n\t" \
    "ds_read_b128 v[96:99], %3 offset:3840\n\t" \
    "s_waitcnt lgkmcnt(6)\n\t" \
    "v_mfma_f32_32x32x16_bf16 v[32:47], v[72:75], %8, 0\n\t" \
    "v_mfma_f32_32x32x8_bf16 v[32:47], v[80:81], %9, v[32:47]\n\t" \
    "s_nop 15\n\t" \
    "1:\n\t" \
    "s_waitcnt vmcnt(3) lgkmcnt(0)\n\t" \
    "s_barrier\n\t" \
    "v_add_u32 %1, %6, %12\n\t" \
    "v_add_u32 %2, %6, %13\n\t" \
    "v_add_u32 %3, %6, %14\n\t" \
    "s_sub_u32 %5, %6, 0x6000\n\t" \
    "s_cmp_eq_u32 %6, 0\n\t" \
    "s_cselect_b32 %5, 0xc000, %5\n\t" \
    "s_add_u32 %5, %5, %15\n\t" \
    "v_mfma_f32_32x32x16_bf16 v[48:63], v[76:79], %8, 0\n\t" \
    "v_exp_f32 v32, v32\n\t" \
    "ds_read_b128 v[72:75], %1 offset:0\n\t" \
    "v_exp_f32 v33, v33\n\t" \
    "ds_read_b64 v[80:81], %2 offset:0\n\t" \
    "v_exp_f32 v34, v34\n\t" \
    "v_exp_f32 v35, v35\n\t" \
    "v_exp_f32 v36, v36\n\t" \
    "s_mov_b32 m0, %5\n\t" \
    "v_exp_f32 v37, v37\n\t" \
    "buffer_load_dwordx4 %10, %11, %4 offen offset:0 lds\n\t" \
    "v_exp_f32 v38, v38\n\t" \
    "v_exp_f32 v39, v39\n\t" \
    "v_mfma_f32_32x32x8_bf16 v[48:63], v[82:83], %9, v[48:63]\n\t" \
    "v_cvt_pk_bf16_f32 v64, v32, v33\n\t" \
    "v_cvt_pk_bf16_f32 v65, v34, v35\n\t" \
    "v_cvt_pk_bf16_f32 v66, v36, v37\n\t" \
    "v_cvt_pk_bf16_f32 v67, v38, v39\n\t" \
    "v_exp_f32 v40, v40\n\t" \
    "v_exp_f32 v41, v41\n\t" \
    "v_exp_f32 v42, v42\n\t" \
    "v_exp_f32 v43, v43\n\t" \
    "v_mfma_f32_32x32x16_bf16 %0, v[84:87], v[64:67], %0\n\t" \
    "ds_read_b128 v[84:87], %3 offset:0\n\t" \
    "v_exp_f32 v44, v44\n\t" \
    "v_exp_f32 v45, v45\n\t" \
    "v_exp_f32 v46, v46\n\t" \
    "v_exp_f32 v47, v47\n\t" \
    "v_cvt_pk_bf16_f32 v68, v40, v41\n\t" \
    "v_cvt_pk_bf16_f32 v69, v42, v43\n\t" \
    "v_cvt_pk_bf16_f32 v70, v44, v45\n\t" \
    "v_cvt_pk_bf16_f32 v71, v46, v47\n\t" \
    "s_waitcnt lgkmcnt(1)\n\t" \
    "v_mfma_f32_32x32x16_bf16 v[32:47], v[72:75], %8, 0\n\t" \
    "v_exp_f32 v48, v48\n\t" \
    "ds_read_b128 v[76:79], %1 offset:3072\n\t" \
    "v_exp_f32 v49, v49\n\t" \
    "ds_read_b64 v[82:83], %2 offset:3072\n\t" \
    "v_exp_f32 v50, v50\n\t" \
    "v_exp_f32 v51, v51\n\t" \
    "v_mfma_f32_32x32x16_bf16 %0, v[88:91], v[68:71], %0\n\t" \
    "ds_read_b128 v[88:91], %3 offset:768\n\t" \
    "v_exp_f32 v52, v52\n\t" \
    "v_exp_f32 v53, v53\n\t" \
    "buffer_load_dwordx4 %10, %11, %4 offen offset:1024 lds\n\t" \
    "v_exp_f32 v54, v54\n\t" \
    "v_exp_f32 v55, v55\n\t" \
    "v_mfma_f32_32x32x8_bf16 v[32:47], v[80:81], %9, v[32:47]\n\t" \
    "v_cvt_pk_bf16_f32 v64, v48, v49\n\t" \
    "v_cvt_pk_bf16_f32 v65, v50, v51\n\t" \
    "v_cvt_pk_bf16_f32 v66, v52, v53\n\t" \
    "v_cvt_pk_bf16_f32 v67, v54, v55\n\t" \
    "v_exp_f32 v56, v56\n\t" \
    "v_exp_f32 v57, v57\n\t" \
    "v_exp_f32 v58, v58\n\t" \
    "v_exp_f32 v59, v59\n\t" \
    "v_mfma_f32_32x32x16_bf16 %0, v[92:95], v[64:67], %0\n\t" \
    "ds_read_b128 v[92:95], %3 offset:3072\n\t" \
    "v_exp_f32 v60, v60\n\t" \
    "v_exp_f32 v61, v61\n\t" \
    "v_exp_f32 v62, v62\n\t" \
    "v_exp_f32 v63, v63\n\t" \
    "v_cvt_pk_bf16_f32 v68, v56, v57\n\t" \
    "v_cvt_pk_bf16_f32 v69, v58, v59\n\t" \
    "v_cvt_pk_bf16_f32 v70, v60, v61\n\t" \
    "v_cvt_pk_bf16_f32 v71, v62, v63\n\t" \
    "s_waitcnt lgkmcnt(1)\n\t" \
    "v_mfma_f32_32x32x16_bf16 v[48:63], v[76:79], %8, 0\n\t" \
    "v_exp_f32 v32, v32\n\t" \
    "ds_read_b128 v[72:75], %1 offset:6144\n\t" \
    "v_exp_f32 v33, v33\n\t" \
    "ds_read_b64 v[80:81], %2 offset:6144\n\t" \
    "v_exp_f32 v34, v34\n\t" \
    "v_exp_f32 v35, v35\n\t" \
    "v_mfma_f32_32x32x16_bf16 %0, v[96:99], v[68:71], %0\n\t" \
    "ds_read_b128 v[96:99], %3 offset:3840\n\t" \
    "v_exp_f32 v36, v36\n\t" \
    "v_exp_f32 v37, v37\n\t" \
    "buffer_load_dwordx4 %10, %11, %4 offen offset:2048 lds\n\t" \
    "v_exp_f32 v38, v38\n\t" \
    "v_exp_f32 v39, v39\n\t" \
    "v_mfma_f32_32x32x8_bf16 v[48:63], v[82:83], %9, v[48:63]\n\t" \
    "v_cvt_pk_bf16_f32 v64, v32, v33\n\t" \
    "v_cvt_pk_bf16_f32 v65, v34, v35\n\t" \
    "v_cvt_pk_bf16_f32 v66, v36, v37\n\t" \
    "v_cvt_pk_bf16_f32 v67, v38, v39\n\t" \
    "v_exp_f32 v40, v40\n\t" \
    "v_exp_f32 v41, v41\n\t" \
    "v_exp_f32 v42, v42\n\t" \
    "v_exp_f32 v43, v43\n\t" \
    "v_mfma_f32_32x32x16_bf16 %0, v[84:87], v[64:67], %0\n\t" \
    "ds_read_b128 v[84:87], %3 offset:6144\n\t" \
    "v_exp_f32 v44, v44\n\t" \
    "v_exp_f32 v45, v45\n\t" \
    "v_exp_f32 v46, v46\n\t" \
    "v_exp_f32 v47, v47\n\t" \
    "v_cvt_pk_bf16_f32 v68, v40, v41\n\t" \
    "v_cvt_pk_bf16_f32 v69, v42, v43\n\t" \
    "v_cvt_pk_bf16_f32 v70, v44, v45\n\t" \
    "v_cvt_pk_bf16_f32 v71, v46, v47\n\t" \
    "s_waitcnt lgkmcnt(1)\n\t" \
    "v_mfma_f32_32x32x16_bf16 v[32:47], v[72:75], %8, 0\n\t" \
    "v_exp_f32 v48, v48\n\t" \
    "ds_read_b128 v[76:79], %1 offset:9216\n\t" \
    "v_exp_f32 v49, v49\n\t" \
    "ds_read_b64 v[82:83], %2 offset:9216\n\t" \
    "v_exp_f32 v50, v50\n\t" \
    "v_exp_f32 v51, v51\n\t" \
    "v_mfma_f32_32x32x16_bf16 %0, v[88:91], v[68:71], %0\n\t" \
    "ds_read_b128 v[88:91], %3 offset:6912\n\t" \
    "v_exp_f32 v52, v52\n\t" \
    "v_exp_f32 v53, v53\n\t" \
    "v_exp_f32 v54, v54\n\t" \
    "v_exp_f32 v55, v55\n\t" \
    "v_mfma_f32_32x32x8_bf16 v[32:47], v[80:81], %9, v[32:47]\n\t" \
    "v_cvt_pk_bf16_f32 v64, v48, v49\n\t" \
    "v_cvt_pk_bf16_f32 v65, v50, v51\n\t" \
    "v_cvt_pk_bf16_f32 v66, v52, v53\n\t" \
    "v_cvt_pk_bf16_f32 v67, v54, v55\n\t" \
    "v_exp_f32 v56, v56\n\t" \
    "v_exp_f32 v57, v57\n\t" \
    "v_exp_f32 v58, v58\n\t" \
    "v_exp_f32 v59, v59\n\t" \
    "v_mfma_f32_32x32x16_bf16 %0, v[92:95], v[64:67], %0\n\t" \
    "ds_read_b128 v[92:95], %3 offset:9216\n\t" \
    "v_exp_f32 v60, v60\n\t" \
    "v_exp_f32 v61, v61\n\t" \
    "v_exp_f32 v62, v62\n\t" \
    "v_exp_f32 v63, v63\n\t" \
    "v_cvt_pk_bf16_f32 v68, v56, v57\n\t" \
    "v_cvt_pk_bf16_f32 v69, v58, v59\n\t" \
    "v_cvt_pk_bf16_f32 v70, v60, v61\n\t" \
    "v_cvt_pk_bf16_f32 v71, v62, v63\n\t" \
    "s_waitcnt lgkmcnt(1)\n\t" \
    "v_mfma_f32_32x32x16_bf16 v[48:63], v[76:79], %8, 0\n\t" \
    "v_exp_f32 v32, v32\n\t" \
    "ds_read_b128 v[72:75], %1 offset:12288\n\t" \
    "v_exp_f32 v33, v33\n\t" \
    "ds_read_b64 v[80:81], %2 offset:12288\n\t" \
    "v_exp_f32 v34, v34\n\t" \
    "v_exp_f32 v35, v35\n\t" \
    "v_mfma_f32_32x32x16_bf16 %0, v[96:99], v[68:71], %0\n\t" \
    "ds_read_b128 v[96:99], %3 offset:9984\n\t" \
    "v_exp_f32 v36, v36\n\t" \
    "v_exp_f32 v37, v37\n\t" \
    "v_exp_f32 v38, v38\n\t" \
    "v_exp_f32 v39, v39\n\t" \
    "v_mfma_f32_32x32x8_bf16 v[48:63], v[82:83], %9, v[48:63]\n\t" \
    "v_cvt_pk_bf16_f32 v64, v32, v33\n\t" \
    "v_cvt_pk_bf16_f32 v65, v34, v35\n\t" \
    "v_cvt_pk_bf16_f32 v66, v36, v37\n\t" \
    "v_cvt_pk_bf16_f32 v67, v38, v39\n\t" \
    "v_exp_f32 v40, v40\n\t" \
    "v_exp_f32 v41, v41\n\t" \
    "v_exp_f32 v42, v42\n\t" \
    "v_exp_f32 v43, v43\n\t" \
    "v_mfma_f32_32x32x16_bf16 %0, v[84:87], v[64:67], %0\n\t" \
    "ds_read_b128 v[84:87], %3 offset:12288\n\t" \
    "v_exp_f32 v44, v44\n\t" \
    "v_exp_f32 v45, v45\n\t" \
    "v_exp_f32 v46, v46\n\t" \
    "v_exp_f32 v47, v47\n\t" \
    "v_cvt_pk_bf16_f32 v68, v40, v41\n\t" \
    "v_cvt_pk_bf16_f32 v69, v42, v43\n\t" \
    "v_cvt_pk_bf16_f32 v70, v44, v45\n\t" \
    "v_cvt_pk_bf16_f32 v71, v46, v47\n\t" \
    "s_waitcnt lgkmcnt(1)\n\t" \
    "v_mfma_f32_32x32x16_bf16 v[32:47], v[72:75], %8, 0\n\t" \
    "v_exp_f32 v48, v48\n\t" \
    "ds_read_b128 v[76:79], %1 offset:15360\n\t" \
    "v_exp_f32 v49, v49\n\t" \
    "ds_read_b64 v[82:83], %2 offset:15360\n\t" \
    "v_exp_f32 v50, v50\n\t" \
    "v_exp_f32 v51, v51\n\t" \
    "v_mfma_f32_32x32x16_bf16 %0, v[88:91], v[68:71], %0\n\t" \
    "ds_read_b128 v[88:91], %3 offset:13056\n\t" \
    "v_exp_f32 v52, v52\n\t" \
    "v_exp_f32 v53, v53\n\t" \
    "v_exp_f32 v54, v54\n\t" \
    "v_exp_f32 v55, v55\n\t" \
    "v_mfma_f32_32x32x8_bf16 v[32:47], v[80:81], %9, v[32:47]\n\t" \
    "v_cvt_pk_bf16_f32 v64, v48, v49\n\t" \
    "v_cvt_pk_bf16_f32 v65, v50, v51\n\t" \
    "v_cvt_pk_bf16_f32 v66, v52, v53\n\t" \
    "v_cvt_pk_bf16_f32 v67, v54, v55\n\t" \
    "v_exp_f32 v56, v56\n\t" \
    "v_exp_f32 v57, v57\n\t" \
    "v_exp_f32 v58, v58\n\t" \
    "v_exp_f32 v59, v59\n\t" \
    "v_mfma_f32_32x32x16_bf16 %0, v[92:95], v[64:67], %0\n\t" \
    "ds_read_b128 v[92:95], %3 offset:15360\n\t" \
    "v_exp_f32 v60, v60\n\t" \
    "v_exp_f32 v61, v61\n\t" \
    "v_exp_f32 v62, v62\n\t" \
    "v_exp_f32 v63, v63\n\t" \
    "v_cvt_pk_bf16_f32 v68, v56, v57\n\t" \
    "v_cvt_pk_bf16_f32 v69, v58, v59\n\t" \
    "v_cvt_pk_bf16_f32 v70, v60, v61\n\t" \
    "v_cvt_pk_bf16_f32 v71, v62, v63\n\t" \
    "s_waitcnt lgkmcnt(1)\n\t" \
    "v_mfma_f32_32x32x16_bf16 v[48:63], v[76:79], %8, 0\n\t" \
    "v_exp_f32 v32, v32\n\t" \
    "ds_read_b128 v[72:75], %1 offset:18432\n\t" \
    "v_exp_f32 v33, v33\n\t" \
    "ds_read_b64 v[80:81], %2 offset:18432\n\t" \
    "v_exp_f32 v34, v34\n\t" \
    "v_exp_f32 v35, v35\n\t" \
    "v_mfma_f32_32x32x16_bf16 %0, v[96:99], v[68:71], %0\n\t" \
    "ds_read_b128 v[96:99], %3 offset:16128\n\t" \
    "v_exp_f32 v36, v36\n\t" \
    "v_exp_f32 v37, v37\n\t" \
    "v_exp_f32 v38, v38\n\t" \
    "v_exp_f32 v39, v39\n\t" \
    "v_mfma_f32_32x32x8_bf16 v[48:63], v[82:83], %9, v[48:63]\n\t" \
    "v_cvt_pk_bf16_f32 v64, v32, v33\n\t" \
    "v_cvt_pk_bf16_f32 v65, v34, v35\n\t" \
    "v_cvt_pk_bf16_f32 v66, v36, v37\n\t" \
    "v_cvt_pk_bf16_f32 v67, v38, v39\n\t" \
    "v_exp_f32 v40, v40\n\t" \
    "v_exp_f32 v41, v41\n\t" \
    "v_exp_f32 v42, v42\n\t" \
    "v_exp_f32 v43, v43\n\t" \
    "v_mfma_f32_32x32x16_bf16 %0, v[84:87], v[64:67], %0\n\t" \
    "ds_read_b128 v[84:87], %3 offset:18432\n\t" \
    "v_exp_f32 v44, v44\n\t" \
    "v_exp_f32 v45, v45\n\t" \
    "v_exp_f32 v46, v46\n\t" \
    "v_exp_f32 v47, v47\n\t" \
    "v_cvt_pk_bf16_f32 v68, v40, v41\n\t" \
    "v_cvt_pk_bf16_f32 v69, v42, v43\n\t" \
    "v_cvt_pk_bf16_f32 v70, v44, v45\n\t" \
    "v_cvt_pk_bf16_f32 v71, v46, v47\n\t" \
    "s_waitcnt lgkmcnt(1)\n\t" \
    "v_mfma_f32_32x32x16_bf16 v[32:47], v[72:75], %8, 0\n\t" \
    "v_exp_f32 v48, v48\n\t" \
    "ds_read_b128 v[76:79], %1 offset:21504\n\t" \
    "v_exp_f32 v49, v49\n\t" \
    "ds_read_b64 v[82:83], %2 offset:21504\n\t" \
    "v_exp_f32 v50, v50\n\t" \
    "v_exp_f32 v51, v51\n\t" \
    "v_mfma_f32_32x32x16_bf16 %0, v[88:91], v[68:71], %0\n\t" \
    "ds_read_b128 v[88:91], %3 offset:19200\n\t" \
    "v_exp_f32 v52, v52\n\t" \
    "v_exp_f32 v53, v53\n\t" \
    "v_exp_f32 v54, v54\n\t" \
    "v_exp_f32 v55, v55\n\t" \
    "v_mfma_f32_32x32x8_bf16 v[32:47], v[80:81], %9, v[32:47]\n\t" \
    "v_cvt_pk_bf16_f32 v64, v48, v49\n\t" \
    "v_cvt_pk_bf16_f32 v65, v50, v51\n\t" \
    "v_cvt_pk_bf16_f32 v66, v52, v53\n\t" \
    "v_cvt_pk_bf16_f32 v67, v54, v55\n\t" \
    "v_exp_f32 v56, v56\n\t" \
    "v_exp_f32 v57, v57\n\t" \
    "v_exp_f32 v58, v58\n\t" \
    "v_exp_f32 v59, v59\n\t" \
    "v_mfma_f32_32x32x16_bf16 %0, v[92:95], v[64:67], %0\n\t" \
    "ds_read_b128 v[92:95], %3 offset:21504\n\t" \
    "v_exp_f32 v60, v60\n\t" \
    "v_exp_f32 v61, v61\n\t" \
    "v_exp_f32 v62, v62\n\t" \
    "v_exp_f32 v63, v63\n\t" \
    "v_cvt_pk_bf16_f32 v68, v56, v57\n\t" \
    "v_cvt_pk_bf16_f32 v69, v58, v59\n\t" \
    "v_cvt_pk_bf16_f32 v70, v60, v61\n\t" \
    "v_cvt_pk_bf16_f32 v71, v62, v63\n\t" \
    "s_nop 1\n\t" \
    "v_mfma_f32_32x32x16_bf16 %0, v[96:99], v[68:71], %0\n\t" \
    "ds_read_b128 v[96:99], %3 offset:22272\n\t" \
    "s_add_u32 %4, %4, 0x6000\n\t" \
    "s_add_u32 %6, %6, 0x6000\n\t" \
    "s_cmp_eq_u32 %6, 0x12000\n\t" \
    "s_cselect_b32 %6, 0, %6\n\t" \
    "s_sub_u32 %7, %7, 1\n\t" \
    "s_cmp_lg_u32 %7, 0\n\t" \
    "s_cbranch_scc1 1b\n\t" \
    "s_waitcnt vmcnt(0) lgkmcnt(0)\n\t" \
    "s_nop 15"
#define ATT_CLOBBERS "v32", "v33", "v34", "v35", "v36", "v37", "v38", "v39", "v40", "v41", "v42", "v43", "v44", "v45", "v46", "v47", "v48", "v49", "v50", "v51", "v52", "v53", "v54", "v55", "v56", "v57", "v58", "v59", "v60", "v61", "v62", "v63", "v64", "v65", "v66", "v67", "v68", "v69", "v70", "v71", "v72", "v73", "v74", "v75", "v76", "v77", "v78", "v79", "v80", "v81", "v82", "v83", "v84", "v85", "v86", "v87", "v88", "v89", "v90", "v91", "v92", "v93", "v94", "v95", "v96", "v97", "v98", "v99"
DEV void attn_role(char* ws, int item, lds_ptr lds) {
    constexpr int SK = 1;
    const int tid = threadIdx.x, lane = tid & 63, w = __builtin_amdgcn_readfirstlane(tid >> 6);
    const int r = lane & 31, h = lane >> 5;
    const int head = (item & 7) * 4 + (item >> 6), qb = (item >> 3) & 7;
    const char* kvh = ws + WS_KV + (size_t)head * 64 * 3072;
    i32x4 skv = make_srd(kvh);
    skv[2] = 64 * 3072;
    const unsigned l0 = (unsigned)(uintptr_t)lds;
    const unsigned vo = lane * 16;
    STAMP(0);
    __builtin_amdgcn_s_setprio(3);
    if (w < 6) dma16(skv, vo, (unsigned)w * 1024u, l0 + 3 * AT_CHUNK + (unsigned)w * 1024u);
#pragma unroll
    for (int c = 0; c < 2; ++c)
#pragma unroll
        for (int j = 0; j < 3; ++j) dma16(skv, vo, (unsigned)(6144 + c * AT_CHUNK + (3 * w + j) * 1024), l0 + (unsigned)(c * AT_CHUNK + (3 * w + j) * 1024));
    const int tile = qb * 8 + w;
    const char* qp = ws + WS_QF + (size_t)(head * 64 + tile) * 1280;
    const bf16x8 q1 = *(const bf16x8*)(qp + lane * 16);
    s16x4 q2 = *(const s16x4*)(qp + 1024 + r * 8);
    if (h) q2 = (s16x4){0, 0, 0, 0};
    const int vrow = r < 21 ? r : 21;
    const unsigned kbase = l0 + lane * 16, k2base = l0 + 1024 + lane * 8, vbase = l0 + 1536 + h * 384 + vrow * 16;
    WAIT_VM_BAR(6);
    STAMP(1);
    {
        const bf16x8 k1 = lds_ld128((lds_cptr)lds + 3 * AT_CHUNK + lane * 16); const s16x4 k2 = lds_ld64((lds_cptr)lds + 3 * AT_CHUNK + 1024 + lane * 8);
        f32x16 s = mfma16(k1, q1, f32x16{});
        s = mfma8(k2, q2, s);
        float tm = s[0];
#pragma unroll
        for (int i = 1; i < 16; ++i) tm = fmaxf(tm, s[i]);
        tm = fmaxf(tm, __shfl_xor(tm, 32));
        const unsigned nb = pk2(-tm, 0.f) & 0xffffu;
        if (h) q2[0] = (short)nb;
    }
    const float gq = ((const float*)(ws + WS_G))[tile * 32 + r];
    asm volatile("" :: "v"(gq));
    f32x16 o = f32x16{};
    __builtin_amdgcn_s_setprio(A_PRIO);
    STAMP(2);
    {
        unsigned ka = kbase + 3 * AT_CHUNK, kb = k2base + 3 * AT_CHUNK, va = vbase + 3 * AT_CHUNK;
        unsigned soff = 6144 + 2 * AT_CHUNK + w * 3072, ldst, cur = 0, cnt = 8;
        const unsigned wbase = l0 + w * 3072;
        asm volatile(ATT_LOOP_ASM
                     : "+v"(o), "+v"(ka), "+v"(kb), "+v"(va), "+s"(soff), "=&s"(ldst), "+s"(cur), "+s"(cnt)
                     : "v"(q1), "v"(q2), "v"(vo), "s"(skv), "v"(kbase), "v"(k2base), "v"(vbase), "s"(wbase)
                     : "memory", "scc", ATT_CLOBBERS);
    }
    STAMP(3);
    float l = __shfl(o[8], r + 32);
    bool ok = (l > 9.094947e-13f) && (l < 1.8446744e19f);
#pragma unroll
    for (int i = 0; i < 12; ++i) ok = ok && (fabsf(o[i]) < INFINITY || (h && i >= 8));
    if (!__all(ok)) {
        if (h) q2[0] = 0;
        attn_slow(kvh, q1, q2, lane, o);
        l = __shfl(o[8], r + 32);
    }
    const float sc = gq * __builtin_amdgcn_rcpf(l);
    const __amdgpu_buffer_rsrc_t ro = make_rsrc(ws + WS_OF);
    const unsigned ob = (unsigned)tile * 40u * 1024u;
    u32x4 m = {pk2(o[0] * sc, o[1] * sc), pk2(o[2] * sc, o[3] * sc), pk2(o[4] * sc, o[5] * sc), pk2(o[6] * sc, o[7] * sc)};
    st16_sc1(ro, ob + (unsigned)(head * 1024 + lane * 16), m);
    if (h == 0) {
        u32x2 e = {pk2(o[8] * sc, o[9] * sc), pk2(o[10] * sc, o[11] * sc)};
        st8_sc1(ro, ob + (unsigned)((32 + (head >> 2)) * 1024 + (((head >> 1) & 1) * 32 + r) * 16 + (head & 1) * 8), e);
    }
    publish(ws, qb);
    STAMP(4);
}

DEV void bufld16(bf16x8& dst, const i32x4& srd, unsigned voff, unsigned soff) {
    asm volatile(PADS "buffer_load_dwordx4 %0, %1, %2, %3 offen" : "=v"(dst) : "v"(voff), "s"(srd), "s"(RFL(soff)) : "memory");
}
DEV void bufld16_o(bf16x8& dst, const i32x4& srd, unsigned voff, unsigned soff) {
    asm volatile(PADS "buffer_load_dwordx4 %0, %1, %2, %3 offen sc1" : "=v"(dst) : "v"(voff), "s"(srd), "s"(RFL(soff)) : "memory");
}
#define WAIT_VM_2(n, a, b) asm volatile("s_waitcnt vmcnt(" #n ")" : "+v"(a), "+v"(b) :: "memory")

template <int N> DEV void pin_vm(bf16x8& a) { asm volatile("s_waitcnt vmcnt(%1)" : "+v"(a) : "n"(N) : "memory"); }
template <int N> DEV void pin_vm(bf16x8& a, bf16x8& b, bf16x8& c) { asm volatile("s_waitcnt vmcnt(%3)" : "+v"(a), "+v"(b), "+v"(c) : "n"(N) : "memory"); }
template <int N> DEV void pin_vm(bf16x8& a, bf16x8& b, bf16x8& c, bf16x8& d) { asm volatile("s_waitcnt vmcnt(%4)" : "+v"(a), "+v"(b), "+v"(c), "+v"(d) : "n"(N) : "memory"); }
template <int N> DEV void pin_vm(bf16x8& a, bf16x8& b, bf16x8& c, bf16x8& d, bf16x8& e, bf16x8& f) { asm volatile("s_waitcnt vmcnt(%6)" : "+v"(a), "+v"(b), "+v"(c), "+v"(d), "+v"(e), "+v"(f) : "n"(N) : "memory"); }
constexpr int P2_NF = 24, P2_PD = 3, P2_AD = 6;
constexpr int p2_allowed(int s, bool up) {
    int idx[256] = {}, n = 0;
    const int nw = up ? 2 : 3;
    auto A = [&](int k) { idx[n] = k; ++n; };
    auto W = [&](int k) { for (int i = 0; i < nw; ++i) { idx[n] = k; ++n; } };
    for (int k = 0; k < P2_PD; ++k) W(k);
    for (int k = 0; k < P2_AD; ++k) A(k);
    for (int u = 0; u < s; ++u) { if (u + P2_PD < P2_NF + 2) W(u + P2_PD); if (u + P2_AD < P2_NF) A(u + P2_AD); }
    int last = -1;
    for (int i = 0; i < n; ++i) if (idx[i] <= s) last = i;
    return n - 1 - last;
}

DEV void g_role(char* ws, char* out, int item, lds_ptr lds) {
    constexpr int SK = 1;
    const int tid = threadIdx.x, lane = tid & 63, w = __builtin_amdgcn_readfirstlane(tid >> 6);
    const int r = lane & 31, h = lane >> 5;
    const int xcd = item & 7, j = item >> 3;
    const unsigned l0 = (unsigned)(uintptr_t)lds;
    STAMP(0);
    __builtin_amdgcn_s_setprio(G_PRIO);
    const int reg = j >> 2, sub = j & 3, kg = w & 3;
    const bool up = w >= 4;
    const int rgp = (xcd & 3) * 4 + (reg & 3), cgp = (xcd >> 2) * 2 + (reg >> 2);
    const int rp = sub >> 1, cp = sub & 1;
    const int t0 = rgp * 4 + 2 * rp + cp, t1 = rgp * 4 + 2 * rp + (cp ^ 1);
    const int c0 = cgp * 128 + cp * 64, c4 = 512 + cgp * 32;
    const int ct0 = cgp * 4 + cp * 2, ct4 = 16 + cgp;
    const int tme = up ? t1 : t0;
    const i32x4 swf = make_srd(ws + WS_WF), sh = make_srd(ws + WS_HF), so_ = make_srd(ws + WS_OF);
    const unsigned va = lane * 16;
    f32x16 acc[3];
#pragma unroll
    for (int i = 0; i < 3; ++i) acc[i] = f32x16{};
    const float gm = 1.f - ((const float*)(ws + WS_G))[tme * 32 + r];
    asm volatile("" :: "v"(gm));
    auto run = [&](auto upc) {
        constexpr bool UP = decltype(upc)::value;
        constexpr int NW = UP ? 2 : 3;
        bf16x8 wf[P2_PD][3];
        auto wbase = [&](int s) -> unsigned { return s < P2_NF ? (unsigned)(WF_WD + (kg * 24 + s) * 20) * 1024u : (unsigned)(WF_WO + (kg * 10 + s - P2_NF) * 20) * 1024u; };
        auto issue_w = [&](int s, int slot) {
            const unsigned fb = wbase(s);
            bufld16(wf[slot][0], swf, va, fb + (unsigned)ct0 * 1024u); bufld16(wf[slot][1], swf, va, fb + (unsigned)(ct0 + 1) * 1024u);
            if constexpr (!UP) bufld16(wf[slot][2], swf, va, fb + (unsigned)ct4 * 1024u);
        };
        auto mma = [&](const bf16x8& w0, const bf16x8& w1, const bf16x8& w2, const bf16x8& a) {
            acc[0] = mfma16(w0, a, acc[0]); acc[1] = mfma16(w1, a, acc[1]);
            if constexpr (!UP) acc[2] = mfma16(w2, a, acc[2]);
        };
        auto pin = [&](auto nc, bf16x8& a, int slot) {
            constexpr int N = decltype(nc)::value;
            if constexpr (UP) pin_vm<N>(a, wf[slot][0], wf[slot][1]); else pin_vm<N>(a, wf[slot][0], wf[slot][1], wf[slot][2]);
        };
        {
            bf16x8 af[P2_AD];
            auto issue_a = [&](int s) { bufld16(af[s % P2_AD], sh, va, (unsigned)(tme * 96 + kg * 24 + s) * 1024u); };
#pragma unroll
            for (int s = 0; s < P2_PD; ++s) issue_w(s, s);
#pragma unroll
            for (int s = 0; s < P2_AD; ++s) issue_a(s);
            STAMP(3);
            auto step = [&](auto sc) {
                constexpr int s = decltype(sc)::value;
                pin(std::integral_constant<int, p2_allowed(s, UP)>{}, af[s % P2_AD], s % P2_PD);
                mma(wf[s % P2_PD][0], wf[s % P2_PD][1], wf[s % P2_PD][2], af[s % P2_AD]);
                SBAR();
                if constexpr (s + P2_PD < P2_NF + 2) issue_w(s + P2_PD, (s + P2_PD) % P2_PD);
                if constexpr (s + P2_AD < P2_NF) issue_a(s + P2_AD);
                SBAR();
            };
            static_for<0, P2_NF>(step);
        }
#pragma unroll
        for (int i = 0; i < 16; ++i) { acc[0][i] *= gm; acc[1][i] *= gm; acc[2][i] *= gm; }
#pragma unroll
        for (int q = 0; q < 3; ++q) {
            const int s = 26 + 2 * q + (UP ? 1 : 0);
            const unsigned fb = wbase(s), lb = l0 + (unsigned)kg * 18432u + (unsigned)(s - 26) * 3072u;
            dma16(swf, va, fb + (unsigned)ct0 * 1024u, lb); dma16(swf, va, fb + (unsigned)(ct0 + 1) * 1024u, lb + 1024u); dma16(swf, va, fb + (unsigned)ct4 * 1024u, lb + 2048u);
        }
        if constexpr (UP) pin_vm<0>(wf[0][0], wf[0][1], wf[1][0], wf[1][1]); else pin_vm<0>(wf[0][0], wf[0][1], wf[0][2], wf[1][0], wf[1][1], wf[1][2]);
        STAMP(4);
        consume<false>(ws, rgp >> 1, 32);
        STAMP(5);
        bf16x8 ao[10];
#pragma unroll
        for (int i = 0; i < 10; ++i) bufld16_o(ao[i], so_, va, (unsigned)(tme * 40 + kg * 10 + i) * 1024u);
        const lds_cptr wl = (lds_cptr)lds + kg * 18432 + lane * 16;
        auto ldw = [&](int s) { wf[2][0] = lds_ld128(wl + (s - 26) * 3072); wf[2][1] = lds_ld128(wl + (s - 26) * 3072 + 1024); if constexpr (!UP) wf[2][2] = lds_ld128(wl + (s - 26) * 3072 + 2048); };
        pin(std::integral_constant<int, 9>{}, ao[0], 0);
        mma(wf[0][0], wf[0][1], wf[0][2], ao[0]); SBAR();
        issue_w(32, 0); SBAR();
        pin(std::integral_constant<int, 8 + NW>{}, ao[1], 1);
        mma(wf[1][0], wf[1][1], wf[1][2], ao[1]); SBAR();
        issue_w(33, 1); ldw(26); SBAR();
        auto stepo = [&](auto sc) {
            constexpr int s = decltype(sc)::value;
            pin_vm<33 - s + 2 * NW>(ao[s - 24]);
            mma(wf[2][0], wf[2][1], wf[2][2], ao[s - 24]);
            SBAR();
            if constexpr (s < 31) ldw(s + 1);
            SBAR();
        };
        static_for<26, 32>(stepo);
        pin(std::integral_constant<int, NW>{}, ao[8], 0);
        mma(wf[0][0], wf[0][1], wf[0][2], ao[8]); SBAR();
        pin(std::integral_constant<int, 0>{}, ao[9], 1);
        mma(wf[1][0], wf[1][1], wf[1][2], ao[9]);
    };
    if (up) run(std::true_type{}); else run(std::false_type{});
    STAMP(6);
    typedef __attribute__((ext_vector_type(4))) float f32x4;
    typedef __attribute__((address_space(3))) f32x4* lds_f4;
    __syncthreads();
    {
        lds_ptr mine = lds + kg * 20480 + r * 128;
#pragma unroll
        for (int i = 0; i < 3; ++i) {
            if (i == 2 && up) break;
            const int tl = up ? 2 + i : (i == 2 ? 4 : i);
#pragma unroll
            for (int g = 0; g < 4; ++g) {
                f32x4 v = {acc[i][4 * g], acc[i][4 * g + 1], acc[i][4 * g + 2], acc[i][4 * g + 3]};
                *(lds_f4)(mine + tl * 4096 + (((2 * g + h) ^ (r & 7)) * 16)) = v;
            }
        }
    }
    __syncthreads();
#pragma unroll
    for (int it = 0; it < 2; ++it) {
        const int u = tid + 512 * it;
        if (u < 640) {
            const int tl = u >> 7, tok = (u & 127) >> 2, ch = u & 3;
            f32x4 lo = {0, 0, 0, 0}, hi = {0, 0, 0, 0};
#pragma unroll
            for (int ww = 0; ww < 4; ++ww) {
                lds_ptr p = lds + ww * 20480 + tl * 4096 + tok * 128;
                lo += *(lds_f4)(p + (((2 * ch) ^ (tok & 7)) * 16));
                hi += *(lds_f4)(p + (((2 * ch + 1) ^ (tok & 7)) * 16));
            }
            const int tt = (tl == 2 || tl == 3) ? t1 : t0, cc = tl == 4 ? c4 : c0 + (tl & 1) * 32;
            u32x4 o = {pk2(lo[0], lo[1]), pk2(lo[2], lo[3]), pk2(hi[0], hi[1]), pk2(hi[2], hi[3])};
            *(u32x4*)(out + (size_t)(tt * 32 + tok) * 1280 + (cc + ch * 8) * 2) = o;
        }
    }
    STAMP(7);
}

__global__ __launch_bounds__(512, 4) void k2_kernel(char* __restrict__ ws, char* __restrict__ out) {
    extern __shared__ __attribute__((aligned(16))) char lds_raw[];
    lds_ptr lds = (lds_ptr)lds_raw;
    if (blockIdx.x < 256) attn_role(ws, blockIdx.x, lds);
    else g_role(ws, out, blockIdx.x - 256, lds);
}

extern "C" void kernel_launch(void* const* d_in, const int* in_sizes, int n_in, void* d_out, int out_size, void* d_ws, size_t ws_size, hipStream_t stream) {
    const char* ax = (const char*)d_in[0];
    const char* wqkv = (const char*)d_in[1];
    const char* wo = (const char*)d_in[2];
    const char* wg = (const char*)d_in[4];
    const char* wu = (const char*)d_in[5];
    const char* wd = (const char*)d_in[6];
    const char* gw = (const char*)d_in[7];
    char* ws = (char*)d_ws;
    (void)hipFuncSetAttribute((const void*)k1_kernel, hipFuncAttributeMaxDynamicSharedMemorySize, K1_LDS);
    (void)hipFuncSetAttribute((const void*)k2_kernel, hipFuncAttributeMaxDynamicSharedMemorySize, 81920);
    k1_kernel<<<256, 512, K1_LDS, stream>>>(ax, wqkv, gw, wg, wu, wd, wo, ws);
    k2_kernel<<<512, 512, 81920, stream>>>(ws, (char*)d_out);
}
```

```cpp
#define A_PRIO 1
#define G_PRIO 0
#define POLL_SLEEP 8
#define K1_POL 2
#define PACE 0
#include <hip/hip_runtime.h>
#include <hip/hip_bf16.h>
#include <stdint.h>
#include <type_traits>

typedef __attribute__((ext_vector_type(8))) __bf16 bf16x8;
typedef __attribute__((ext_vector_type(8))) short s16x8;
typedef __attribute__((ext_vector_type(4))) short s16x4;
typedef __attribute__((ext_vector_type(2))) __bf16 bf16x2;
typedef __attribute__((ext_vector_type(2))) float f32x2;
typedef __attribute__((ext_vector_type(16))) float f32x16;
typedef __attribute__((ext_vector_type(4))) unsigned u32x4;
typedef __attribute__((ext_vector_type(2))) unsigned u32x2;
typedef __attribute__((ext_vector_type(4))) int i32x4;
typedef __attribute__((address_space(3))) const char* lds_cptr;
typedef __attribute__((address_space(3))) char* lds_ptr;

#define DEV __device__ __forceinline__
#define PHMUL 1
#define RL_AT 2
#define RL_DO 6
#define WARM_POL ""
#define WARM_AT 1

constexpr int T_ = 2048, D_ = 640, NH = 32, DH = 20, F_ = 1536;
constexpr float QSCALE = 0.22360679774997896f * 1.4426950408889634f;

constexpr size_t WS_QF = 0;
constexpr size_t WS_KV = WS_QF + (size_t)NH * 64 * 1280;
constexpr size_t WS_HF = WS_KV + (size_t)NH * 64 * 3072;
constexpr size_t WS_OF = WS_HF + (size_t)64 * 96 * 1024;
constexpr size_t WS_WF = WS_OF + (size_t)64 * 40 * 1024;
constexpr int WF_WD = 0, WF_WO = 1920, WF_N = 2720;
constexpr size_t WS_G  = WS_WF + (size_t)WF_N * 1024;
constexpr size_t WS_FLAG = WS_G + 2048 * 4;
constexpr int FLAG_LINES = 8;
constexpr size_t WS_END = WS_FLAG + FLAG_LINES * 256;

DEV i32x4 make_srd(const void* p) {
    i32x4 s;
    s[0] = __builtin_amdgcn_readfirstlane((int)(uint32_t)(uintptr_t)p);
    s[1] = __builtin_amdgcn_readfirstlane((int)(uint32_t)((uintptr_t)p >> 32)) & 0xffff;
    s[2] = 0x7fffffff;
    s[3] = 0x00020000;
    return s;
}
#define RFL(x) (x)
#define PADS
DEV void dma16(const i32x4& srd, unsigned voff, unsigned soff, unsigned ldsaddr) {
    asm volatile(PADS "s_mov_b32 m0, %3\n\ts_nop 0\n\tbuffer_load_dwordx4 %0, %1, %2 offen lds"
                 :: "v"(voff), "s"(srd), "s"(RFL(soff)), "s"(RFL(ldsaddr)) : "memory");
}
DEV void warm(unsigned& sink, const char* base, unsigned voff) {
    asm volatile("global_load_dword %0, %1, %2" : "=v"(sink) : "v"(voff), "s"(base) : "memory");
}
#define WAIT_VM(n) asm volatile("s_waitcnt vmcnt(" #n ")" ::: "memory")
#define WAIT_VM_BAR(n) asm volatile("s_waitcnt vmcnt(" #n ")\n\ts_barrier" ::: "memory")
#define SBAR() __builtin_amdgcn_sched_barrier(0)

DEV f32x16 mfma16(bf16x8 a, bf16x8 b, f32x16 c) { return __builtin_amdgcn_mfma_f32_32x32x16_bf16(a, b, c, 0, 0, 0); }
DEV f32x16 mfma8(s16x4 a, s16x4 b, f32x16 c) { return __builtin_amdgcn_mfma_f32_32x32x8bf16_1k(a, b, c, 0, 0, 0); }
DEV unsigned pk2(float lo, float hi) {
    f32x2 v = {lo, hi};
    bf16x2 r = __builtin_convertvector(v, bf16x2);
    return __builtin_bit_cast(unsigned, r);
}
DEV bf16x8 lds_ld128(lds_cptr p) { return *(const __attribute__((address_space(3))) bf16x8*)p; }
DEV s16x4 lds_ld64(lds_cptr p) { return *(const __attribute__((address_space(3))) s16x4*)p; }
DEV s16x4 lds_tr(lds_cptr p) { return __builtin_amdgcn_ds_read_tr16_b64_v4i16((__attribute__((address_space(3))) s16x4*)p); }
DEV bf16x8 cat8(s16x4 lo, s16x4 hi) {
    s16x8 r = __builtin_shufflevector(lo, hi, 0, 1, 2, 3, 4, 5, 6, 7);
    return __builtin_bit_cast(bf16x8, r);
}
DEV bf16x8 pack8(const f32x16& v, int base) {
    u32x4 r = {pk2(v[base], v[base + 1]), pk2(v[base + 2], v[base + 3]), pk2(v[base + 4], v[base + 5]), pk2(v[base + 6], v[base + 7])};
    return __builtin_bit_cast(bf16x8, r);
}

typedef __attribute__((address_space(1))) unsigned gu32;
#define RLX_AGENT __ATOMIC_RELAXED, __HIP_MEMORY_SCOPE_AGENT
DEV __amdgpu_buffer_rsrc_t make_rsrc(void* p) { return __builtin_amdgcn_make_buffer_rsrc(p, 0, 0x7fffffff, 0x00020000); }
DEV void st16_sc1(__amdgpu_buffer_rsrc_t rs, unsigned voff, u32x4 v) { __builtin_amdgcn_raw_buffer_store_b128(v, rs, voff, 0, 16); }
DEV void st8_sc1(__amdgpu_buffer_rsrc_t rs, unsigned voff, u32x2 v) { __builtin_amdgcn_raw_buffer_store_b64(v, rs, voff, 0, 16); }
#define K1_WT_AUX 16
DEV void st16_pol(__amdgpu_buffer_rsrc_t rs, unsigned voff, u32x4 v) { __builtin_amdgcn_raw_buffer_store_b128(v, rs, voff, 0, K1_WT_AUX); }
DEV void st8_pol(__amdgpu_buffer_rsrc_t rs, unsigned voff, u32x2 v) { __builtin_amdgcn_raw_buffer_store_b64(v, rs, voff, 0, K1_WT_AUX); }
DEV void publish(char* ws, int line) {
    asm volatile("s_waitcnt vmcnt(0)\n\ts_barrier" ::: "memory");
    if (threadIdx.x == 0) __hip_atomic_fetch_add((gu32*)(ws + WS_FLAG + line * 256), 1u, RLX_AGENT);
}
template <bool ACQ = true>
DEV void consume(char* ws, int line, unsigned target) {
    if (threadIdx.x < 64) {
        gu32* f = (gu32*)(ws + WS_FLAG + line * 256);
        for (unsigned spins = 0; spins < (1u << 20); ++spins) {
            if ((unsigned)__builtin_amdgcn_readfirstlane(__hip_atomic_load(f, RLX_AGENT)) >= target) break;
            __builtin_amdgcn_s_sleep(POLL_SLEEP);
        }
        if constexpr (ACQ) {
            __builtin_amdgcn_fence(__ATOMIC_ACQUIRE, "agent");
            asm volatile("s_waitcnt vmcnt(0)" ::: "memory");
        }
    }
    asm volatile("s_barrier" ::: "memory");
}

#define STAMP(i) do {} while (0)
constexpr int K1_XS = 16384, K1_WS = 20480, K1_WB = 3 * K1_XS, K1_GW = K1_WB + 5 * K1_WS, K1_LDS = K1_GW + 1280 + 64;

DEV f32x2 gelu_mul2(f32x2 x, f32x2 u) {
    const float c0 = -2.0f * 0.7978845608028654f * 1.4426950408889634f, c1 = c0 * 0.044715f;
    const f32x2 t = x * __builtin_elementwise_fma(x * x, (f32x2){c1, c1}, (f32x2){c0, c0});
    f32x2 d = {__builtin_amdgcn_exp2f(t[0]), __builtin_amdgcn_exp2f(t[1])};
    d += (f32x2){1.0f, 1.0f};
    const f32x2 rc = {__builtin_amdgcn_rcpf(d[0]), __builtin_amdgcn_rcpf(d[1])};
    return (x * u) * rc;
}
DEV float gelu_tanh(float x) {
    const float c0 = -2.0f * 0.7978845608028654f * 1.4426950408889634f, c1 = c0 * 0.044715f;
    const float e = __builtin_amdgcn_exp2f(x * __builtin_fmaf(x * x, c1, c0));
    return x * __builtin_amdgcn_rcpf(1.0f + e);
}
template <int I, int N, class F> DEV void static_for(F&& f) { if constexpr (I < N) { f(std::integral_constant<int, I>{}); static_for<I + 1, N>(f); } }
template <int N> DEV void wait_vm() { asm volatile("s_waitcnt vmcnt(%0)" :: "n"(N) : "memory"); }
template <int N> DEV void wait_vm_bar() { asm volatile("s_waitcnt vmcnt(%0)\n\ts_barrier" :: "n"(N) : "memory"); }

constexpr int k1_allowed(int t) {
    int kind[160] = {}, idx[160] = {}, n = 0;
    auto P = [&](int k, int i, int c) { for (int q = 0; q < c; ++q) { kind[n] = k; idx[n] = i; ++n; } };
    P(0, 0, 4); P(1, 0, 5); P(1, 1, 5); P(0, 1, 4); P(1, 2, 5); P(1, 3, 5);
    for (int u = 0; u < t; ++u) { if ((u & 1) == 0 && u / 2 + 2 < 10) P(0, u / 2 + 2, 4); if (u + 4 < 20) P(1, u + 4, 5); if (u == 16) P(2, 0, 4); }
    int last = -1;
    for (int i = 0; i < n; ++i) {
        const bool need = (kind[i] == 0 && idx[i] <= ((t + 1) >> 1)) || (kind[i] == 1 && idx[i] <= t + 1);
        if (need) last = i;
    }
    return n - 1 - last;
}

DEV void k1_st16(char* ws, char* p, u32x4 v) { __builtin_amdgcn_raw_buffer_store_b128(v, make_rsrc(ws), (unsigned)(p - ws), 0, K1_POL); }
DEV void k1_st8(char* ws, char* p, u32x2 v) { __builtin_amdgcn_raw_buffer_store_b64(v, make_rsrc(ws), (unsigned)(p - ws), 0, K1_POL); }
template <int MODE>
DEV void k1_gemm(const char* x, const char* wqkv, const char* gw, const char* wg, const char* wu, const char* wd, const char* wo, char* ws, int mt, int j, lds_ptr lds) {
    constexpr int SK = 0;
    const int tid = threadIdx.x, lane = tid & 63, w8 = __builtin_amdgcn_readfirstlane(tid >> 6), w = w8 & 3;
    const int wt = w >> 1, ww = w & 1, r = lane & 31, h = lane >> 5;
    const int m0 = mt * 128, n0 = (j < 15 ? j : 14) * 128, f0 = j * 96;
    const unsigned l0 = (unsigned)(uintptr_t)lds;
    const int ph = (PHMUL * ((mt & 7) + 3 * (j & 3))) % 10;
    STAMP(0);
    if (w8 >= 4) {
        const i32x4 sx = make_srd(x), sw = make_srd(wqkv), sg = make_srd(wg), su = make_srd(wu);
        const unsigned xq = lane >> 4;
        const unsigned vx0 = (unsigned)(lane >> 3) * 1280u + (unsigned)(((lane & 7) ^ xq) * 16);
        const unsigned vx1 = (unsigned)(lane >> 3) * 1280u + (unsigned)(((lane & 7) ^ xq ^ 4) * 16);
        const unsigned vq = (unsigned)(lane >> 4) * 3840u + (unsigned)(((lane & 15) ^ ((lane >> 4) << 2)) * 16);
        unsigned vf[3];
#pragma unroll
        for (int i = 0; i < 3; ++i) { const int p = 3 * w + i, L = (p % 6) * 64 + lane, row = L / 12, c16 = L - row * 12; vf[i] = (unsigned)row * 3072u + (unsigned)((p < 6 ? c16 : (c16 ^ 2)) * 16); }
        const unsigned sxb = (unsigned)(m0 + 32 * w) * 1280u, sqb = (unsigned)(8 * w) * 3840u + (unsigned)n0 * 2u, sfb = (unsigned)f0 * 2u;
        auto issue_x = [&](int u) {
            const unsigned lb = l0 + (unsigned)(u % 3) * K1_XS;
            const int ur = u + ph < 10 ? u + ph : u + ph - 10;
#pragma unroll
            for (int i = 0; i < 4; ++i) dma16(sx, (i & 1) ? vx1 : vx0, sxb + (unsigned)i * 10240u + (unsigned)ur * 128u, lb + (unsigned)(4 * w + i) * 1024u);
        };
        auto issue_w = [&](int t) {
            const unsigned lb = l0 + K1_WB + (unsigned)(t % 5) * K1_WS;
            const int tr = t + 2 * ph < 20 ? t + 2 * ph : t + 2 * ph - 20;
#pragma unroll
            for (int i = 0; i < 2; ++i) dma16(sw, vq, sqb + (unsigned)(tr * 32 + 4 * i) * 3840u, lb + (unsigned)(2 * w + i) * 1024u);
            const unsigned so = sfb + (unsigned)tr * (32u * 3072u);
            if (w < 2) {
#pragma unroll
                for (int i = 0; i < 3; ++i) dma16(sg, vf[i], so, lb + 8192u + (unsigned)(3 * w + i) * 1024u);
            } else {
#pragma unroll
                for (int i = 0; i < 3; ++i) dma16(su, vf[i], so, lb + 8192u + (unsigned)(3 * w + i) * 1024u);
            }
        };
        const int gwv = (mt * 16 + j) * 4 + w;
        const int u0 = gwv, u1 = gwv + 1024 < 1360 ? gwv + 1024 : gwv;
        const unsigned wl = l0 + K1_WB + (unsigned)w * 4096u;
        const char* rsrc[4];
        auto prep = [&](int i) {
            int u = (i >> 1) ? u1 : u0;
            asm volatile("" : "+s"(u) :: "memory");
            const int row = 8 * (i & 1) + (lane >> 3), c16 = lane & 7;
            if (u < 960) { const int ks = u / 10, cq = u - ks * 10; rsrc[i] = wd + (size_t)(16 * ks + row) * 1280 + cq * 128 + c16 * 16; }
            else { const int q = u - 960, ks = q / 10, cq = q - ks * 10;
                   const int k = ks < 32 ? 20 * ks + row : 20 * (4 * (ks - 32) + 2 * ((row >> 2) & 1) + (row >> 3)) + 16 + (row & 3);
                   rsrc[i] = wo + (size_t)k * 1280 + cq * 128 + c16 * 16; }
            asm volatile("" : "+v"(rsrc[i]) :: "memory");
        };
        auto rl_issue = [&]() {
#pragma unroll
            for (int i = 0; i < 4; ++i) { const char* p = rsrc[i]; const unsigned d = wl + (unsigned)i * 1024u; asm volatile("s_mov_b32 m0, %1\n\ts_nop 0\n\tglobal_load_lds_dwordx4 %0, off" :: "v"(p), "s"(d) : "memory"); }
        };
        issue_x(0); issue_w(0);
        __builtin_amdgcn_s_sleep(PACE); issue_w(1);
        __builtin_amdgcn_s_sleep(PACE); issue_x(1); issue_w(2);
        __builtin_amdgcn_s_sleep(PACE); issue_w(3);
        wait_vm_bar<k1_allowed(-1)>();
        auto top = [&](auto tc) {
            constexpr int t = decltype(tc)::value;
            wait_vm_bar<k1_allowed(t)>();
            if constexpr ((t & 1) == 0 && t / 2 + 2 < 10) issue_x(t / 2 + 2);
            if constexpr (t + 4 < 20) issue_w(t + 4);
            if constexpr (t >= 2 && t < 6) prep(t - 2);
            if constexpr (t == 16) rl_issue();
        };
        static_for<0, 19>(top);
        wait_vm<0>();
        {
            const int g = lane >> 4, nh = g & 1, q = (lane & 15) >> 2, p = lane & 3;
            lds_cptr base = (lds_cptr)lds + K1_WB + w * 4096;
            const __amdgpu_buffer_rsrc_t rw = make_rsrc(ws + WS_WF);
            bf16x8 v[4];
#pragma unroll
            for (int i = 0; i < 4; ++i) { lds_cptr pp = base + (i >> 1) * 2048 + (4 * h + q) * 128 + (i & 1) * 64 + nh * 32 + p * 8; v[i] = cat8(lds_tr(pp), lds_tr(pp + 1024)); }
#pragma unroll
            for (int i = 0; i < 4; ++i) k1_st16(ws, ws + WS_WF + (size_t)(2 * ((i >> 1) ? u1 : u0) + (i & 1)) * 1024 + lane * 16, __builtin_bit_cast(u32x4, v[i]));
            (void)rw;
        }
        return;
    }
    if constexpr (MODE == 2) {
        if (tid < 80) *(__attribute__((address_space(3))) u32x4*)(lds + K1_GW + tid * 16) = *(const u32x4*)(gw + tid * 16);
        else if (tid == 80) *(__attribute__((address_space(3))) u32x4*)(lds + K1_GW + 1280) = (u32x4){0u, 0u, 0u, 0u};
    }
    lds_cptr xa[4], qa[2], fa[3];
#pragma unroll
    for (int s = 0; s < 4; ++s) xa[s] = (lds_cptr)lds + (wt * 64 + r) * 128 + (((2 * s + h) ^ ((r >> 1) & 7)) * 16);
    {
        const int g = lane >> 4, nh = g & 1, q = (lane & 15) >> 2, p = lane & 3;
#pragma unroll
        for (int ct = 0; ct < 2; ++ct) {
            const int slot = ww * 8 + ct * 4 + 2 * nh + (p >> 1);
            qa[ct] = (lds_cptr)lds + K1_WB + (8 * h + q) * 256 + ((slot ^ (q << 2)) * 16) + 8 * (p & 1);
        }
#pragma unroll
        for (int a = 0; a < 3; ++a) {
            const int aa = ww * 3 + a, c16 = 2 * (nh ? (aa ^ 1) : aa) + (p >> 1);
            fa[a] = (lds_cptr)lds + K1_WB + 8192 + nh * 6144 + (8 * h + q) * 192 + c16 * 16 + 8 * (p & 1);
        }
    }
    const lds_cptr ga = (lds_cptr)lds + K1_GW + (r == 0 ? h * 16 : 1280);
    const int gstep = r == 0 ? 32 : 0;
    f32x16 aq[2][2], af[3][2];
#pragma unroll
    for (int i = 0; i < 2; ++i) { aq[i][0] = f32x16{}; aq[i][1] = f32x16{}; }
#pragma unroll
    for (int a = 0; a < 3; ++a) { af[a][0] = f32x16{}; af[a][1] = f32x16{}; }
    bf16x8 xf[2][2], qf[2][2], ff[2][3];
    auto rd_x = [&](int b, int t, int s, int tt) { xf[b][tt] = lds_ld128(xa[2 * (t & 1) + s] + ((t >> 1) % 3) * K1_XS + tt * 4096); };
    auto rd_q = [&](int b, int t, int s, int ct) {
        if constexpr (MODE == 2) { if (ct == 0) { const int tr = t + 2 * ph < 20 ? t + 2 * ph : t + 2 * ph - 20; qf[b][0] = lds_ld128(ga + (2 * tr + s) * gstep); } }
        else qf[b][ct] = cat8(lds_tr(qa[ct] + (t % 5) * K1_WS + s * 4096), lds_tr(qa[ct] + (t % 5) * K1_WS + s * 4096 + 1024));
    };
    auto rd_f = [&](int b, int t, int s, int a) { ff[b][a] = cat8(lds_tr(fa[a] + (t % 5) * K1_WS + s * 3072), lds_tr(fa[a] + (t % 5) * K1_WS + s * 3072 + 768)); };
    asm volatile("s_waitcnt lgkmcnt(0)\n\ts_barrier" ::: "memory");
    STAMP(1);
    rd_x(0, 0, 0, 0); rd_x(0, 0, 0, 1); rd_q(0, 0, 0, 0); rd_q(0, 0, 0, 1); rd_f(0, 0, 0, 0); rd_f(0, 0, 0, 1); rd_f(0, 0, 0, 2);
    auto stage = [&](auto tc) {
        constexpr int t = decltype(tc)::value;
        if constexpr (t < 19) asm volatile("s_barrier" ::: "memory");
#pragma unroll
        for (int s = 0; s < 2; ++s) {
            const int c = s, nb = s ^ 1;
            const bool more = s == 0 || t < 19;
            const int tn = s == 0 ? t : t + 1, sn = s ^ 1;
            auto mq = [&](int ct, int tt) {
                if constexpr (MODE == 0) aq[ct][tt] = mfma16(qf[c][ct], xf[c][tt], aq[ct][tt]);
                else if constexpr (MODE == 1) aq[tt][ct] = mfma16(xf[c][tt], qf[c][ct], aq[tt][ct]);
                else { if (ct == 0) aq[0][tt] = mfma16(qf[c][0], xf[c][tt], aq[0][tt]); }
            };
            auto mf = [&](int a, int tt) { af[a][tt] = mfma16(ff[c][a], xf[c][tt], af[a][tt]); };
            mq(0, 0); SBAR(); if (more) { rd_x(nb, tn, sn, 0); rd_x(nb, tn, sn, 1); } SBAR();
            mq(0, 1); SBAR(); if (more) rd_q(nb, tn, sn, 0); SBAR();
            mq(1, 0); SBAR(); if (more) rd_q(nb, tn, sn, 1); SBAR();
            mq(1, 1); SBAR(); if (more) rd_f(nb, tn, sn, 0); SBAR();
            mf(0, 0); SBAR(); if (more) rd_f(nb, tn, sn, 1); SBAR();
            mf(0, 1); SBAR(); if (more) rd_f(nb, tn, sn, 2); SBAR();
            mf(1, 0); SBAR(); mf(1, 1); SBAR(); mf(2, 0); SBAR(); mf(2, 1); SBAR();
        }
    };
    static_for<0, 20>(stage);
    STAMP(2);
    if constexpr (MODE == 0) {
        const bool isq = j < 5;
        const int csec = (isq ? n0 : n0 - 640) + ww * 64;
        char* base = ws + (isq ? WS_QF : WS_KV);
        const int tstride = isq ? 1280 : 3072;
        const float sc = isq ? QSCALE : 1.0f;
#pragma unroll
        for (int ct = 0; ct < 2; ++ct)
#pragma unroll
            for (int g = 0; g < 4; ++g) {
                const int qd = (csec + ct * 32 + 8 * g + 4 * h) >> 2, head = qd / 5, dq = qd - head * 5;
                const int inoff = dq < 4 ? (dq >> 1) * 512 + r * 16 + (dq & 1) * 8 : 1024 + r * 8;
#pragma unroll
                for (int tt = 0; tt < 2; ++tt) {
                    const int tile = mt * 4 + wt * 2 + tt;
                    char* p = base + (size_t)(head * 64 + tile) * tstride + inoff;
                    const f32x16& a = aq[ct][tt];
                    u32x2 v = {pk2(a[4 * g] * sc, a[4 * g + 1] * sc), pk2(a[4 * g + 2] * sc, a[4 * g + 3] * sc)};
                    k1_st8(ws, p, v);
                    if (!isq && dq == 4) k1_st8(ws, p + 256, (u32x2){0x00003f80u, 0u});
                }
            }
    } else if constexpr (MODE == 1) {
#pragma unroll
        for (int ct = 0; ct < 2; ++ct) {
            const int col = n0 - 1280 + ww * 64 + ct * 32 + r, head = col / 20, d = col - head * 20;
#pragma unroll
            for (int tt = 0; tt < 2; ++tt) {
                const int tile = mt * 4 + wt * 2 + tt;
                char* p = ws + WS_KV + (size_t)(head * 64 + tile) * 3072 + 1536 + h * 384 + d * 16;
#pragma unroll
                for (int s = 0; s < 2; ++s) {
                    k1_st16(ws, p + s * 768, __builtin_bit_cast(u32x4, pack8(aq[tt][ct], 8 * s)));
                    if (d == 0) k1_st16(ws, p + s * 768 + 320, (u32x4){0x3f803f80u, 0x3f803f80u, 0x3f803f80u, 0x3f803f80u});
                    if (d == 1) k1_st16(ws, p + s * 768 + 320, (u32x4){0u, 0u, 0u, 0u});
                }
            }
        }
    } else {
        if (ww == 0 && h == 0) {
#pragma unroll
            for (int tt = 0; tt < 2; ++tt) ((float*)(ws + WS_G))[m0 + wt * 64 + tt * 32 + r] = __builtin_amdgcn_rcpf(1.0f + __builtin_amdgcn_exp2f(-1.4426950408889634f * aq[0][tt][0]));
        }
        if (mt == 0 && tid < FLAG_LINES) *(unsigned*)(ws + WS_FLAG + tid * 256) = 0u;
    }
#pragma unroll
    for (int a = 0; a < 3; ++a)
#pragma unroll
        for (int tt = 0; tt < 2; ++tt) {
            f32x16 hv;
#pragma unroll
            for (int i = 0; i < 8; i += 2) { const f32x2 v = gelu_mul2((f32x2){af[a][tt][i], af[a][tt][i + 1]}, (f32x2){af[a][tt][8 + i], af[a][tt][9 + i]}); hv[i] = v[0]; hv[i + 1] = v[1]; }
            const int tile = mt * 4 + wt * 2 + tt, fs = j * 6 + ww * 3 + a;
            k1_st16(ws, ws + WS_HF + (size_t)(tile * 96 + fs) * 1024 + lane * 16, __builtin_bit_cast(u32x4, pack8(hv, 0)));
        }
    STAMP(3);
}

__global__ __launch_bounds__(512, 2) void k1_kernel(const char* __restrict__ x, const char* __restrict__ wqkv, const char* __restrict__ gw, const char* __restrict__ wg, const char* __restrict__ wu,
                                                   const char* __restrict__ wd, const char* __restrict__ wo, char* __restrict__ ws) {
    extern __shared__ __attribute__((aligned(16))) char lds_raw[];
    lds_ptr lds = (lds_ptr)lds_raw;
    const int b = blockIdx.x, xcd = b & 7, jj = b >> 3;
    const int mt = (xcd & 1) * 8 + (jj & 7), j = (xcd >> 1) * 4 + (jj >> 3);
    if (j < 10) k1_gemm<0>(x, wqkv, gw, wg, wu, wd, wo, ws, mt, j, lds);
    else if (j < 15) k1_gemm<1>(x, wqkv, gw, wg, wu, wd, wo, ws, mt, j, lds);
    else k1_gemm<2>(x, wqkv, gw, wg, wu, wd, wo, ws, mt, j, lds);
}

constexpr int AT_CHUNK = 24576;

DEV void attn_slow(const char* kvh, bf16x8 q1, s16x4 q2, int lane, f32x16& o) {
    const int r = lane & 31, h = lane >> 5;
    const int vrow = r < 21 ? r : 21;
    float m = -INFINITY;
    o = f32x16{};
    for (int t = 0; t < 64; ++t) {
        const char* p = kvh + t * 3072;
        const bf16x8 k1 = *(const bf16x8*)(p + lane * 16);
        const s16x4 k2 = *(const s16x4*)(p + 1024 + lane * 8);
        const bf16x8 v0 = *(const bf16x8*)(p + 1536 + h * 384 + vrow * 16);
        const bf16x8 v1 = *(const bf16x8*)(p + 2304 + h * 384 + vrow * 16);
        f32x16 s = mfma16(k1, q1, f32x16{});
        s = mfma8(k2, q2, s);
        float tm = s[0];
#pragma unroll
        for (int i = 1; i < 16; ++i) tm = fmaxf(tm, s[i]);
        tm = fmaxf(tm, __shfl_xor(tm, 32));
        const float mn = fmaxf(m, tm);
        const float alpha = __builtin_amdgcn_exp2f(m - mn);
        m = mn;
#pragma unroll
        for (int i = 0; i < 16; ++i) { s[i] = __builtin_amdgcn_exp2f(s[i] - mn); o[i] *= alpha; }
        o = mfma16(v0, pack8(s, 0), o);
        o = mfma16(v1, pack8(s, 8), o);
    }
}

#define ATT_LOOP_ASM \
    "ds_read_b128 v[72:75], %1\n\t" \
    "ds_read_b64 v[80:81], %2\n\t" \
    "ds_read_b128 v[76:79], %1 offset:3072\n\t" \
    "ds_read_b64 v[82:83], %2 offset:3072\n\t" \
    "ds_read_b128 v[84:87], %3\n\t" \
    "ds_read_b128 v[88:91], %3 offset:768\n\t" \
    "ds_read_b128 v[92:95], %3 offset:3072\n\t" \
    "ds_read_b128 v[96:99], %3 offset:3840\n\t" \
    "s_waitcnt lgkmcnt(6)\n\t" \
    "v_mfma_f32_32x32x16_bf16 v[32:47], v[72:75], %8, 0\n\t" \
    "v_mfma_f32_32x32x8_bf16 v[32:47], v[80:81], %9, v[32:47]\n\t" \
    "s_nop 15\n\t" \
    "1:\n\t" \
    "s_waitcnt vmcnt(3) lgkmcnt(0)\n\t" \
    "s_barrier\n\t" \
    "v_add_u32 %1, %6, %12\n\t" \
    "v_add_u32 %2, %6, %13\n\t" \
    "v_add_u32 %3, %6, %14\n\t" \
    "s_sub_u32 %5, %6, 0x6000\n\t" \
    "s_cmp_eq_u32 %6, 0\n\t" \
    "s_cselect_b32 %5, 0xc000, %5\n\t" \
    "s_add_u32 %5, %5, %15\n\t" \
    "v_mfma_f32_32x32x16_bf16 v[48:63], v[76:79], %8, 0\n\t" \
    "v_exp_f32 v32, v32\n\t" \
    "ds_read_b128 v[72:75], %1 offset:0\n\t" \
    "v_exp_f32 v33, v33\n\t" \
    "ds_read_b64 v[80:81], %2 offset:0\n\t" \
    "v_exp_f32 v34, v34\n\t" \
    "v_exp_f32 v35, v35\n\t" \
    "v_exp_f32 v36, v36\n\t" \
    "s_mov_b32 m0, %5\n\t" \
    "v_exp_f32 v37, v37\n\t" \
    "buffer_load_dwordx4 %10, %11, %4 offen offset:0 lds\n\t" \
    "v_exp_f32 v38, v38\n\t" \
    "v_exp_f32 v39, v39\n\t" \
    "v_mfma_f32_32x32x8_bf16 v[48:63], v[82:83], %9, v[48:63]\n\t" \
    "v_cvt_pk_bf16_f32 v64, v32, v33\n\t" \
    "v_cvt_pk_bf16_f32 v65, v34, v35\n\t" \
    "v_cvt_pk_bf16_f32 v66, v36, v37\n\t" \
    "v_cvt_pk_bf16_f32 v67, v38, v39\n\t" \
    "v_exp_f32 v40, v40\n\t" \
    "v_exp_f32 v41, v41\n\t" \
    "v_exp_f32 v42, v42\n\t" \
    "v_exp_f32 v43, v43\n\t" \
    "v_mfma_f32_32x32x16_bf16 %0, v[84:87], v[64:67], %0\n\t" \
    "ds_read_b128 v[84:87], %3 offset:0\n\t" \
    "v_exp_f32 v44, v44\n\t" \
    "v_exp_f32 v45, v45\n\t" \
    "v_exp_f32 v46, v46\n\t" \
    "v_exp_f32 v47, v47\n\t" \
    "v_cvt_pk_bf16_f32 v68, v40, v41\n\t" \
    "v_cvt_pk_bf16_f32 v69, v42, v43\n\t" \
    "v_cvt_pk_bf16_f32 v70, v44, v45\n\t" \
    "v_cvt_pk_bf16_f32 v71, v46, v47\n\t" \
    "s_waitcnt lgkmcnt(1)\n\t" \
    "v_mfma_f32_32x32x16_bf16 v[32:47], v[72:75], %8, 0\n\t" \
    "v_exp_f32 v48, v48\n\t" \
    "ds_read_b128 v[76:79], %1 offset:3072\n\t" \
    "v_exp_f32 v49, v49\n\t" \
    "ds_read_b64 v[82:83], %2 offset:3072\n\t" \
    "v_exp_f32 v50, v50\n\t" \
    "v_exp_f32 v51, v51\n\t" \
    "v_mfma_f32_32x32x16_bf16 %0, v[88:91], v[68:71], %0\n\t" \
    "ds_read_b128 v[88:91], %3 offset:768\n\t" \
    "v_exp_f32 v52, v52\n\t" \
    "v_exp_f32 v53, v53\n\t" \
    "buffer_load_dwordx4 %10, %11, %4 offen offset:1024 lds\n\t" \
    "v_exp_f32 v54, v54\n\t" \
    "v_exp_f32 v55, v55\n\t" \
    "v_mfma_f32_32x32x8_bf16 v[32:47], v[80:81], %9, v[32:47]\n\t" \
    "v_cvt_pk_bf16_f32 v64, v48, v49\n\t" \
    "v_cvt_pk_bf16_f32 v65, v50, v51\n\t" \
    "v_cvt_pk_bf16_f32 v66, v52, v53\n\t" \
    "v_cvt_pk_bf16_f32 v67, v54, v55\n\t" \
    "v_exp_f32 v56, v56\n\t" \
    "v_exp_f32 v57, v57\n\t" \
    "v_exp_f32 v58, v58\n\t" \
    "v_exp_f32 v59, v59\n\t" \
    "v_mfma_f32_32x32x16_bf16 %0, v[92:95], v[64:67], %0\n\t" \
    "ds_read_b128 v[92:95], %3 offset:3072\n\t" \
    "v_exp_f32 v60, v60\n\t" \
    "v_exp_f32 v61, v61\n\t" \
    "v_exp_f32 v62, v62\n\t" \
    "v_exp_f32 v63, v63\n\t" \
    "v_cvt_pk_bf16_f32 v68, v56, v57\n\t" \
    "v_cvt_pk_bf16_f32 v69, v58, v59\n\t" \
    "v_cvt_pk_bf16_f32 v70, v60, v61\n\t" \
    "v_cvt_pk_bf16_f32 v71, v62, v63\n\t" \
    "s_waitcnt lgkmcnt(1)\n\t" \
    "v_mfma_f32_32x32x16_bf16 v[48:63], v[76:79], %8, 0\n\t" \
    "v_exp_f32 v32, v32\n\t" \
    "ds_read_b128 v[72:75], %1 offset:6144\n\t" \
    "v_exp_f32 v33, v33\n\t" \
    "ds_read_b64 v[80:81], %2 offset:6144\n\t" \
    "v_exp_f32 v34, v34\n\t" \
    "v_exp_f32 v35, v35\n\t" \
    "v_mfma_f32_32x32x16_bf16 %0, v[96:99], v[68:71], %0\n\t" \
    "ds_read_b128 v[96:99], %3 offset:3840\n\t" \
    "v_exp_f32 v36, v36\n\t" \
    "v_exp_f32 v37, v37\n\t" \
    "buffer_load_dwordx4 %10, %11, %4 offen offset:2048 lds\n\t" \
    "v_exp_f32 v38, v38\n\t" \
    "v_exp_f32 v39, v39\n\t" \
    "v_mfma_f32_32x32x8_bf16 v[48:63], v[82:83], %9, v[48:63]\n\t" \
    "v_cvt_pk_bf16_f32 v64, v32, v33\n\t" \
    "v_cvt_pk_bf16_f32 v65, v34, v35\n\t" \
    "v_cvt_pk_bf16_f32 v66, v36, v37\n\t" \
    "v_cvt_pk_bf16_f32 v67, v38, v39\n\t" \
    "v_exp_f32 v40, v40\n\t" \
    "v_exp_f32 v41, v41\n\t" \
    "v_exp_f32 v42, v42\n\t" \
    "v_exp_f32 v43, v43\n\t" \
    "v_mfma_f32_32x32x16_bf16 %0, v[84:87], v[64:67], %0\n\t" \
    "ds_read_b128 v[84:87], %3 offset:6144\n\t" \
    "v_exp_f32 v44, v44\n\t" \
    "v_exp_f32 v45, v45\n\t" \
    "v_exp_f32 v46, v46\n\t" \
    "v_exp_f32 v47, v47\n\t" \
    "v_cvt_pk_bf16_f32 v68, v40, v41\n\t" \
    "v_cvt_pk_bf16_f32 v69, v42, v43\n\t" \
    "v_cvt_pk_bf16_f32 v70, v44, v45\n\t" \
    "v_cvt_pk_bf16_f32 v71, v46, v47\n\t" \
    "s_waitcnt lgkmcnt(1)\n\t" \
    "v_mfma_f32_32x32x16_bf16 v[32:47], v[72:75], %8, 0\n\t" \
    "v_exp_f32 v48, v48\n\t" \
    "ds_read_b128 v[76:79], %1 offset:9216\n\t" \
    "v_exp_f32 v49, v49\n\t" \
    "ds_read_b64 v[82:83], %2 offset:9216\n\t" \
    "v_exp_f32 v50, v50\n\t" \
    "v_exp_f32 v51, v51\n\t" \
    "v_mfma_f32_32x32x16_bf16 %0, v[88:91], v[68:71], %0\n\t" \
    "ds_read_b128 v[88:91], %3 offset:6912\n\t" \
    "v_exp_f32 v52, v52\n\t" \
    "v_exp_f32 v53, v53\n\t" \
    "v_exp_f32 v54, v54\n\t" \
    "v_exp_f32 v55, v55\n\t" \
    "v_mfma_f32_32x32x8_bf16 v[32:47], v[80:81], %9, v[32:47]\n\t" \
    "v_cvt_pk_bf16_f32 v64, v48, v49\n\t" \
    "v_cvt_pk_bf16_f32 v65, v50, v51\n\t" \
    "v_cvt_pk_bf16_f32 v66, v52, v53\n\t" \
    "v_cvt_pk_bf16_f32 v67, v54, v55\n\t" \
    "v_exp_f32 v56, v56\n\t" \
    "v_exp_f32 v57, v57\n\t" \
    "v_exp_f32 v58, v58\n\t" \
    "v_exp_f32 v59, v59\n\t" \
    "v_mfma_f32_32x32x16_bf16 %0, v[92:95], v[64:67], %0\n\t" \
    "ds_read_b128 v[92:95], %3 offset:9216\n\t" \
    "v_exp_f32 v60, v60\n\t" \
    "v_exp_f32 v61, v61\n\t" \
    "v_exp_f32 v62, v62\n\t" \
    "v_exp_f32 v63, v63\n\t" \
    "v_cvt_pk_bf16_f32 v68, v56, v57\n\t" \
    "v_cvt_pk_bf16_f32 v69, v58, v59\n\t" \
    "v_cvt_pk_bf16_f32 v70, v60, v61\n\t" \
    "v_cvt_pk_bf16_f32 v71, v62, v63\n\t" \
    "s_waitcnt lgkmcnt(1)\n\t" \
    "v_mfma_f32_32x32x16_bf16 v[48:63], v[76:79], %8, 0\n\t" \
    "v_exp_f32 v32, v32\n\t" \
    "ds_read_b128 v[72:75], %1 offset:12288\n\t" \
    "v_exp_f32 v33, v33\n\t" \
    "ds_read_b64 v[80:81], %2 offset:12288\n\t" \
    "v_exp_f32 v34, v34\n\t" \
    "v_exp_f32 v35, v35\n\t" \
    "v_mfma_f32_32x32x16_bf16 %0, v[96:99], v[68:71], %0\n\t" \
    "ds_read_b128 v[96:99], %3 offset:9984\n\t" \
    "v_exp_f32 v36, v36\n\t" \
    "v_exp_f32 v37, v37\n\t" \
    "v_exp_f32 v38, v38\n\t" \
    "v_exp_f32 v39, v39\n\t" \
    "v_mfma_f32_32x32x8_bf16 v[48:63], v[82:83], %9, v[48:63]\n\t" \
    "v_cvt_pk_bf16_f32 v64, v32, v33\n\t" \
    "v_cvt_pk_bf16_f32 v65, v34, v35\n\t" \
    "v_cvt_pk_bf16_f32 v66, v36, v37\n\t" \
    "v_cvt_pk_bf16_f32 v67, v38, v39\n\t" \
    "v_exp_f32 v40, v40\n\t" \
    "v_exp_f32 v41, v41\n\t" \
    "v_exp_f32 v42, v42\n\t" \
    "v_exp_f32 v43, v43\n\t" \
    "v_mfma_f32_32x32x16_bf16 %0, v[84:87], v[64:67], %0\n\t" \
    "ds_read_b128 v[84:87], %3 offset:12288\n\t" \
    "v_exp_f32 v44, v44\n\t" \
    "v_exp_f32 v45, v45\n\t" \
    "v_exp_f32 v46, v46\n\t" \
    "v_exp_f32 v47, v47\n\t" \
    "v_cvt_pk_bf16_f32 v68, v40, v41\n\t" \
    "v_cvt_pk_bf16_f32 v69, v42, v43\n\t" \
    "v_cvt_pk_bf16_f32 v70, v44, v45\n\t" \
    "v_cvt_pk_bf16_f32 v71, v46, v47\n\t" \
    "s_waitcnt lgkmcnt(1)\n\t" \
    "v_mfma_f32_32x32x16_bf16 v[32:47], v[72:75], %8, 0\n\t" \
    "v_exp_f32 v48, v48\n\t" \
    "ds_read_b128 v[76:79], %1 offset:15360\n\t" \
    "v_exp_f32 v49, v49\n\t" \
    "ds_read_b64 v[82:83], %2 offset:15360\n\t" \
    "v_exp_f32 v50, v50\n\t" \
    "v_exp_f32 v51, v51\n\t" \
    "v_mfma_f32_32x32x16_bf16 %0, v[88:91], v[68:71], %0\n\t" \
    "ds_read_b128 v[88:91], %3 offset:13056\n\t" \
    "v_exp_f32 v52, v52\n\t" \
    "v_exp_f32 v53, v53\n\t" \
    "v_exp_f32 v54, v54\n\t" \
    "v_exp_f32 v55, v55\n\t" \
    "v_mfma_f32_32x32x8_bf16 v[32:47], v[80:81], %9, v[32:47]\n\t" \
    "v_cvt_pk_bf16_f32 v64, v48, v49\n\t" \
    "v_cvt_pk_bf16_f32 v65, v50, v51\n\t" \
    "v_cvt_pk_bf16_f32 v66, v52, v53\n\t" \
    "v_cvt_pk_bf16_f32 v67, v54, v55\n\t" \
    "v_exp_f32 v56, v56\n\t" \
    "v_exp_f32 v57, v57\n\t" \
    "v_exp_f32 v58, v58\n\t" \
    "v_exp_f32 v59, v59\n\t" \
    "v_mfma_f32_32x32x16_bf16 %0, v[92:95], v[64:67], %0\n\t" \
    "ds_read_b128 v[92:95], %3 offset:15360\n\t" \
    "v_exp_f32 v60, v60\n\t" \
    "v_exp_f32 v61, v61\n\t" \
    "v_exp_f32 v62, v62\n\t" \
    "v_exp_f32 v63, v63\n\t" \
    "v_cvt_pk_bf16_f32 v68, v56, v57\n\t" \
    "v_cvt_pk_bf16_f32 v69, v58, v59\n\t" \
    "v_cvt_pk_bf16_f32 v70, v60, v61\n\t" \
    "v_cvt_pk_bf16_f32 v71, v62, v63\n\t" \
    "s_waitcnt lgkmcnt(1)\n\t" \
    "v_mfma_f32_32x32x16_bf16 v[48:63], v[76:79], %8, 0\n\t" \
    "v_exp_f32 v32, v32\n\t" \
    "ds_read_b128 v[72:75], %1 offset:18432\n\t" \
    "v_exp_f32 v33, v33\n\t" \
    "ds_read_b64 v[80:81], %2 offset:18432\n\t" \
    "v_exp_f32 v34, v34\n\t" \
    "v_exp_f32 v35, v35\n\t" \
    "v_mfma_f32_32x32x16_bf16 %0, v[96:99], v[68:71], %0\n\t" \
    "ds_read_b128 v[96:99], %3 offset:16128\n\t" \
    "v_exp_f32 v36, v36\n\t" \
    "v_exp_f32 v37, v37\n\t" \
    "v_exp_f32 v38, v38\n\t" \
    "v_exp_f32 v39, v39\n\t" \
    "v_mfma_f32_32x32x8_bf16 v[48:63], v[82:83], %9, v[48:63]\n\t" \
    "v_cvt_pk_bf16_f32 v64, v32, v33\n\t" \
    "v_cvt_pk_bf16_f32 v65, v34, v35\n\t" \
    "v_cvt_pk_bf16_f32 v66, v36, v37\n\t" \
    "v_cvt_pk_bf16_f32 v67, v38, v39\n\t" \
    "v_exp_f32 v40, v40\n\t" \
    "v_exp_f32 v41, v41\n\t" \
    "v_exp_f32 v42, v42\n\t" \
    "v_exp_f32 v43, v43\n\t" \
    "v_mfma_f32_32x32x16_bf16 %0, v[84:87], v[64:67], %0\n\t" \
    "ds_read_b128 v[84:87], %3 offset:18432\n\t" \
    "v_exp_f32 v44, v44\n\t" \
    "v_exp_f32 v45, v45\n\t" \
    "v_exp_f32 v46, v46\n\t" \
    "v_exp_f32 v47, v47\n\t" \
    "v_cvt_pk_bf16_f32 v68, v40, v41\n\t" \
    "v_cvt_pk_bf16_f32 v69, v42, v43\n\t" \
    "v_cvt_pk_bf16_f32 v70, v44, v45\n\t" \
    "v_cvt_pk_bf16_f32 v71, v46, v47\n\t" \
    "s_waitcnt lgkmcnt(1)\n\t" \
    "v_mfma_f32_32x32x16_bf16 v[32:47], v[72:75], %8, 0\n\t" \
    "v_exp_f32 v48, v48\n\t" \
    "ds_read_b128 v[76:79], %1 offset:21504\n\t" \
    "v_exp_f32 v49, v49\n\t" \
    "ds_read_b64 v[82:83], %2 offset:21504\n\t" \
    "v_exp_f32 v50, v50\n\t" \
    "v_exp_f32 v51, v51\n\t" \
    "v_mfma_f32_32x32x16_bf16 %0, v[88:91], v[68:71], %0\n\t" \
    "ds_read_b128 v[88:91], %3 offset:19200\n\t" \
    "v_exp_f32 v52, v52\n\t" \
    "v_exp_f32 v53, v53\n\t" \
    "v_exp_f32 v54, v54\n\t" \
    "v_exp_f32 v55, v55\n\t" \
    "v_mfma_f32_32x32x8_bf16 v[32:47], v[80:81], %9, v[32:47]\n\t" \
    "v_cvt_pk_bf16_f32 v64, v48, v49\n\t" \
    "v_cvt_pk_bf16_f32 v65, v50, v51\n\t" \
    "v_cvt_pk_bf16_f32 v66, v52, v53\n\t" \
    "v_cvt_pk_bf16_f32 v67, v54, v55\n\t" \
    "v_exp_f32 v56, v56\n\t" \
    "v_exp_f32 v57, v57\n\t" \
    "v_exp_f32 v58, v58\n\t" \
    "v_exp_f32 v59, v59\n\t" \
    "v_mfma_f32_32x32x16_bf16 %0, v[92:95], v[64:67], %0\n\t" \
    "ds_read_b128 v[92:95], %3 offset:21504\n\t" \
    "v_exp_f32 v60, v60\n\t" \
    "v_exp_f32 v61, v61\n\t" \
    "v_exp_f32 v62, v62\n\t" \
    "v_exp_f32 v63, v63\n\t" \
    "v_cvt_pk_bf16_f32 v68, v56, v57\n\t" \
    "v_cvt_pk_bf16_f32 v69, v58, v59\n\t" \
    "v_cvt_pk_bf16_f32 v70, v60, v61\n\t" \
    "v_cvt_pk_bf16_f32 v71, v62, v63\n\t" \
    "s_nop 1\n\t" \
    "v_mfma_f32_32x32x16_bf16 %0, v[96:99], v[68:71], %0\n\t" \
    "ds_read_b128 v[96:99], %3 offset:22272\n\t" \
    "s_add_u32 %4, %4, 0x6000\n\t" \
    "s_add_u32 %6, %6, 0x6000\n\t" \
    "s_cmp_eq_u32 %6, 0x12000\n\t" \
    "s_cselect_b32 %6, 0, %6\n\t" \
    "s_sub_u32 %7, %7, 1\n\t" \
    "s_cmp_lg_u32 %7, 0\n\t" \
    "s_cbranch_scc1 1b\n\t" \
    "s_waitcnt vmcnt(0) lgkmcnt(0)\n\t" \
    "s_nop 15"
#define ATT_CLOBBERS "v32", "v33", "v34", "v35", "v36", "v37", "v38", "v39", "v40", "v41", "v42", "v43", "v44", "v45", "v46", "v47", "v48", "v49", "v50", "v51", "v52", "v53", "v54", "v55", "v56", "v57", "v58", "v59", "v60", "v61", "v62", "v63", "v64", "v65", "v66", "v67", "v68", "v69", "v70", "v71", "v72", "v73", "v74", "v75", "v76", "v77", "v78", "v79", "v80", "v81", "v82", "v83", "v84", "v85", "v86", "v87", "v88", "v89", "v90", "v91", "v92", "v93", "v94", "v95", "v96", "v97", "v98", "v99"
DEV void attn_role(char* ws, int item, lds_ptr lds) {
    constexpr int SK = 1;
    const int tid = threadIdx.x, lane = tid & 63, w = __builtin_amdgcn_readfirstlane(tid >> 6);
    const int r = lane & 31, h = lane >> 5;
    const int head = (item & 7) * 4 + (item >> 6), qb = (item >> 3) & 7;
    const char* kvh = ws + WS_KV + (size_t)head * 64 * 3072;
    i32x4 skv = make_srd(kvh);
    skv[2] = 64 * 3072;
    const unsigned l0 = (unsigned)(uintptr_t)lds;
    const unsigned vo = lane * 16;
    STAMP(0);
    __builtin_amdgcn_s_setprio(3);
    if (w < 6) dma16(skv, vo, (unsigned)w * 1024u, l0 + 3 * AT_CHUNK + (unsigned)w * 1024u);
#pragma unroll
    for (int c = 0; c < 2; ++c)
#pragma unroll
        for (int j = 0; j < 3; ++j) dma16(skv, vo, (unsigned)(6144 + c * AT_CHUNK + (3 * w + j) * 1024), l0 + (unsigned)(c * AT_CHUNK + (3 * w + j) * 1024));
    const int tile = qb * 8 + w;
    const char* qp = ws + WS_QF + (size_t)(head * 64 + tile) * 1280;
    const bf16x8 q1 = *(const bf16x8*)(qp + lane * 16);
    s16x4 q2 = *(const s16x4*)(qp + 1024 + r * 8);
    if (h) q2 = (s16x4){0, 0, 0, 0};
    const int vrow = r < 21 ? r : 21;
    const unsigned kbase = l0 + lane * 16, k2base = l0 + 1024 + lane * 8, vbase = l0 + 1536 + h * 384 + vrow * 16;
    WAIT_VM_BAR(6);
    STAMP(1);
    {
        const bf16x8 k1 = lds_ld128((lds_cptr)lds + 3 * AT_CHUNK + lane * 16); const s16x4 k2 = lds_ld64((lds_cptr)lds + 3 * AT_CHUNK + 1024 + lane * 8);
        f32x16 s = mfma16(k1, q1, f32x16{});
        s = mfma8(k2, q2, s);
        float tm = s[0];
#pragma unroll
        for (int i = 1; i < 16; ++i) tm = fmaxf(tm, s[i]);
        tm = fmaxf(tm, __shfl_xor(tm, 32));
        const unsigned nb = pk2(-tm, 0.f) & 0xffffu;
        if (h) q2[0] = (short)nb;
    }
    const float gq = ((const float*)(ws + WS_G))[tile * 32 + r];
    asm volatile("" :: "v"(gq));
    f32x16 o = f32x16{};
    __builtin_amdgcn_s_setprio(A_PRIO);
    STAMP(2);
    {
        unsigned ka = kbase + 3 * AT_CHUNK, kb = k2base + 3 * AT_CHUNK, va = vbase + 3 * AT_CHUNK;
        unsigned soff = 6144 + 2 * AT_CHUNK + w * 3072, ldst, cur = 0, cnt = 8;
        const unsigned wbase = l0 + w * 3072;
        asm volatile(ATT_LOOP_ASM
                     : "+v"(o), "+v"(ka), "+v"(kb), "+v"(va), "+s"(soff), "=&s"(ldst), "+s"(cur), "+s"(cnt)
                     : "v"(q1), "v"(q2), "v"(vo), "s"(skv), "v"(kbase), "v"(k2base), "v"(vbase), "s"(wbase)
                     : "memory", "scc", ATT_CLOBBERS);
    }
    STAMP(3);
    float l = __shfl(o[8], r + 32);
    bool ok = (l > 9.094947e-13f) && (l < 1.8446744e19f);
#pragma unroll
    for (int i = 0; i < 12; ++i) ok = ok && (fabsf(o[i]) < INFINITY || (h && i >= 8));
    if (!__all(ok)) {
        if (h) q2[0] = 0;
        attn_slow(kvh, q1, q2, lane, o);
        l = __shfl(o[8], r + 32);
    }
    const float sc = gq * __builtin_amdgcn_rcpf(l);
    const __amdgpu_buffer_rsrc_t ro = make_rsrc(ws + WS_OF);
    const unsigned ob = (unsigned)tile * 40u * 1024u;
    u32x4 m = {pk2(o[0] * sc, o[1] * sc), pk2(o[2] * sc, o[3] * sc), pk2(o[4] * sc, o[5] * sc), pk2(o[6] * sc, o[7] * sc)};
    st16_sc1(ro, ob + (unsigned)(head * 1024 + lane * 16), m);
    if (h == 0) {
        u32x2 e = {pk2(o[8] * sc, o[9] * sc), pk2(o[10] * sc, o[11] * sc)};
        st8_sc1(ro, ob + (unsigned)((32 + (head >> 2)) * 1024 + (((head >> 1) & 1) * 32 + r) * 16 + (head & 1) * 8), e);
    }
    publish(ws, qb);
    STAMP(4);
}

DEV void bufld16(bf16x8& dst, const i32x4& srd, unsigned voff, unsigned soff) {
    asm volatile(PADS "buffer_load_dwordx4 %0, %1, %2, %3 offen" : "=v"(dst) : "v"(voff), "s"(srd), "s"(RFL(soff)) : "memory");
}
DEV void bufld16_o(bf16x8& dst, const i32x4& srd, unsigned voff, unsigned soff) {
    asm volatile(PADS "buffer_load_dwordx4 %0, %1, %2, %3 offen sc1" : "=v"(dst) : "v"(voff), "s"(srd), "s"(RFL(soff)) : "memory");
}
#define WAIT_VM_2(n, a, b) asm volatile("s_waitcnt vmcnt(" #n ")" : "+v"(a), "+v"(b) :: "memory")

template <int N> DEV void pin_vm(bf16x8& a) { asm volatile("s_waitcnt vmcnt(%1)" : "+v"(a) : "n"(N) : "memory"); }
template <int N> DEV void pin_vm(bf16x8& a, bf16x8& b, bf16x8& c) { asm volatile("s_waitcnt vmcnt(%3)" : "+v"(a), "+v"(b), "+v"(c) : "n"(N) : "memory"); }
template <int N> DEV void pin_vm(bf16x8& a, bf16x8& b, bf16x8& c, bf16x8& d) { asm volatile("s_waitcnt vmcnt(%4)" : "+v"(a), "+v"(b), "+v"(c), "+v"(d) : "n"(N) : "memory"); }
template <int N> DEV void pin_vm(bf16x8& a, bf16x8& b, bf16x8& c, bf16x8& d, bf16x8& e, bf16x8& f) { asm volatile("s_waitcnt vmcnt(%6)" : "+v"(a), "+v"(b), "+v"(c), "+v"(d), "+v"(e), "+v"(f) : "n"(N) : "memory"); }
constexpr int P2_NF = 24, P2_PD = 3, P2_AD = 6;
constexpr int p2_allowed(int s, bool up) {
    int idx[256] = {}, n = 0;
    const int nw = up ? 2 : 3;
    auto A = [&](int k) { idx[n] = k; ++n; };
    auto W = [&](int k) { for (int i = 0; i < nw; ++i) { idx[n] = k; ++n; } };
    for (int k = 0; k < P2_PD; ++k) W(k);
    for (int k = 0; k < P2_AD; ++k) A(k);
    for (int u = 0; u < s; ++u) { if (u + P2_PD < P2_NF + 2) W(u + P2_PD); if (u + P2_AD < P2_NF) A(u + P2_AD); }
    int last = -1;
    for (int i = 0; i < n; ++i) if (idx[i] <= s) last = i;
    return n - 1 - last;
}

DEV void g_role(char* ws, char* out, int item, lds_ptr lds) {
    constexpr int SK = 1;
    const int tid = threadIdx.x, lane = tid & 63, w = __builtin_amdgcn_readfirstlane(tid >> 6);
    const int r = lane & 31, h = lane >> 5;
    const int xcd = item & 7, j = item >> 3;
    const unsigned l0 = (unsigned)(uintptr_t)lds;
    STAMP(0);
    __builtin_amdgcn_s_setprio(G_PRIO);
    const int reg = j >> 2, sub = j & 3, kg = w & 3;
    const bool up = w >= 4;
    const int rgp = (xcd & 3) * 4 + (reg & 3), cgp = (xcd >> 2) * 2 + (reg >> 2);
    const int rp = sub >> 1, cp = sub & 1;
    const int t0 = rgp * 4 + 2 * rp + cp, t1 = rgp * 4 + 2 * rp + (cp ^ 1);
    const int c0 = cgp * 128 + cp * 64, c4 = 512 + cgp * 32;
    const int ct0 = cgp * 4 + cp * 2, ct4 = 16 + cgp;
    const int tme = up ? t1 : t0;
    const i32x4 swf = make_srd(ws + WS_WF), sh = make_srd(ws + WS_HF), so_ = make_srd(ws + WS_OF);
    const unsigned va = lane * 16;
    f32x16 acc[3];
#pragma unroll
    for (int i = 0; i < 3; ++i) acc[i] = f32x16{};
    const float gm = 1.f - ((const float*)(ws + WS_G))[tme * 32 + r];
    asm volatile("" :: "v"(gm));
    auto run = [&](auto upc) {
        constexpr bool UP = decltype(upc)::value;
        constexpr int NW = UP ? 2 : 3;
        bf16x8 wf[P2_PD][3];
        auto wbase = [&](int s) -> unsigned { return s < P2_NF ? (unsigned)(WF_WD + (kg * 24 + s) * 20) * 1024u : (unsigned)(WF_WO + (kg * 10 + s - P2_NF) * 20) * 1024u; };
        auto issue_w = [&](int s, int slot) {
            const unsigned fb = wbase(s);
            bufld16(wf[slot][0], swf, va, fb + (unsigned)ct0 * 1024u); bufld16(wf[slot][1], swf, va, fb + (unsigned)(ct0 + 1) * 1024u);
            if constexpr (!UP) bufld16(wf[slot][2], swf, va, fb + (unsigned)ct4 * 1024u);
        };
        auto mma = [&](const bf16x8& w0, const bf16x8& w1, const bf16x8& w2, const bf16x8& a) {
            acc[0] = mfma16(w0, a, acc[0]); acc[1] = mfma16(w1, a, acc[1]);
            if constexpr (!UP) acc[2] = mfma16(w2, a, acc[2]);
        };
        auto pin = [&](auto nc, bf16x8& a, int slot) {
            constexpr int N = decltype(nc)::value;
            if constexpr (UP) pin_vm<N>(a, wf[slot][0], wf[slot][1]); else pin_vm<N>(a, wf[slot][0], wf[slot][1], wf[slot][2]);
        };
        {
            bf16x8 af[P2_AD];
            auto issue_a = [&](int s) { bufld16(af[s % P2_AD], sh, va, (unsigned)(tme * 96 + kg * 24 + s) * 1024u); };
#pragma unroll
            for (int s = 0; s < P2_PD; ++s) issue_w(s, s);
#pragma unroll
            for (int s = 0; s < P2_AD; ++s) issue_a(s);
            STAMP(3);
            auto step = [&](auto sc) {
                constexpr int s = decltype(sc)::value;
                pin(std::integral_constant<int, p2_allowed(s, UP)>{}, af[s % P2_AD], s % P2_PD);
                mma(wf[s % P2_PD][0], wf[s % P2_PD][1], wf[s % P2_PD][2], af[s % P2_AD]);
                SBAR();
                if constexpr (s + P2_PD < P2_NF + 2) issue_w(s + P2_PD, (s + P2_PD) % P2_PD);
                if constexpr (s + P2_AD < P2_NF) issue_a(s + P2_AD);
                SBAR();
            };
            static_for<0, P2_NF>(step);
        }
#pragma unroll
        for (int i = 0; i < 16; ++i) { acc[0][i] *= gm; acc[1][i] *= gm; acc[2][i] *= gm; }
#pragma unroll
        for (int q = 0; q < 3; ++q) {
            const int s = 26 + 2 * q + (UP ? 1 : 0);
            const unsigned fb = wbase(s), lb = l0 + (unsigned)kg * 18432u + (unsigned)(s - 26) * 3072u;
            dma16(swf, va, fb + (unsigned)ct0 * 1024u, lb); dma16(swf, va, fb + (unsigned)(ct0 + 1) * 1024u, lb + 1024u); dma16(swf, va, fb + (unsigned)ct4 * 1024u, lb + 2048u);
        }
        if constexpr (UP) pin_vm<0>(wf[0][0], wf[0][1], wf[1][0], wf[1][1]); else pin_vm<0>(wf[0][0], wf[0][1], wf[0][2], wf[1][0], wf[1][1], wf[1][2]);
        STAMP(4);
        consume<false>(ws, rgp >> 1, 32);
        STAMP(5);
        bf16x8 ao[10];
#pragma unroll
        for (int i = 0; i < 10; ++i) bufld16_o(ao[i], so_, va, (unsigned)(tme * 40 + kg * 10 + i) * 1024u);
        const lds_cptr wl = (lds_cptr)lds + kg * 18432 + lane * 16;
        auto ldw = [&](int s) { wf[2][0] = lds_ld128(wl + (s - 26) * 3072); wf[2][1] = lds_ld128(wl + (s - 26) * 3072 + 1024); if constexpr (!UP) wf[2][2] = lds_ld128(wl + (s - 26) * 3072 + 2048); };
        pin(std::integral_constant<int, 9>{}, ao[0], 0);
        mma(wf[0][0], wf[0][1], wf[0][2], ao[0]); SBAR();
        issue_w(32, 0); SBAR();
        pin(std::integral_constant<int, 8 + NW>{}, ao[1], 1);
        mma(wf[1][0], wf[1][1], wf[1][2], ao[1]); SBAR();
        issue_w(33, 1); ldw(26); SBAR();
        auto stepo = [&](auto sc) {
            constexpr int s = decltype(sc)::value;
            pin_vm<33 - s + 2 * NW>(ao[s - 24]);
            mma(wf[2][0], wf[2][1], wf[2][2], ao[s - 24]);
            SBAR();
            if constexpr (s < 31) ldw(s + 1);
            SBAR();
        };
        static_for<26, 32>(stepo);
        pin(std::integral_constant<int, NW>{}, ao[8], 0);
        mma(wf[0][0], wf[0][1], wf[0][2], ao[8]); SBAR();
        pin(std::integral_constant<int, 0>{}, ao[9], 1);
        mma(wf[1][0], wf[1][1], wf[1][2], ao[9]);
    };
    if (up) run(std::true_type{}); else run(std::false_type{});
    STAMP(6);
    typedef __attribute__((ext_vector_type(4))) float f32x4;
    typedef __attribute__((address_space(3))) f32x4* lds_f4;
    __syncthreads();
    {
        lds_ptr mine = lds + kg * 20480 + r * 128;
#pragma unroll
        for (int i = 0; i < 3; ++i) {
            if (i == 2 && up) break;
            const int tl = up ? 2 + i : (i == 2 ? 4 : i);
#pragma unroll
            for (int g = 0; g < 4; ++g) {
                f32x4 v = {acc[i][4 * g], acc[i][4 * g + 1], acc[i][4 * g + 2], acc[i][4 * g + 3]};
                *(lds_f4)(mine + tl * 4096 + (((2 * g + h) ^ (r & 7)) * 16)) = v;
            }
        }
    }
    __syncthreads();
#pragma unroll
    for (int it = 0; it < 2; ++it) {
        const int u = tid + 512 * it;
        if (u < 640) {
            const int tl = u >> 7, tok = (u & 127) >> 2, ch = u & 3;
            f32x4 lo = {0, 0, 0, 0}, hi = {0, 0, 0, 0};
#pragma unroll
            for (int ww = 0; ww < 4; ++ww) {
                lds_ptr p = lds + ww * 20480 + tl * 4096 + tok * 128;
                lo += *(lds_f4)(p + (((2 * ch) ^ (tok & 7)) * 16));
                hi += *(lds_f4)(p + (((2 * ch + 1) ^ (tok & 7)) * 16));
            }
            const int tt = (tl == 2 || tl == 3) ? t1 : t0, cc = tl == 4 ? c4 : c0 + (tl & 1) * 32;
            u32x4 o = {pk2(lo[0], lo[1]), pk2(lo[2], lo[3]), pk2(hi[0], hi[1]), pk2(hi[2], hi[3])};
            *(u32x4*)(out + (size_t)(tt * 32 + tok) * 1280 + (cc + ch * 8) * 2) = o;
        }
    }
    STAMP(7);
}

__global__ __launch_bounds__(512, 4) void k2_kernel(char* __restrict__ ws, char* __restrict__ out) {
    extern __shared__ __attribute__((aligned(16))) char lds_raw[];
    lds_ptr lds = (lds_ptr)lds_raw;
    if (blockIdx.x < 256) attn_role(ws, blockIdx.x, lds);
    else g_role(ws, out, blockIdx.x - 256, lds);
}

extern "C" void kernel_launch(void* const* d_in, const int* in_sizes, int n_in, void* d_out, int out_size, void* d_ws, size_t ws_size, hipStream_t stream) {
    const char* ax = (const char*)d_in[0];
    const char* wqkv = (const char*)d_in[1];
    const char* wo = (const char*)d_in[2];
    const char* wg = (const char*)d_in[4];
    const char* wu = (const char*)d_in[5];
    const char* wd = (const char*)d_in[6];
    const char* gw = (const char*)d_in[7];
    char* ws = (char*)d_ws;
    (void)hipFuncSetAttribute((const void*)k1_kernel, hipFuncAttributeMaxDynamicSharedMemorySize, K1_LDS);
    (void)hipFuncSetAttribute((const void*)k2_kernel, hipFuncAttributeMaxDynamicSharedMemorySize, 81920);
    k1_kernel<<<256, 512, K1_LDS, stream>>>(ax, wqkv, gw, wg, wu, wd, wo, ws);
    k2_kernel<<<512, 512, 81920, stream>>>(ws, (char*)d_out);
}
```

```cpp
#include <hip/hip_runtime.h>
#include <hip/hip_bf16.h>
#include <stdint.h>
#include <type_traits>

typedef __attribute__((ext_vector_type(8))) __bf16 bf16x8;
typedef __attribute__((ext_vector_type(8))) short s16x8;
typedef __attribute__((ext_vector_type(4))) short s16x4;
typedef __attribute__((ext_vector_type(2))) __bf16 bf16x2;
typedef __attribute__((ext_vector_type(2))) float f32x2;
typedef __attribute__((ext_vector_type(16))) float f32x16;
typedef __attribute__((ext_vector_type(4))) unsigned u32x4;
typedef __attribute__((ext_vector_type(2))) unsigned u32x2;
typedef __attribute__((ext_vector_type(4))) int i32x4;
typedef __attribute__((address_space(3))) const char* lds_cptr;
typedef __attribute__((address_space(3))) char* lds_ptr;

#define DEV __device__ __forceinline__

constexpr int NH = 32;
constexpr float QSCALE = 0.22360679774997896f * 1.4426950408889634f;

constexpr size_t WS_QF = 0;
constexpr size_t WS_KV = WS_QF + (size_t)NH * 64 * 1280;
constexpr size_t WS_HF = WS_KV + (size_t)NH * 64 * 3072;
constexpr size_t WS_OF = WS_HF + (size_t)64 * 96 * 1024;
constexpr size_t WS_WF = WS_OF + (size_t)64 * 40 * 1024;
constexpr int WF_WD = 0, WF_WO = 1920, WF_N = 2720;
constexpr size_t WS_G  = WS_WF + (size_t)WF_N * 1024;
constexpr size_t WS_FLAG = WS_G + 2048 * 4;
constexpr int FLAG_LINES = 8;

DEV i32x4 make_srd(const void* p) {
    i32x4 s;
    s[0] = __builtin_amdgcn_readfirstlane((int)(uint32_t)(uintptr_t)p);
    s[1] = __builtin_amdgcn_readfirstlane((int)(uint32_t)((uintptr_t)p >> 32)) & 0xffff;
    s[2] = 0x7fffffff;
    s[3] = 0x00020000;
    return s;
}
DEV void dma16(const i32x4& srd, unsigned voff, unsigned soff, unsigned ldsaddr) {
    asm volatile("s_mov_b32 m0, %3\n\ts_nop 0\n\tbuffer_load_dwordx4 %0, %1, %2 offen lds"
                 :: "v"(voff), "s"(srd), "s"(soff), "s"(ldsaddr) : "memory");
}
#define WAIT_VM_BAR(n) asm volatile("s_waitcnt vmcnt(" #n ")\n\ts_barrier" ::: "memory")
#define SBAR() __builtin_amdgcn_sched_barrier(0)

DEV f32x16 mfma16(bf16x8 a, bf16x8 b, f32x16 c) { return __builtin_amdgcn_mfma_f32_32x32x16_bf16(a, b, c, 0, 0, 0); }
DEV f32x16 mfma8(s16x4 a, s16x4 b, f32x16 c) { return __builtin_amdgcn_mfma_f32_32x32x8bf16_1k(a, b, c, 0, 0, 0); }
DEV unsigned pk2(float lo, float hi) {
    f32x2 v = {lo, hi};
    bf16x2 r = __builtin_convertvector(v, bf16x2);
    return __builtin_bit_cast(unsigned, r);
}
DEV bf16x8 lds_ld128(lds_cptr p) { return *(const __attribute__((address_space(3))) bf16x8*)p; }
DEV s16x4 lds_ld64(lds_cptr p) { return *(const __attribute__((address_space(3))) s16x4*)p; }
DEV s16x4 lds_tr(lds_cptr p) { return __builtin_amdgcn_ds_read_tr16_b64_v4i16((__attribute__((address_space(3))) s16x4*)p); }
DEV bf16x8 cat8(s16x4 lo, s16x4 hi) {
    s16x8 r = __builtin_shufflevector(lo, hi, 0, 1, 2, 3, 4, 5, 6, 7);
    return __builtin_bit_cast(bf16x8, r);
}
DEV bf16x8 pack8(const f32x16& v, int base) {
    u32x4 r = {pk2(v[base], v[base + 1]), pk2(v[base + 2], v[base + 3]), pk2(v[base + 4], v[base + 5]), pk2(v[base + 6], v[base + 7])};
    return __builtin_bit_cast(bf16x8, r);
}

typedef __attribute__((address_space(1))) unsigned gu32;
#define RLX_AGENT __ATOMIC_RELAXED, __HIP_MEMORY_SCOPE_AGENT
DEV __amdgpu_buffer_rsrc_t make_rsrc(void* p) { return __builtin_amdgcn_make_buffer_rsrc(p, 0, 0x7fffffff, 0x00020000); }
DEV void st16_sc1(__amdgpu_buffer_rsrc_t rs, unsigned voff, u32x4 v) { __builtin_amdgcn_raw_buffer_store_b128(v, rs, voff, 0, 16); }
DEV void st8_sc1(__amdgpu_buffer_rsrc_t rs, unsigned voff, u32x2 v) { __builtin_amdgcn_raw_buffer_store_b64(v, rs, voff, 0, 16); }
DEV void publish(char* ws, int line) {
    asm volatile("s_waitcnt vmcnt(0)\n\ts_barrier" ::: "memory");
    if (threadIdx.x == 0) __hip_atomic_fetch_add((gu32*)(ws + WS_FLAG + line * 256), 1u, RLX_AGENT);
}
DEV void consume(char* ws, int line, unsigned target) {
    if (threadIdx.x < 64) {
        gu32* f = (gu32*)(ws + WS_FLAG + line * 256);
        for (unsigned spins = 0; spins < (1u << 20); ++spins) {
            if ((unsigned)__builtin_amdgcn_readfirstlane(__hip_atomic_load(f, RLX_AGENT)) >= target) break;
            __builtin_amdgcn_s_sleep(2);
        }
    }
    asm volatile("s_barrier" ::: "memory");
}

constexpr int K1_XS = 16384, K1_WS = 20480, K1_WB = 3 * K1_XS, K1_GW = K1_WB + 5 * K1_WS, K1_LDS = K1_GW + 1280 + 64;

DEV f32x2 gelu_mul2(f32x2 x, f32x2 u) {
    const float c0 = -2.0f * 0.7978845608028654f * 1.4426950408889634f, c1 = c0 * 0.044715f;
    const f32x2 t = x * __builtin_elementwise_fma(x * x, (f32x2){c1, c1}, (f32x2){c0, c0});
    f32x2 d = {__builtin_amdgcn_exp2f(t[0]), __builtin_amdgcn_exp2f(t[1])};
    d += (f32x2){1.0f, 1.0f};
    const f32x2 rc = {__builtin_amdgcn_rcpf(d[0]), __builtin_amdgcn_rcpf(d[1])};
    return (x * u) * rc;
}
template <int I, int N, class F> DEV void static_for(F&& f) { if constexpr (I < N) { f(std::integral_constant<int, I>{}); static_for<I + 1, N>(f); } }
template <int N> DEV void wait_vm() { asm volatile("s_waitcnt vmcnt(%0)" :: "n"(N) : "memory"); }
template <int N> DEV void wait_vm_bar() { asm volatile("s_waitcnt vmcnt(%0)\n\ts_barrier" :: "n"(N) : "memory"); }

constexpr int k1_allowed(int t) {
    int kind[160] = {}, idx[160] = {}, n = 0;
    auto P = [&](int k, int i, int c) { for (int q = 0; q < c; ++q) { kind[n] = k; idx[n] = i; ++n; } };
    P(0, 0, 4); P(1, 0, 5); P(1, 1, 5); P(0, 1, 4); P(1, 2, 5);
    if (t >= 0) P(1, 3, 5);
    for (int u = 0; u < t; ++u) { if ((u & 1) == 0 && u / 2 + 2 < 10) P(0, u / 2 + 2, 4); if (u + 4 < 20) P(1, u + 4, 5); if (u == 16) P(2, 0, 4); }
    int last = -1;
    for (int i = 0; i < n; ++i) {
        const bool need = (kind[i] == 0 && idx[i] <= ((t + 1) >> 1)) || (kind[i] == 1 && idx[i] <= t + 1);
        if (need) last = i;
    }
    return n - 1 - last;
}

constexpr int K1_POL = 16;
DEV void k1_st16(char* ws, char* p, u32x4 v) { __builtin_amdgcn_raw_buffer_store_b128(v, make_rsrc(ws), (unsigned)(p - ws), 0, K1_POL); }
template <int MODE>
DEV void k1_gemm(const char* x, const char* wqkv, const char* gw, const char* wg, const char* wu, const char* wd, const char* wo, char* ws, int mt, int j, lds_ptr lds) {
    const int tid = threadIdx.x, lane = tid & 63, w8 = __builtin_amdgcn_readfirstlane(tid >> 6), w = w8 & 3;
    const int wt = w >> 1, ww = w & 1, r = lane & 31, h = lane >> 5;
    const int m0 = mt * 128, n0 = (j < 15 ? j : 14) * 128, f0 = j * 96;
    const unsigned l0 = (unsigned)(uintptr_t)lds;
    const int ph = 2 * (((mt & 7) + 3 * (j & 3)) % 5);
    auto xrot = [&](int u) -> int { return u == 0 ? 0 : 1 + (u - 1 + ph < 9 ? u - 1 + ph : u - 1 + ph - 9); };
    if (w8 >= 4) {
        const i32x4 sx = make_srd(x), sw = make_srd(wqkv), sg = make_srd(wg), su = make_srd(wu);
        const unsigned xq = lane >> 4;
        const unsigned vx0 = (unsigned)(lane >> 3) * 1280u + (unsigned)(((lane & 7) ^ xq) * 16);
        const unsigned vx1 = (unsigned)(lane >> 3) * 1280u + (unsigned)(((lane & 7) ^ xq ^ 4) * 16);
        const unsigned vq = (unsigned)(lane >> 4) * 3840u + (unsigned)(((lane & 15) ^ ((lane >> 4) << 2)) * 16);
        unsigned vf[3];
        const unsigned sxb = (unsigned)(m0 + 32 * w) * 1280u, sqb = (unsigned)(8 * w) * 3840u + (unsigned)n0 * 2u, sfb = (unsigned)f0 * 2u;
        auto issue_x = [&](int u) {
            const unsigned lb = l0 + (unsigned)(u % 3) * K1_XS;
            const int ur = xrot(u);
#pragma unroll
            for (int i = 0; i < 4; ++i) dma16(sx, (i & 1) ? vx1 : vx0, sxb + (unsigned)i * 10240u + (unsigned)ur * 128u, lb + (unsigned)(4 * w + i) * 1024u);
        };
        auto issue_wq = [&](int t) {
            const unsigned lb = l0 + K1_WB + (unsigned)(t % 5) * K1_WS;
            const int tr = 2 * xrot(t >> 1) + (t & 1);
#pragma unroll
            for (int i = 0; i < 2; ++i) dma16(sw, vq, sqb + (unsigned)(tr * 32 + 4 * i) * 3840u, lb + (unsigned)(2 * w + i) * 1024u);
        };
        auto issue_wf = [&](int t) {
            const unsigned lb = l0 + K1_WB + (unsigned)(t % 5) * K1_WS;
            const int tr = 2 * xrot(t >> 1) + (t & 1);
            const unsigned so = sfb + (unsigned)tr * (32u * 3072u);
            if (w < 2) {
#pragma unroll
                for (int i = 0; i < 3; ++i) dma16(sg, vf[i], so, lb + 8192u + (unsigned)(3 * w + i) * 1024u);
            } else {
#pragma unroll
                for (int i = 0; i < 3; ++i) dma16(su, vf[i], so, lb + 8192u + (unsigned)(3 * w + i) * 1024u);
            }
        };
        auto issue_w = [&](int t) { issue_wq(t); issue_wf(t); };
        issue_x(0); issue_wq(0);
        {
            int lane2 = lane;
            asm volatile("" : "+v"(lane2) :: "memory");
#pragma unroll
            for (int i = 0; i < 3; ++i) { const int p = 3 * w + i, L = (p % 6) * 64 + lane2, row = L / 12, c16 = L - row * 12; vf[i] = (unsigned)row * 3072u + (unsigned)((p < 6 ? c16 : (c16 ^ 2)) * 16); }
        }
        issue_wf(0);
        const int gwv = (mt * 16 + j) * 4 + w;
        const int u0 = gwv, u1 = gwv + 1024 < 1360 ? gwv + 1024 : gwv;
        const unsigned wl = l0 + K1_WB + (unsigned)w * 4096u;
        const char* rsrc[4];
        auto prep = [&](int i) {
            int u = (i >> 1) ? u1 : u0;
            asm volatile("" : "+s"(u) :: "memory");
            const int row = 8 * (i & 1) + (lane >> 3), c16 = lane & 7;
            if (u < 960) { const int ks = u / 10, cq = u - ks * 10; rsrc[i] = wd + (size_t)(16 * ks + row) * 1280 + cq * 128 + c16 * 16; }
            else { const int q = u - 960, ks = q / 10, cq = q - ks * 10;
                   const int k = ks < 32 ? 20 * ks + row : 20 * (4 * (ks - 32) + 2 * ((row >> 2) & 1) + (row >> 3)) + 16 + (row & 3);
                   rsrc[i] = wo + (size_t)k * 1280 + cq * 128 + c16 * 16; }
            asm volatile("" : "+v"(rsrc[i]) :: "memory");
        };
        auto rl_issue = [&]() {
#pragma unroll
            for (int i = 0; i < 4; ++i) { const char* p = rsrc[i]; const unsigned d = wl + (unsigned)i * 1024u; asm volatile("s_mov_b32 m0, %1\n\ts_nop 0\n\tglobal_load_lds_dwordx4 %0, off" :: "v"(p), "s"(d) : "memory"); }
        };
        issue_w(1); issue_x(1); issue_w(2);
        wait_vm_bar<k1_allowed(-1)>();
        issue_w(3);
        auto top = [&](auto tc) {
            constexpr int t = decltype(tc)::value;
            wait_vm_bar<k1_allowed(t)>();
            if constexpr ((t & 1) == 0 && t / 2 + 2 < 10) issue_x(t / 2 + 2);
            if constexpr (t + 4 < 20) issue_w(t + 4);
            if constexpr (t >= 2 && t < 6) prep(t - 2);
            if constexpr (t == 16) rl_issue();
        };
        static_for<0, 19>(top);
        wait_vm<0>();
        {
            const int g = lane >> 4, nh = g & 1, q = (lane & 15) >> 2, p = lane & 3;
            lds_cptr base = (lds_cptr)lds + K1_WB + w * 4096;
            bf16x8 v[4];
#pragma unroll
            for (int i = 0; i < 4; ++i) { lds_cptr pp = base + (i >> 1) * 2048 + (4 * h + q) * 128 + (i & 1) * 64 + nh * 32 + p * 8; v[i] = cat8(lds_tr(pp), lds_tr(pp + 1024)); }
#pragma unroll
            for (int i = 0; i < 4; ++i) k1_st16(ws, ws + WS_WF + (size_t)(2 * ((i >> 1) ? u1 : u0) + (i & 1)) * 1024 + lane * 16, __builtin_bit_cast(u32x4, v[i]));
        }
        return;
    }
    unsigned csink;
    {
        unsigned lo1, hi1, lo2, hi2;
        asm volatile("s_getpc_b64 s[40:41]\n\ts_add_u32 s40, s40, gm_k1@rel32@lo+4\n\ts_addc_u32 s41, s41, gm_k1@rel32@hi+12\n\ts_mov_b32 %0, s40\n\ts_mov_b32 %1, s41\n\t"
                     "s_getpc_b64 s[40:41]\n\ts_add_u32 s40, s40, gm_k2@rel32@lo+4\n\ts_addc_u32 s41, s41, gm_k2@rel32@hi+12\n\ts_mov_b32 %2, s40\n\ts_mov_b32 %3, s41"
                     : "=&s"(lo1), "=&s"(hi1), "=&s"(lo2), "=&s"(hi2) :: "s40", "s41", "scc");
        constexpr int L1 = 50000 / 128, L2 = 12000 / 128, PER = (L1 + L2 + 127) / 128;
        const int n = min(((int)(blockIdx.x >> 3) * 4 + w) * PER + min(lane, PER - 1), L1 + L2 - 1);
        const char* p = n < L1 ? (const char*)(((uintptr_t)hi1 << 32) | lo1) + (size_t)n * 128 : (const char*)(((uintptr_t)hi2 << 32) | lo2) + (size_t)(n - L1) * 128;
        asm volatile("global_load_dword %0, %1, off" : "=v"(csink) : "v"(p) : "memory");
    }
    if constexpr (MODE == 2) {
        if (tid < 80) *(__attribute__((address_space(3))) u32x4*)(lds + K1_GW + tid * 16) = *(const u32x4*)(gw + tid * 16);
        else if (tid == 80) *(__attribute__((address_space(3))) u32x4*)(lds + K1_GW + 1280) = (u32x4){0u, 0u, 0u, 0u};
    }
    lds_cptr xa[4], qa[2], fa[3];
#pragma unroll
    for (int s = 0; s < 4; ++s) xa[s] = (lds_cptr)lds + (wt * 64 + r) * 128 + (((2 * s + h) ^ ((r >> 1) & 7)) * 16);
    {
        const int g = lane >> 4, nh = g & 1, q = (lane & 15) >> 2, p = lane & 3;
#pragma unroll
        for (int ct = 0; ct < 2; ++ct) {
            const int slot = ww * 8 + ct * 4 + 2 * nh + (p >> 1);
            qa[ct] = (lds_cptr)lds + K1_WB + (8 * h + q) * 256 + ((slot ^ (q << 2)) * 16) + 8 * (p & 1);
        }
#pragma unroll
        for (int a = 0; a < 3; ++a) {
            const int aa = ww * 3 + a, c16 = 2 * (nh ? (aa ^ 1) : aa) + (p >> 1);
            fa[a] = (lds_cptr)lds + K1_WB + 8192 + nh * 6144 + (8 * h + q) * 192 + c16 * 16 + 8 * (p & 1);
        }
    }
    const lds_cptr ga = (lds_cptr)lds + K1_GW + (r == 0 ? h * 16 : 1280);
    const int gstep = r == 0 ? 32 : 0;
    f32x16 aq[2][2], af[3][2];
#pragma unroll
    for (int i = 0; i < 2; ++i) { aq[i][0] = f32x16{}; aq[i][1] = f32x16{}; }
#pragma unroll
    for (int a = 0; a < 3; ++a) { af[a][0] = f32x16{}; af[a][1] = f32x16{}; }
    bf16x8 xf[2][2], qf[2][2], ff[2][3];
    auto rd_x = [&](int b, int t, int s, int tt) { xf[b][tt] = lds_ld128(xa[2 * (t & 1) + s] + ((t >> 1) % 3) * K1_XS + tt * 4096); };
    auto rd_q = [&](int b, int t, int s, int ct) {
        if constexpr (MODE == 2) { if (ct == 0) qf[b][0] = lds_ld128(ga + (2 * (2 * xrot(t >> 1) + (t & 1)) + s) * gstep); }
        else qf[b][ct] = cat8(lds_tr(qa[ct] + (t % 5) * K1_WS + s * 4096), lds_tr(qa[ct] + (t % 5) * K1_WS + s * 4096 + 1024));
    };
    auto rd_f = [&](int b, int t, int s, int a) { ff[b][a] = cat8(lds_tr(fa[a] + (t % 5) * K1_WS + s * 3072), lds_tr(fa[a] + (t % 5) * K1_WS + s * 3072 + 768)); };
    unsigned eo[8];
    if constexpr (MODE == 0) {
        const bool isq = j < 5;
        const int csec = (isq ? n0 : n0 - 640) + ww * 64, tstride = isq ? 1280 : 3072;
#pragma unroll
        for (int e = 0; e < 8; ++e) {
            const int qd = (csec + (e >> 2) * 32 + 8 * (e & 3) + 4 * h) >> 2, head = qd / 5, dq = qd - head * 5;
            const int inoff = dq < 4 ? (dq >> 1) * 512 + r * 16 + (dq & 1) * 8 : 1024 + r * 8;
            eo[e] = (unsigned)(isq ? WS_QF : WS_KV) + (unsigned)((head * 64 + mt * 4 + wt * 2) * tstride + inoff) + (dq == 4 && !isq ? 0x80000000u : 0u);
            asm volatile("" : "+v"(eo[e]));
        }
    } else if constexpr (MODE == 1) {
#pragma unroll
        for (int ct = 0; ct < 2; ++ct) {
            const int col = n0 - 1280 + ww * 64 + ct * 32 + r, head = col / 20, d = col - head * 20;
            eo[ct] = (unsigned)WS_KV + (unsigned)((head * 64 + mt * 4 + wt * 2) * 3072 + 1536 + h * 384 + d * 16) + (d == 0 ? 0x80000000u : d == 1 ? 0x40000000u : 0u);
            asm volatile("" : "+v"(eo[ct]));
        }
    }
    asm volatile("s_waitcnt lgkmcnt(0)\n\ts_barrier" ::: "memory");
    rd_x(0, 0, 0, 0); rd_x(0, 0, 0, 1); rd_q(0, 0, 0, 0); rd_q(0, 0, 0, 1); rd_f(0, 0, 0, 0); rd_f(0, 0, 0, 1); rd_f(0, 0, 0, 2);
    auto stage = [&](auto tc) {
        constexpr int t = decltype(tc)::value;
        if constexpr (t < 19) asm volatile("s_barrier" ::: "memory");
#pragma unroll
        for (int s = 0; s < 2; ++s) {
            const int c = s, nb = s ^ 1;
            const bool more = s == 0 || t < 19;
            const int tn = s == 0 ? t : t + 1, sn = s ^ 1;
            auto mq = [&](int ct, int tt) {
                if constexpr (MODE == 0) aq[ct][tt] = mfma16(qf[c][ct], xf[c][tt], aq[ct][tt]);
                else if constexpr (MODE == 1) aq[tt][ct] = mfma16(xf[c][tt], qf[c][ct], aq[tt][ct]);
                else { if (ct == 0) aq[0][tt] = mfma16(qf[c][0], xf[c][tt], aq[0][tt]); }
            };
            auto mf = [&](int a, int tt) { af[a][tt] = mfma16(ff[c][a], xf[c][tt], af[a][tt]); };
            mq(0, 0); SBAR(); if (more) { rd_x(nb, tn, sn, 0); rd_x(nb, tn, sn, 1); } SBAR();
            mq(0, 1); SBAR(); if (more) rd_q(nb, tn, sn, 0); SBAR();
            mq(1, 0); SBAR(); if (more) rd_q(nb, tn, sn, 1); SBAR();
            mq(1, 1); SBAR(); if (more) rd_f(nb, tn, sn, 0); SBAR();
            mf(0, 0); SBAR(); if (more) rd_f(nb, tn, sn, 1); SBAR();
            mf(0, 1); SBAR(); if (more) rd_f(nb, tn, sn, 2); SBAR();
            mf(1, 0); SBAR(); mf(1, 1); SBAR(); mf(2, 0); SBAR(); mf(2, 1); SBAR();
        }
    };
    static_for<0, 20>(stage);
    asm volatile("s_waitcnt vmcnt(0)" :: "v"(csink) : "memory");
    const __amdgpu_buffer_rsrc_t rws = make_rsrc(ws);
    if constexpr (MODE == 0) {
        const bool isq = j < 5;
        const unsigned tstride = isq ? 1280 : 3072;
        const float sc = isq ? QSCALE : 1.0f;
#pragma unroll
        for (int e = 0; e < 8; ++e) {
            const int ct = e >> 2, g = e & 3;
            const unsigned off = eo[e] & 0x7fffffffu;
#pragma unroll
            for (int tt = 0; tt < 2; ++tt) {
                const f32x16& a = aq[ct][tt];
                u32x2 v = {pk2(a[4 * g] * sc, a[4 * g + 1] * sc), pk2(a[4 * g + 2] * sc, a[4 * g + 3] * sc)};
                __builtin_amdgcn_raw_buffer_store_b64(v, rws, off + tt * tstride, 0, K1_POL);
                if (eo[e] >> 31) __builtin_amdgcn_raw_buffer_store_b64((u32x2){0x00003f80u, 0u}, rws, off + tt * tstride + 256u, 0, K1_POL);
            }
        }
    } else if constexpr (MODE == 1) {
#pragma unroll
        for (int ct = 0; ct < 2; ++ct) {
            const unsigned off = eo[ct] & 0x3fffffffu;
#pragma unroll
            for (int tt = 0; tt < 2; ++tt)
#pragma unroll
                for (int s = 0; s < 2; ++s) {
                    const unsigned o2 = off + tt * 3072u + s * 768u;
                    __builtin_amdgcn_raw_buffer_store_b128(__builtin_bit_cast(u32x4, pack8(aq[tt][ct], 8 * s)), rws, o2, 0, K1_POL);
                    if (eo[ct] >> 30) { const unsigned c = (eo[ct] >> 31) ? 0x3f803f80u : 0u; __builtin_amdgcn_raw_buffer_store_b128((u32x4){c, c, c, c}, rws, o2 + 320u, 0, K1_POL); }
                }
        }
    } else {
        if (ww == 0 && h == 0) {
#pragma unroll
            for (int tt = 0; tt < 2; ++tt) ((float*)(ws + WS_G))[m0 + wt * 64 + tt * 32 + r] = __builtin_amdgcn_rcpf(1.0f + __builtin_amdgcn_exp2f(-1.4426950408889634f * aq[0][tt][0]));
        }
        if (mt == 0 && tid < FLAG_LINES) *(unsigned*)(ws + WS_FLAG + tid * 256) = 0u;
    }
#pragma unroll
    for (int a = 0; a < 3; ++a)
#pragma unroll
        for (int tt = 0; tt < 2; ++tt) {
            f32x16 hv;
#pragma unroll
            for (int i = 0; i < 8; i += 2) { const f32x2 v = gelu_mul2((f32x2){af[a][tt][i], af[a][tt][i + 1]}, (f32x2){af[a][tt][8 + i], af[a][tt][9 + i]}); hv[i] = v[0]; hv[i + 1] = v[1]; }
            const int tile = mt * 4 + wt * 2 + tt, fs = j * 6 + ww * 3 + a;
            k1_st16(ws, ws + WS_HF + (size_t)(tile * 96 + fs) * 1024 + lane * 16, __builtin_bit_cast(u32x4, pack8(hv, 0)));
        }
}

extern "C" __global__ __launch_bounds__(512, 2) void gm_k1(const char* __restrict__ x, const char* __restrict__ wqkv, const char* __restrict__ gw, const char* __restrict__ wg, const char* __restrict__ wu,
                                                   const char* __restrict__ wd, const char* __restrict__ wo, char* __restrict__ ws) {
    extern __shared__ __attribute__((aligned(16))) char lds_raw[];
    lds_ptr lds = (lds_ptr)lds_raw;
    const int b = blockIdx.x, xcd = b & 7, jj = b >> 3;
    const int mt = (xcd & 1) * 8 + (jj & 7), j = (xcd >> 1) * 4 + (jj >> 3);
    if (j < 10) k1_gemm<0>(x, wqkv, gw, wg, wu, wd, wo, ws, mt, j, lds);
    else if (j < 15) k1_gemm<1>(x, wqkv, gw, wg, wu, wd, wo, ws, mt, j, lds);
    else k1_gemm<2>(x, wqkv, gw, wg, wu, wd, wo, ws, mt, j, lds);
}

constexpr int AT_CHUNK = 24576;

DEV void attn_slow(const char* kvh, bf16x8 q1, s16x4 q2, int lane, f32x16& o) {
    const int r = lane & 31, h = lane >> 5;
    const int vrow = r < 21 ? r : 21;
    float m = -INFINITY;
    o = f32x16{};
    for (int t = 0; t < 64; ++t) {
        const char* p = kvh + t * 3072;
        const bf16x8 k1 = *(const bf16x8*)(p + lane * 16);
        const s16x4 k2 = *(const s16x4*)(p + 1024 + lane * 8);
        const bf16x8 v0 = *(const bf16x8*)(p + 1536 + h * 384 + vrow * 16);
        const bf16x8 v1 = *(const bf16x8*)(p + 2304 + h * 384 + vrow * 16);
        f32x16 s = mfma16(k1, q1, f32x16{});
        s = mfma8(k2, q2, s);
        float tm = s[0];
#pragma unroll
        for (int i = 1; i < 16; ++i) tm = fmaxf(tm, s[i]);
        tm = fmaxf(tm, __shfl_xor(tm, 32));
        const float mn = fmaxf(m, tm);
        const float alpha = __builtin_amdgcn_exp2f(m - mn);
        m = mn;
#pragma unroll
        for (int i = 0; i < 16; ++i) { s[i] = __builtin_amdgcn_exp2f(s[i] - mn); o[i] *= alpha; }
        o = mfma16(v0, pack8(s, 0), o);
        o = mfma16(v1, pack8(s, 8), o);
    }
}

#define ATT_LOOP_ASM \
    "ds_read_b128 v[72:75], %1\n\t" \
    "ds_read_b64 v[80:81], %2\n\t" \
    "ds_read_b128 v[76:79], %1 offset:3072\n\t" \
    "ds_read_b64 v[82:83], %2 offset:3072\n\t" \
    "ds_read_b128 v[84:87], %3\n\t" \
    "ds_read_b128 v[88:91], %3 offset:768\n\t" \
    "ds_read_b128 v[92:95], %3 offset:3072\n\t" \
    "ds_read_b128 v[96:99], %3 offset:3840\n\t" \
    "v_mov_b32 v100, %9\n\t" \
    "v_mov_b32 v101, %10\n\t" \
    "s_waitcnt lgkmcnt(6)\n\t" \
    "v_mfma_f32_32x32x16_bf16 v[32:47], v[72:75], %8, 0\n\t" \
    "v_mfma_f32_32x32x8_bf16 v[32:47], v[80:81], v[100:101], v[32:47]\n\t" \
    "s_mov_b32 vcc_lo, 0\n\t" \
    "s_mov_b32 vcc_hi, -1\n\t" \
    "s_nop 15\n\t" \
    "v_max3_f32 v64, v32, v33, v34\n\t" \
    "v_max3_f32 v65, v35, v36, v37\n\t" \
    "v_max3_f32 v66, v38, v39, v40\n\t" \
    "v_max3_f32 v67, v41, v42, v43\n\t" \
    "v_max3_f32 v64, v64, v44, v45\n\t" \
    "v_max3_f32 v65, v65, v46, v47\n\t" \
    "v_max3_f32 v64, v64, v65, v66\n\t" \
    "v_max_f32 v64, v64, v67\n\t" \
    "v_mov_b32 v65, v64\n\t" \
    "s_nop 1\n\t" \
    "v_permlane32_swap_b32 v64, v65\n\t" \
    "s_nop 1\n\t" \
    "v_max_f32 v64, v64, v65\n\t" \
    "v_cvt_pk_bf16_f32 v65, -v64, 0\n\t" \
    "v_lshlrev_b32 v66, 16, v65\n\t" \
    "v_and_b32 v65, 0xffff, v65\n\t" \
    "v_cndmask_b32 v100, v100, v65, vcc\n\t" \
    "v_add_f32 v32, v32, v66\n\t" \
    "v_add_f32 v33, v33, v66\n\t" \
    "v_add_f32 v34, v34, v66\n\t" \
    "v_add_f32 v35, v35, v66\n\t" \
    "v_add_f32 v36, v36, v66\n\t" \
    "v_add_f32 v37, v37, v66\n\t" \
    "v_add_f32 v38, v38, v66\n\t" \
    "v_add_f32 v39, v39, v66\n\t" \
    "v_add_f32 v40, v40, v66\n\t" \
    "v_add_f32 v41, v41, v66\n\t" \
    "v_add_f32 v42, v42, v66\n\t" \
    "v_add_f32 v43, v43, v66\n\t" \
    "v_add_f32 v44, v44, v66\n\t" \
    "v_add_f32 v45, v45, v66\n\t" \
    "v_add_f32 v46, v46, v66\n\t" \
    "v_add_f32 v47, v47, v66\n\t" \
    "1:\n\t" \
    "s_waitcnt vmcnt(3) lgkmcnt(0)\n\t" \
    "s_barrier\n\t" \
    "v_add_u32 %1, %6, %13\n\t" \
    "v_add_u32 %2, %6, %14\n\t" \
    "v_add_u32 %3, %6, %15\n\t" \
    "s_sub_u32 %5, %6, 0x6000\n\t" \
    "s_cmp_eq_u32 %6, 0\n\t" \
    "s_cselect_b32 %5, 0xc000, %5\n\t" \
    "s_add_u32 %5, %5, %16\n\t" \
    "v_mfma_f32_32x32x16_bf16 v[48:63], v[76:79], %8, 0\n\t" \
    "v_exp_f32 v32, v32\n\t" \
    "ds_read_b128 v[72:75], %1 offset:0\n\t" \
    "v_exp_f32 v33, v33\n\t" \
    "ds_read_b64 v[80:81], %2 offset:0\n\t" \
    "v_exp_f32 v34, v34\n\t" \
    "v_exp_f32 v35, v35\n\t" \
    "v_exp_f32 v36, v36\n\t" \
    "s_mov_b32 m0, %5\n\t" \
    "v_exp_f32 v37, v37\n\t" \
    "buffer_load_dwordx4 %11, %12, %4 offen offset:0 lds\n\t" \
    "v_exp_f32 v38, v38\n\t" \
    "v_exp_f32 v39, v39\n\t" \
    "v_mfma_f32_32x32x8_bf16 v[48:63], v[82:83], v[100:101], v[48:63]\n\t" \
    "v_cvt_pk_bf16_f32 v64, v32, v33\n\t" \
    "v_cvt_pk_bf16_f32 v65, v34, v35\n\t" \
    "v_cvt_pk_bf16_f32 v66, v36, v37\n\t" \
    "v_cvt_pk_bf16_f32 v67, v38, v39\n\t" \
    "v_exp_f32 v40, v40\n\t" \
    "v_exp_f32 v41, v41\n\t" \
    "v_exp_f32 v42, v42\n\t" \
    "v_exp_f32 v43, v43\n\t" \
    "v_mfma_f32_32x32x16_bf16 %0, v[84:87], v[64:67], %0\n\t" \
    "ds_read_b128 v[84:87], %3 offset:0\n\t" \
    "v_exp_f32 v44, v44\n\t" \
    "v_exp_f32 v45, v45\n\t" \
    "v_exp_f32 v46, v46\n\t" \
    "v_exp_f32 v47, v47\n\t" \
    "v_cvt_pk_bf16_f32 v68, v40, v41\n\t" \
    "v_cvt_pk_bf16_f32 v69, v42, v43\n\t" \
    "v_cvt_pk_bf16_f32 v70, v44, v45\n\t" \
    "v_cvt_pk_bf16_f32 v71, v46, v47\n\t" \
    "s_waitcnt lgkmcnt(1)\n\t" \
    "v_mfma_f32_32x32x16_bf16 v[32:47], v[72:75], %8, 0\n\t" \
    "v_exp_f32 v48, v48\n\t" \
    "ds_read_b128 v[76:79], %1 offset:3072\n\t" \
    "v_exp_f32 v49, v49\n\t" \
    "ds_read_b64 v[82:83], %2 offset:3072\n\t" \
    "v_exp_f32 v50, v50\n\t" \
    "v_exp_f32 v51, v51\n\t" \
    "v_mfma_f32_32x32x16_bf16 %0, v[88:91], v[68:71], %0\n\t" \
    "ds_read_b128 v[88:91], %3 offset:768\n\t" \
    "v_exp_f32 v52, v52\n\t" \
    "v_exp_f32 v53, v53\n\t" \
    "buffer_load_dwordx4 %11, %12, %4 offen offset:1024 lds\n\t" \
    "v_exp_f32 v54, v54\n\t" \
    "v_exp_f32 v55, v55\n\t" \
    "v_mfma_f32_32x32x8_bf16 v[32:47], v[80:81], v[100:101], v[32:47]\n\t" \
    "v_cvt_pk_bf16_f32 v64, v48, v49\n\t" \
    "v_cvt_pk_bf16_f32 v65, v50, v51\n\t" \
    "v_cvt_pk_bf16_f32 v66, v52, v53\n\t" \
    "v_cvt_pk_bf16_f32 v67, v54, v55\n\t" \
    "v_exp_f32 v56, v56\n\t" \
    "v_exp_f32 v57, v57\n\t" \
    "v_exp_f32 v58, v58\n\t" \
    "v_exp_f32 v59, v59\n\t" \
    "v_mfma_f32_32x32x16_bf16 %0, v[92:95], v[64:67], %0\n\t" \
    "ds_read_b128 v[92:95], %3 offset:3072\n\t" \
    "v_exp_f32 v60, v60\n\t" \
    "v_exp_f32 v61, v61\n\t" \
    "v_exp_f32 v62, v62\n\t" \
    "v_exp_f32 v63, v63\n\t" \
    "v_cvt_pk_bf16_f32 v68, v56, v57\n\t" \
    "v_cvt_pk_bf16_f32 v69, v58, v59\n\t" \
    "v_cvt_pk_bf16_f32 v70, v60, v61\n\t" \
    "v_cvt_pk_bf16_f32 v71, v62, v63\n\t" \
    "s_waitcnt lgkmcnt(1)\n\t" \
    "v_mfma_f32_32x32x16_bf16 v[48:63], v[76:79], %8, 0\n\t" \
    "v_exp_f32 v32, v32\n\t" \
    "ds_read_b128 v[72:75], %1 offset:6144\n\t" \
    "v_exp_f32 v33, v33\n\t" \
    "ds_read_b64 v[80:81], %2 offset:6144\n\t" \
    "v_exp_f32 v34, v34\n\t" \
    "v_exp_f32 v35, v35\n\t" \
    "v_mfma_f32_32x32x16_bf16 %0, v[96:99], v[68:71], %0\n\t" \
    "ds_read_b128 v[96:99], %3 offset:3840\n\t" \
    "v_exp_f32 v36, v36\n\t" \
    "v_exp_f32 v37, v37\n\t" \
    "buffer_load_dwordx4 %11, %12, %4 offen offset:2048 lds\n\t" \
    "v_exp_f32 v38, v38\n\t" \
    "v_exp_f32 v39, v39\n\t" \
    "v_mfma_f32_32x32x8_bf16 v[48:63], v[82:83], v[100:101], v[48:63]\n\t" \
    "v_cvt_pk_bf16_f32 v64, v32, v33\n\t" \
    "v_cvt_pk_bf16_f32 v65, v34, v35\n\t" \
    "v_cvt_pk_bf16_f32 v66, v36, v37\n\t" \
    "v_cvt_pk_bf16_f32 v67, v38, v39\n\t" \
    "v_exp_f32 v40, v40\n\t" \
    "v_exp_f32 v41, v41\n\t" \
    "v_exp_f32 v42, v42\n\t" \
    "v_exp_f32 v43, v43\n\t" \
    "v_mfma_f32_32x32x16_bf16 %0, v[84:87], v[64:67], %0\n\t" \
    "ds_read_b128 v[84:87], %3 offset:6144\n\t" \
    "v_exp_f32 v44, v44\n\t" \
    "v_exp_f32 v45, v45\n\t" \
    "v_exp_f32 v46, v46\n\t" \
    "v_exp_f32 v47, v47\n\t" \
    "v_cvt_pk_bf16_f32 v68, v40, v41\n\t" \
    "v_cvt_pk_bf16_f32 v69, v42, v43\n\t" \
    "v_cvt_pk_bf16_f32 v70, v44, v45\n\t" \
    "v_cvt_pk_bf16_f32 v71, v46, v47\n\t" \
    "s_waitcnt lgkmcnt(1)\n\t" \
    "v_mfma_f32_32x32x16_bf16 v[32:47], v[72:75], %8, 0\n\t" \
    "v_exp_f32 v48, v48\n\t" \
    "ds_read_b128 v[76:79], %1 offset:9216\n\t" \
    "v_exp_f32 v49, v49\n\t" \
    "ds_read_b64 v[82:83], %2 offset:9216\n\t" \
    "v_exp_f32 v50, v50\n\t" \
    "v_exp_f32 v51, v51\n\t" \
    "v_mfma_f32_32x32x16_bf16 %0, v[88:91], v[68:71], %0\n\t" \
    "ds_read_b128 v[88:91], %3 offset:6912\n\t" \
    "v_exp_f32 v52, v52\n\t" \
    "v_exp_f32 v53, v53\n\t" \
    "v_exp_f32 v54, v54\n\t" \
    "v_exp_f32 v55, v55\n\t" \
    "v_mfma_f32_32x32x8_bf16 v[32:47], v[80:81], v[100:101], v[32:47]\n\t" \
    "v_cvt_pk_bf16_f32 v64, v48, v49\n\t" \
    "v_cvt_pk_bf16_f32 v65, v50, v51\n\t" \
    "v_cvt_pk_bf16_f32 v66, v52, v53\n\t" \
    "v_cvt_pk_bf16_f32 v67, v54, v55\n\t" \
    "v_exp_f32 v56, v56\n\t" \
    "v_exp_f32 v57, v57\n\t" \
    "v_exp_f32 v58, v58\n\t" \
    "v_exp_f32 v59, v59\n\t" \
    "v_mfma_f32_32x32x16_bf16 %0, v[92:95], v[64:67], %0\n\t" \
    "ds_read_b128 v[92:95], %3 offset:9216\n\t" \
    "v_exp_f32 v60, v60\n\t" \
    "v_exp_f32 v61, v61\n\t" \
    "v_exp_f32 v62, v62\n\t" \
    "v_exp_f32 v63, v63\n\t" \
    "v_cvt_pk_bf16_f32 v68, v56, v57\n\t" \
    "v_cvt_pk_bf16_f32 v69, v58, v59\n\t" \
    "v_cvt_pk_bf16_f32 v70, v60, v61\n\t" \
    "v_cvt_pk_bf16_f32 v71, v62, v63\n\t" \
    "s_waitcnt lgkmcnt(1)\n\t" \
    "v_mfma_f32_32x32x16_bf16 v[48:63], v[76:79], %8, 0\n\t" \
    "v_exp_f32 v32, v32\n\t" \
    "ds_read_b128 v[72:75], %1 offset:12288\n\t" \
    "v_exp_f32 v33, v33\n\t" \
    "ds_read_b64 v[80:81], %2 offset:12288\n\t" \
    "v_exp_f32 v34, v34\n\t" \
    "v_exp_f32 v35, v35\n\t" \
    "v_mfma_f32_32x32x16_bf16 %0, v[96:99], v[68:71], %0\n\t" \
    "ds_read_b128 v[96:99], %3 offset:9984\n\t" \
    "v_exp_f32 v36, v36\n\t" \
    "v_exp_f32 v37, v37\n\t" \
    "v_exp_f32 v38, v38\n\t" \
    "v_exp_f32 v39, v39\n\t" \
    "v_mfma_f32_32x32x8_bf16 v[48:63], v[82:83], v[100:101], v[48:63]\n\t" \
    "v_cvt_pk_bf16_f32 v64, v32, v33\n\t" \
    "v_cvt_pk_bf16_f32 v65, v34, v35\n\t" \
    "v_cvt_pk_bf16_f32 v66, v36, v37\n\t" \
    "v_cvt_pk_bf16_f32 v67, v38, v39\n\t" \
    "v_exp_f32 v40, v40\n\t" \
    "v_exp_f32 v41, v41\n\t" \
    "v_exp_f32 v42, v42\n\t" \
    "v_exp_f32 v43, v43\n\t" \
    "v_mfma_f32_32x32x16_bf16 %0, v[84:87], v[64:67], %0\n\t" \
    "ds_read_b128 v[84:87], %3 offset:12288\n\t" \
    "v_exp_f32 v44, v44\n\t" \
    "v_exp_f32 v45, v45\n\t" \
    "v_exp_f32 v46, v46\n\t" \
    "v_exp_f32 v47, v47\n\t" \
    "v_cvt_pk_bf16_f32 v68, v40, v41\n\t" \
    "v_cvt_pk_bf16_f32 v69, v42, v43\n\t" \
    "v_cvt_pk_bf16_f32 v70, v44, v45\n\t" \
    "v_cvt_pk_bf16_f32 v71, v46, v47\n\t" \
    "s_waitcnt lgkmcnt(1)\n\t" \
    "v_mfma_f32_32x32x16_bf16 v[32:47], v[72:75], %8, 0\n\t" \
    "v_exp_f32 v48, v48\n\t" \
    "ds_read_b128 v[76:79], %1 offset:15360\n\t" \
    "v_exp_f32 v49, v49\n\t" \
    "ds_read_b64 v[82:83], %2 offset:15360\n\t" \
    "v_exp_f32 v50, v50\n\t" \
    "v_exp_f32 v51, v51\n\t" \
    "v_mfma_f32_32x32x16_bf16 %0, v[88:91], v[68:71], %0\n\t" \
    "ds_read_b128 v[88:91], %3 offset:13056\n\t" \
    "v_exp_f32 v52, v52\n\t" \
    "v_exp_f32 v53, v53\n\t" \
    "v_exp_f32 v54, v54\n\t" \
    "v_exp_f32 v55, v55\n\t" \
    "v_mfma_f32_32x32x8_bf16 v[32:47], v[80:81], v[100:101], v[32:47]\n\t" \
    "v_cvt_pk_bf16_f32 v64, v48, v49\n\t" \
    "v_cvt_pk_bf16_f32 v65, v50, v51\n\t" \
    "v_cvt_pk_bf16_f32 v66, v52, v53\n\t" \
    "v_cvt_pk_bf16_f32 v67, v54, v55\n\t" \
    "v_exp_f32 v56, v56\n\t" \
    "v_exp_f32 v57, v57\n\t" \
    "v_exp_f32 v58, v58\n\t" \
    "v_exp_f32 v59, v59\n\t" \
    "v_mfma_f32_32x32x16_bf16 %0, v[92:95], v[64:67], %0\n\t" \
    "ds_read_b128 v[92:95], %3 offset:15360\n\t" \
    "v_exp_f32 v60, v60\n\t" \
    "v_exp_f32 v61, v61\n\t" \
    "v_exp_f32 v62, v62\n\t" \
    "v_exp_f32 v63, v63\n\t" \
    "v_cvt_pk_bf16_f32 v68, v56, v57\n\t" \
    "v_cvt_pk_bf16_f32 v69, v58, v59\n\t" \
    "v_cvt_pk_bf16_f32 v70, v60, v61\n\t" \
    "v_cvt_pk_bf16_f32 v71, v62, v63\n\t" \
    "s_waitcnt lgkmcnt(1)\n\t" \
    "v_mfma_f32_32x32x16_bf16 v[48:63], v[76:79], %8, 0\n\t" \
    "v_exp_f32 v32, v32\n\t" \
    "ds_read_b128 v[72:75], %1 offset:18432\n\t" \
    "v_exp_f32 v33, v33\n\t" \
    "ds_read_b64 v[80:81], %2 offset:18432\n\t" \
    "v_exp_f32 v34, v34\n\t" \
    "v_exp_f32 v35, v35\n\t" \
    "v_mfma_f32_32x32x16_bf16 %0, v[96:99], v[68:71], %0\n\t" \
    "ds_read_b128 v[96:99], %3 offset:16128\n\t" \
    "v_exp_f32 v36, v36\n\t" \
    "v_exp_f32 v37, v37\n\t" \
    "v_exp_f32 v38, v38\n\t" \
    "v_exp_f32 v39, v39\n\t" \
    "v_mfma_f32_32x32x8_bf16 v[48:63], v[82:83], v[100:101], v[48:63]\n\t" \
    "v_cvt_pk_bf16_f32 v64, v32, v33\n\t" \
    "v_cvt_pk_bf16_f32 v65, v34, v35\n\t" \
    "v_cvt_pk_bf16_f32 v66, v36, v37\n\t" \
    "v_cvt_pk_bf16_f32 v67, v38, v39\n\t" \
    "v_exp_f32 v40, v40\n\t" \
    "v_exp_f32 v41, v41\n\t" \
    "v_exp_f32 v42, v42\n\t" \
    "v_exp_f32 v43, v43\n\t" \
    "v_mfma_f32_32x32x16_bf16 %0, v[84:87], v[64:67], %0\n\t" \
    "ds_read_b128 v[84:87], %3 offset:18432\n\t" \
    "v_exp_f32 v44, v44\n\t" \
    "v_exp_f32 v45, v45\n\t" \
    "v_exp_f32 v46, v46\n\t" \
    "v_exp_f32 v47, v47\n\t" \
    "v_cvt_pk_bf16_f32 v68, v40, v41\n\t" \
    "v_cvt_pk_bf16_f32 v69, v42, v43\n\t" \
    "v_cvt_pk_bf16_f32 v70, v44, v45\n\t" \
    "v_cvt_pk_bf16_f32 v71, v46, v47\n\t" \
    "s_waitcnt lgkmcnt(1)\n\t" \
    "v_mfma_f32_32x32x16_bf16 v[32:47], v[72:75], %8, 0\n\t" \
    "v_exp_f32 v48, v48\n\t" \
    "ds_read_b128 v[76:79], %1 offset:21504\n\t" \
    "v_exp_f32 v49, v49\n\t" \
    "ds_read_b64 v[82:83], %2 offset:21504\n\t" \
    "v_exp_f32 v50, v50\n\t" \
    "v_exp_f32 v51, v51\n\t" \
    "v_mfma_f32_32x32x16_bf16 %0, v[88:91], v[68:71], %0\n\t" \
    "ds_read_b128 v[88:91], %3 offset:19200\n\t" \
    "v_exp_f32 v52, v52\n\t" \
    "v_exp_f32 v53, v53\n\t" \
    "v_exp_f32 v54, v54\n\t" \
    "v_exp_f32 v55, v55\n\t" \
    "v_mfma_f32_32x32x8_bf16 v[32:47], v[80:81], v[100:101], v[32:47]\n\t" \
    "v_cvt_pk_bf16_f32 v64, v48, v49\n\t" \
    "v_cvt_pk_bf16_f32 v65, v50, v51\n\t" \
    "v_cvt_pk_bf16_f32 v66, v52, v53\n\t" \
    "v_cvt_pk_bf16_f32 v67, v54, v55\n\t" \
    "v_exp_f32 v56, v56\n\t" \
    "v_exp_f32 v57, v57\n\t" \
    "v_exp_f32 v58, v58\n\t" \
    "v_exp_f32 v59, v59\n\t" \
    "v_mfma_f32_32x32x16_bf16 %0, v[92:95], v[64:67], %0\n\t" \
    "ds_read_b128 v[92:95], %3 offset:21504\n\t" \
    "v_exp_f32 v60, v60\n\t" \
    "v_exp_f32 v61, v61\n\t" \
    "v_exp_f32 v62, v62\n\t" \
    "v_exp_f32 v63, v63\n\t" \
    "v_cvt_pk_bf16_f32 v68, v56, v57\n\t" \
    "v_cvt_pk_bf16_f32 v69, v58, v59\n\t" \
    "v_cvt_pk_bf16_f32 v70, v60, v61\n\t" \
    "v_cvt_pk_bf16_f32 v71, v62, v63\n\t" \
    "s_nop 1\n\t" \
    "v_mfma_f32_32x32x16_bf16 %0, v[96:99], v[68:71], %0\n\t" \
    "ds_read_b128 v[96:99], %3 offset:22272\n\t" \
    "s_add_u32 %4, %4, 0x6000\n\t" \
    "s_add_u32 %6, %6, 0x6000\n\t" \
    "s_cmp_eq_u32 %6, 0x12000\n\t" \
    "s_cselect_b32 %6, 0, %6\n\t" \
    "s_sub_u32 %7, %7, 1\n\t" \
    "s_cmp_lg_u32 %7, 0\n\t" \
    "s_cbranch_scc1 1b\n\t" \
    "s_waitcnt vmcnt(0) lgkmcnt(0)\n\t" \
    "s_nop 15"
#define ATT_CLOBBERS "v32", "v33", "v34", "v35", "v36", "v37", "v38", "v39", "v40", "v41", "v42", "v43", "v44", "v45", "v46", "v47", "v48", "v49", "v50", "v51", "v52", "v53", "v54", "v55", "v56", "v57", "v58", "v59", "v60", "v61", "v62", "v63", "v64", "v65", "v66", "v67", "v68", "v69", "v70", "v71", "v72", "v73", "v74", "v75", "v76", "v77", "v78", "v79", "v80", "v81", "v82", "v83", "v84", "v85", "v86", "v87", "v88", "v89", "v90", "v91", "v92", "v93", "v94", "v95", "v96", "v97", "v98", "v99", "v100", "v101", "vcc"
DEV void attn_role(char* ws, int item, lds_ptr lds) {
    const int tid = threadIdx.x, lane = tid & 63, w = __builtin_amdgcn_readfirstlane(tid >> 6);
    const int r = lane & 31, h = lane >> 5;
    const int head = (item & 7) * 4 + (item >> 6), qb = (item >> 3) & 7;
    const char* kvh = ws + WS_KV + (size_t)head * 64 * 3072;
    i32x4 skv = make_srd(kvh);
    skv[2] = 64 * 3072;
    const unsigned l0 = (unsigned)(uintptr_t)lds;
    const unsigned vo = lane * 16;
    __builtin_amdgcn_s_setprio(3);
    const int tile = qb * 8 + w;
    bf16x8 q1; u32x2 q2w; float gq;
    {
        const char* qp = ws + WS_QF + (size_t)(head * 64 + tile) * 1280;
        asm volatile("global_load_dwordx4 %0, %1, off" : "=v"(q1) : "v"(qp + lane * 16) : "memory");
        asm volatile("global_load_dwordx2 %0, %1, off" : "=v"(q2w) : "v"(qp + 1024 + r * 8) : "memory");
        asm volatile("global_load_dword %0, %1, off" : "=v"(gq) : "v"(ws + WS_G + (tile * 32 + r) * 4) : "memory");
    }
    if (w < 6) dma16(skv, vo, (unsigned)w * 1024u, l0 + 3 * AT_CHUNK + (unsigned)w * 1024u);
#pragma unroll
    for (int c = 0; c < 2; ++c)
#pragma unroll
        for (int j = 0; j < 3; ++j) dma16(skv, vo, (unsigned)(6144 + c * AT_CHUNK + (3 * w + j) * 1024), l0 + (unsigned)(c * AT_CHUNK + (3 * w + j) * 1024));
    const int vrow = r < 21 ? r : 21;
    const unsigned kbase = l0 + lane * 16, k2base = l0 + 1024 + lane * 8, vbase = l0 + 1536 + h * 384 + vrow * 16;
    asm volatile("s_waitcnt vmcnt(6)\n\ts_barrier" : "+v"(q1), "+v"(q2w), "+v"(gq) :: "memory");
    if (h) q2w = (u32x2){0u, 0u};
    const s16x4 q2 = __builtin_bit_cast(s16x4, q2w);
    f32x16 o = f32x16{};
    __builtin_amdgcn_s_setprio(0);
    {
        unsigned ka = kbase + 3 * AT_CHUNK, kb = k2base + 3 * AT_CHUNK, va = vbase + 3 * AT_CHUNK;
        unsigned soff = 6144 + 2 * AT_CHUNK + w * 3072, ldst, cur = 0, cnt = 8;
        const unsigned wbase = l0 + w * 3072;
        asm volatile(ATT_LOOP_ASM
                     : "+v"(o), "+v"(ka), "+v"(kb), "+v"(va), "+s"(soff), "=&s"(ldst), "+s"(cur), "+s"(cnt)
                     : "v"(q1), "v"(q2w[0]), "v"(q2w[1]), "v"(vo), "s"(skv), "v"(kbase), "v"(k2base), "v"(vbase), "s"(wbase)
                     : "memory", "scc", ATT_CLOBBERS);
    }
    auto lrow = [&](const f32x16& v) { auto sw = __builtin_amdgcn_permlane32_swap(__float_as_uint(v[8]), __float_as_uint(v[8]), false, false); return __uint_as_float(sw[1]); };
    float l = lrow(o);
    bool ok = (l > 9.094947e-13f) && (l < 1.2980742e33f);
#pragma unroll
    for (int i = 0; i < 12; ++i) ok = ok && (fabsf(o[i]) < INFINITY || (h && i >= 8));
    if (!__all(ok)) {
        attn_slow(kvh, q1, q2, lane, o);
        l = lrow(o);
    }
    const float sc = gq * __builtin_amdgcn_rcpf(l);
    const __amdgpu_buffer_rsrc_t ro = make_rsrc(ws + WS_OF);
    const unsigned ob = (unsigned)tile * 40u * 1024u;
    u32x4 m = {pk2(o[0] * sc, o[1] * sc), pk2(o[2] * sc, o[3] * sc), pk2(o[4] * sc, o[5] * sc), pk2(o[6] * sc, o[7] * sc)};
    st16_sc1(ro, ob + (unsigned)(head * 1024 + lane * 16), m);
    if (h == 0) {
        u32x2 e = {pk2(o[8] * sc, o[9] * sc), pk2(o[10] * sc, o[11] * sc)};
        st8_sc1(ro, ob + (unsigned)((32 + (head >> 2)) * 1024 + (((head >> 1) & 1) * 32 + r) * 16 + (head & 1) * 8), e);
    }
    publish(ws, qb);
}

DEV void bufld16(bf16x8& dst, const i32x4& srd, unsigned voff, unsigned soff) {
    asm volatile("buffer_load_dwordx4 %0, %1, %2, %3 offen" : "=v"(dst) : "v"(voff), "s"(srd), "s"(soff) : "memory");
}
DEV void bufld16_o(bf16x8& dst, const i32x4& srd, unsigned voff, unsigned soff) {
    asm volatile("buffer_load_dwordx4 %0, %1, %2, %3 offen sc1" : "=v"(dst) : "v"(voff), "s"(srd), "s"(soff) : "memory");
}

template <int N> DEV void pin_vm(bf16x8& a) { asm volatile("s_waitcnt vmcnt(%1)" : "+v"(a) : "n"(N) : "memory"); }
template <int N> DEV void pin_vm(bf16x8& a, bf16x8& b, bf16x8& c) { asm volatile("s_waitcnt vmcnt(%3)" : "+v"(a), "+v"(b), "+v"(c) : "n"(N) : "memory"); }
template <int N> DEV void pin_vm(bf16x8& a, bf16x8& b, bf16x8& c, bf16x8& d) { asm volatile("s_waitcnt vmcnt(%4)" : "+v"(a), "+v"(b), "+v"(c), "+v"(d) : "n"(N) : "memory"); }
template <int N> DEV void pin_vm(bf16x8& a, bf16x8& b, bf16x8& c, bf16x8& d, bf16x8& e, bf16x8& f) { asm volatile("s_waitcnt vmcnt(%6)" : "+v"(a), "+v"(b), "+v"(c), "+v"(d), "+v"(e), "+v"(f) : "n"(N) : "memory"); }
constexpr int P2_NF = 24, P2_PD = 3, P2_AD = 6;
constexpr int p2_allowed(int s, bool up) {
    int idx[256] = {}, n = 0;
    const int nw = up ? 2 : 3;
    auto A = [&](int k) { idx[n] = k; ++n; };
    auto W = [&](int k) { for (int i = 0; i < nw; ++i) { idx[n] = k; ++n; } };
    for (int k = 0; k < P2_PD; ++k) W(k);
    for (int k = 0; k < P2_AD; ++k) A(k);
    for (int u = 0; u < s; ++u) { if (u + P2_PD < P2_NF + 2) W(u + P2_PD); if (u + P2_AD < P2_NF) A(u + P2_AD); }
    int last = -1;
    for (int i = 0; i < n; ++i) if (idx[i] <= s) last = i;
    return n - 1 - last;
}

DEV void g_role(char* ws, char* out, int item, lds_ptr lds) {
    const int tid = threadIdx.x, lane = tid & 63, w = __builtin_amdgcn_readfirstlane(tid >> 6);
    const int r = lane & 31, h = lane >> 5;
    const int xcd = item & 7, j = item >> 3;
    const unsigned l0 = (unsigned)(uintptr_t)lds;
    const int reg = j >> 2, sub = j & 3, kg = w & 3;
    const bool up = w >= 4;
    const int rgp = (xcd & 3) * 4 + (reg & 3), cgp = (xcd >> 2) * 2 + (reg >> 2);
    const int rp = sub >> 1, cp = sub & 1;
    const int t0 = rgp * 4 + 2 * rp + cp, t1 = rgp * 4 + 2 * rp + (cp ^ 1);
    const int c0 = cgp * 128 + cp * 64, c4 = 512 + cgp * 32;
    const int ct0 = cgp * 4 + cp * 2, ct4 = 16 + cgp;
    const int tme = up ? t1 : t0;
    const i32x4 swf = make_srd(ws + WS_WF), sh = make_srd(ws + WS_HF), so_ = make_srd(ws + WS_OF);
    const unsigned va = lane * 16;
    f32x16 acc[3];
#pragma unroll
    for (int i = 0; i < 3; ++i) acc[i] = f32x16{};
    const float gm = 1.f - ((const float*)(ws + WS_G))[tme * 32 + r];
    asm volatile("" :: "v"(gm));
    auto run = [&](auto upc) {
        constexpr bool UP = decltype(upc)::value;
        constexpr int NW = UP ? 2 : 3;
        bf16x8 wf[P2_PD][3];
        auto wbase = [&](int s) -> unsigned { return s < P2_NF ? (unsigned)(WF_WD + (kg * 24 + s) * 20) * 1024u : (unsigned)(WF_WO + (kg * 10 + s - P2_NF) * 20) * 1024u; };
        auto issue_w = [&](int s, int slot) {
            const unsigned fb = wbase(s);
            bufld16(wf[slot][0], swf, va, fb + (unsigned)ct0 * 1024u); bufld16(wf[slot][1], swf, va, fb + (unsigned)(ct0 + 1) * 1024u);
            if constexpr (!UP) bufld16(wf[slot][2], swf, va, fb + (unsigned)ct4 * 1024u);
        };
        auto mma = [&](const bf16x8& w0, const bf16x8& w1, const bf16x8& w2, const bf16x8& a) {
            acc[0] = mfma16(w0, a, acc[0]); acc[1] = mfma16(w1, a, acc[1]);
            if constexpr (!UP) acc[2] = mfma16(w2, a, acc[2]);
        };
        auto pin = [&](auto nc, bf16x8& a, int slot) {
            constexpr int N = decltype(nc)::value;
            if constexpr (UP) pin_vm<N>(a, wf[slot][0], wf[slot][1]); else pin_vm<N>(a, wf[slot][0], wf[slot][1], wf[slot][2]);
        };
        {
            bf16x8 af[P2_AD];
            auto issue_a = [&](int s) { bufld16(af[s % P2_AD], sh, va, (unsigned)(tme * 96 + kg * 24 + s) * 1024u); };
#pragma unroll
            for (int s = 0; s < P2_PD; ++s) issue_w(s, s);
#pragma unroll
            for (int s = 0; s < P2_AD; ++s) issue_a(s);
            auto step = [&](auto sc) {
                constexpr int s = decltype(sc)::value;
                pin(std::integral_constant<int, p2_allowed(s, UP)>{}, af[s % P2_AD], s % P2_PD);
                mma(wf[s % P2_PD][0], wf[s % P2_PD][1], wf[s % P2_PD][2], af[s % P2_AD]);
                SBAR();
                if constexpr (s + P2_PD < P2_NF + 2) issue_w(s + P2_PD, (s + P2_PD) % P2_PD);
                if constexpr (s + P2_AD < P2_NF) issue_a(s + P2_AD);
                SBAR();
            };
            static_for<0, P2_NF>(step);
        }
#pragma unroll
        for (int i = 0; i < 16; ++i) { acc[0][i] *= gm; acc[1][i] *= gm; acc[2][i] *= gm; }
#pragma unroll
        for (int q = 0; q < 3; ++q) {
            const int s = 26 + 2 * q + (UP ? 1 : 0);
            const unsigned fb = wbase(s), lb = l0 + (unsigned)kg * 18432u + (unsigned)(s - 26) * 3072u;
            dma16(swf, va, fb + (unsigned)ct0 * 1024u, lb); dma16(swf, va, fb + (unsigned)(ct0 + 1) * 1024u, lb + 1024u); dma16(swf, va, fb + (unsigned)ct4 * 1024u, lb + 2048u);
        }
        if constexpr (UP) pin_vm<0>(wf[0][0], wf[0][1], wf[1][0], wf[1][1]); else pin_vm<0>(wf[0][0], wf[0][1], wf[0][2], wf[1][0], wf[1][1], wf[1][2]);
        consume(ws, rgp >> 1, 32);
        bf16x8 ao[10];
#pragma unroll
        for (int i = 0; i < 10; ++i) bufld16_o(ao[i], so_, va, (unsigned)(tme * 40 + kg * 10 + i) * 1024u);
        const lds_cptr wl = (lds_cptr)lds + kg * 18432 + lane * 16;
        auto ldw = [&](int s) { wf[2][0] = lds_ld128(wl + (s - 26) * 3072); wf[2][1] = lds_ld128(wl + (s - 26) * 3072 + 1024); if constexpr (!UP) wf[2][2] = lds_ld128(wl + (s - 26) * 3072 + 2048); };
        pin(std::integral_constant<int, 9>{}, ao[0], 0);
        mma(wf[0][0], wf[0][1], wf[0][2], ao[0]); SBAR();
        issue_w(32, 0); SBAR();
        pin(std::integral_constant<int, 8 + NW>{}, ao[1], 1);
        mma(wf[1][0], wf[1][1], wf[1][2], ao[1]); SBAR();
        issue_w(33, 1); ldw(26); SBAR();
        auto stepo = [&](auto sc) {
            constexpr int s = decltype(sc)::value;
            pin_vm<33 - s + 2 * NW>(ao[s - 24]);
            mma(wf[2][0], wf[2][1], wf[2][2], ao[s - 24]);
            SBAR();
            if constexpr (s < 31) ldw(s + 1);
            SBAR();
        };
        static_for<26, 32>(stepo);
        pin(std::integral_constant<int, NW>{}, ao[8], 0);
        mma(wf[0][0], wf[0][1], wf[0][2], ao[8]); SBAR();
        pin(std::integral_constant<int, 0>{}, ao[9], 1);
        mma(wf[1][0], wf[1][1], wf[1][2], ao[9]);
    };
    if (up) run(std::true_type{}); else run(std::false_type{});
    typedef __attribute__((ext_vector_type(4))) float f32x4;
    typedef __attribute__((address_space(3))) f32x4* lds_f4;
    __syncthreads();
    {
        lds_ptr mine = lds + kg * 20480 + r * 128;
#pragma unroll
        for (int i = 0; i < 3; ++i) {
            if (i == 2 && up) break;
            const int tl = up ? 2 + i : (i == 2 ? 4 : i);
#pragma unroll
            for (int g = 0; g < 4; ++g) {
                f32x4 v = {acc[i][4 * g], acc[i][4 * g + 1], acc[i][4 * g + 2], acc[i][4 * g + 3]};
                *(lds_f4)(mine + tl * 4096 + (((2 * g + h) ^ (r & 7)) * 16)) = v;
            }
        }
    }
    __syncthreads();
#pragma unroll
    for (int it = 0; it < 2; ++it) {
        const int u = tid + 512 * it;
        if (u < 640) {
            const int tl = u >> 7, tok = (u & 127) >> 2, ch = u & 3;
            f32x4 lo = {0, 0, 0, 0}, hi = {0, 0, 0, 0};
#pragma unroll
            for (int ww = 0; ww < 4; ++ww) {
                lds_ptr p = lds + ww * 20480 + tl * 4096 + tok * 128;
                lo += *(lds_f4)(p + (((2 * ch) ^ (tok & 7)) * 16));
                hi += *(lds_f4)(p + (((2 * ch + 1) ^ (tok & 7)) * 16));
            }
            const int tt = (tl == 2 || tl == 3) ? t1 : t0, cc = tl == 4 ? c4 : c0 + (tl & 1) * 32;
            u32x4 o = {pk2(lo[0], lo[1]), pk2(lo[2], lo[3]), pk2(hi[0], hi[1]), pk2(hi[2], hi[3])};
            __builtin_amdgcn_raw_buffer_store_b128(o, make_rsrc(out), (unsigned)((tt * 32 + tok) * 1280 + (cc + ch * 8) * 2), 0, 16);
        }
    }
}

extern "C" __global__ __launch_bounds__(512, 4) void gm_k2(char* __restrict__ ws, char* __restrict__ out) {
    extern __shared__ __attribute__((aligned(16))) char lds_raw[];
    lds_ptr lds = (lds_ptr)lds_raw;
    if (blockIdx.x < 256) attn_role(ws, blockIdx.x, lds);
    else g_role(ws, out, blockIdx.x - 256, lds);
}

extern "C" void kernel_launch(void* const* d_in, const int* in_sizes, int n_in, void* d_out, int out_size, void* d_ws, size_t ws_size, hipStream_t stream) {
    const char* ax = (const char*)d_in[0];
    const char* wqkv = (const char*)d_in[1];
    const char* wo = (const char*)d_in[2];
    const char* wg = (const char*)d_in[4];
    const char* wu = (const char*)d_in[5];
    const char* wd = (const char*)d_in[6];
    const char* gw = (const char*)d_in[7];
    char* ws = (char*)d_ws;
    (void)hipFuncSetAttribute((const void*)gm_k1, hipFuncAttributeMaxDynamicSharedMemorySize, K1_LDS);
    (void)hipFuncSetAttribute((const void*)gm_k2, hipFuncAttributeMaxDynamicSharedMemorySize, 81920);
    gm_k1<<<256, 512, K1_LDS, stream>>>(ax, wqkv, gw, wg, wu, wd, wo, ws);
    gm_k2<<<512, 512, 81920, stream>>>(ws, (char*)d_out);
}
```
